```python
import torch

def _softmax(x, axis=-1):
    return torch.softmax(x, dim=axis)
def _gelu(x):
    return torch.nn.functional.gelu(x, approximate="tanh")
def _silu(x):
    return torch.nn.functional.silu(x)
def _sigmoid(x):
    return torch.sigmoid(x)
def _layernorm(x, axis=-1, eps=1e-5):
    m = x.mean(dim=axis, keepdim=True)
    v = x.var(dim=axis, keepdim=True, unbiased=False)
    return (x - m) / torch.sqrt(v + eps)
def _scatter_add(tgt, idx, src, axis=0):
    return tgt.clone().index_add_(axis, idx, src)
def _segment_sum(src, seg, n):
    out = torch.zeros((n,) + src.shape[1:], dtype=src.dtype)
    return out.index_add_(0, seg, src)
def _pad(x, pw):
    flat = tuple(v for pair in reversed(pw) for v in pair)
    return torch.nn.functional.pad(x, flat)

kernel_name = 'gated_mix__sliding_local_dilated__swiglu_ffn__prompt'

def setup_inputs():

    _w = (lambda a: (lambda t: t.to(torch.bfloat16) if t.is_floating_point() else t)(torch.as_tensor(a)))
    def _A_setup():
        B, S, D = 1, 2048, 640
        return {
            "x": _w((lambda *s: torch.randn(*s, dtype=torch.bfloat16) * 0.05)(B, S, D)),
            "wqkv": _w((lambda *s: torch.randn(*s, dtype=torch.bfloat16) * 0.8)(D, 3 * D)),
            "wo": _w((lambda *s: torch.randn(*s, dtype=torch.bfloat16) * 0.05)(D, D)),
        }
    _A_in = _A_setup()

    def _B_setup():
        B, S, D, F = 1, 2048, 640, 1536
        return {
            "x": _w((lambda *s: torch.randn(*s, dtype=torch.bfloat16) * 0.05)(B, S, D)),
            "w_gate": _w((lambda *s: torch.randn(*s, dtype=torch.bfloat16) * 0.2)(D, F)),
            "w_up": _w((lambda *s: torch.randn(*s, dtype=torch.bfloat16) * 0.2)(D, F)),
            "w_down": _w((lambda *s: torch.randn(*s, dtype=torch.bfloat16) * 0.2)(F, D)),
        }
    _B_in = _B_setup()

    return {
        "A_x": _A_in["x"],
        "A_wqkv": _A_in["wqkv"],
        "A_wo": _A_in["wo"],
        "B_x": _B_in["x"],
        "B_w_gate": _B_in["w_gate"],
        "B_w_up": _B_in["w_up"],
        "B_w_down": _B_in["w_down"],
        "gate_w": _w((lambda *s: torch.randn(*s, dtype=torch.bfloat16) * 0.05)(640) * 16.0),
    }

def reference(A_x, A_wqkv, A_wo, B_x, B_w_gate, B_w_up, B_w_down, gate_w):
    def _A_ref(x, wqkv, wo):
        B, S, D = x.shape
        H = 32; Dh = D // H
        W, dil = 8, 2
        qkv = (lambda x, *d: x.permute(*d))((x @ wqkv).reshape(B, S, 3, H, Dh), 2, 0, 3, 1, 4)
        q, k, v = qkv[0], qkv[1], qkv[2]
        scale = Dh ** -0.5
        pos = torch.arange(S)
        off = torch.arange(W) * dil
        idx = pos[:, None] - off[None, :]
        mask = idx >= 0
        idx_c = torch.where(mask, idx, 0).reshape(-1)
        kg = k[:, :, idx_c, :].reshape(B, H, S, W, Dh)
        vg = v[:, :, idx_c, :].reshape(B, H, S, W, Dh)
        logits = torch.einsum("bhsd,bhswd->bhsw", q, kg) * scale
        logits = torch.where(mask[None, None, :, :], logits, -1e9)
        attn = _softmax(logits, axis=-1)
        o = torch.einsum("bhsw,bhswd->bhsd", attn, vg)
        return (lambda x, *d: x.permute(*d))(o, 0, 2, 1, 3).reshape(B, S, D) @ wo

    def _B_ref(x, w_gate, w_up, w_down):
        g = _silu(x @ w_gate)
        u = x @ w_up
        return (g * u) @ w_down

    x = A_x
    g = _sigmoid((x * gate_w).sum(-1, keepdims=True))
    a = _A_ref(x, A_wqkv, A_wo)
    b = _B_ref(x, B_w_gate, B_w_up, B_w_down)
    return g * a + (1 - g) * b
```

```cpp
#include <hip/hip_runtime.h>
#include <hip/hip_bf16.h>
#include <stdint.h>

typedef unsigned short u16;
__device__ __forceinline__ float bf2f(u16 v) { return __uint_as_float(((unsigned)v) << 16); }
__device__ __forceinline__ u16 f2bf(float f) { __hip_bfloat16 b = __float2bfloat16(f); return *(u16*)&b; }
__device__ __forceinline__ float rbf(float f) { return bf2f(f2bf(f)); }

constexpr int S = 2048, D = 640, F = 1536, H = 32, DH = 20;

__global__ void k_gemm(const u16* A, const u16* B, u16* C, int M, int N, int K) {
  __shared__ float sa[16][17], sb[16][17];
  int tx = threadIdx.x & 15, ty = threadIdx.x >> 4;
  int row = blockIdx.y * 16 + ty, col = blockIdx.x * 16 + tx;
  float acc = 0.f;
  for (int k0 = 0; k0 < K; k0 += 16) {
    sa[ty][tx] = bf2f(A[(size_t)row * K + k0 + tx]);
    sb[ty][tx] = bf2f(B[(size_t)(k0 + ty) * N + col]);
    __syncthreads();
    for (int k = 0; k < 16; ++k) acc += sa[ty][k] * sb[k][tx];
    __syncthreads();
  }
  C[(size_t)row * N + col] = f2bf(acc);
}
__global__ void k_attn(const u16* qkv, u16* o) {
  int t = blockIdx.x * blockDim.x + threadIdx.x;
  int s = t / H, h = t % H;
  float lg[8];
  float m = -INFINITY;
  const float scale = 0.22360679774997896f;
  for (int w = 0; w < 8; ++w) {
    int idx = s - 2 * w; bool ok = idx >= 0; int ic = ok ? idx : 0;
    float acc = 0.f;
    for (int d = 0; d < DH; ++d) acc += bf2f(qkv[(size_t)s * 1920 + h * DH + d]) * bf2f(qkv[(size_t)ic * 1920 + 640 + h * DH + d]);
    float l = rbf(rbf(acc) * scale);
    lg[w] = ok ? l : rbf(-1e9f);
    m = fmaxf(m, lg[w]);
  }
  float sum = 0.f;
  for (int w = 0; w < 8; ++w) { lg[w] = __expf(lg[w] - m); sum += lg[w]; }
  for (int w = 0; w < 8; ++w) lg[w] = rbf(lg[w] / sum);
  for (int d = 0; d < DH; ++d) {
    float acc = 0.f;
    for (int w = 0; w < 8; ++w) { int idx = s - 2 * w; int ic = idx >= 0 ? idx : 0; acc += lg[w] * bf2f(qkv[(size_t)ic * 1920 + 1280 + h * DH + d]); }
    o[(size_t)s * D + h * DH + d] = f2bf(acc);
  }
}
__global__ void k_glu(const u16* g, const u16* u, u16* h, int n) {
  int i = blockIdx.x * blockDim.x + threadIdx.x;
  if (i < n) { float gv = bf2f(g[i]); float s = rbf(gv / (1.f + __expf(-gv))); h[i] = f2bf(s * bf2f(u[i])); }
}
__global__ void k_final(const u16* x, const u16* gw, const u16* a, const u16* b, u16* out) {
  int s = blockIdx.x;
  __shared__ float red[64];
  float acc = 0.f;
  for (int d = threadIdx.x; d < D; d += 64) acc += rbf(bf2f(x[(size_t)s * D + d]) * bf2f(gw[d]));
  red[threadIdx.x] = acc; __syncthreads();
  float tot = 0.f; for (int i = 0; i < 64; ++i) tot += red[i];
  float z = rbf(tot);
  float g = rbf(1.f / (1.f + __expf(-z)));
  float og = rbf(1.f - g);
  for (int d = threadIdx.x; d < D; d += 64) {
    float ta = rbf(g * bf2f(a[(size_t)s * D + d]));
    float tb = rbf(og * bf2f(b[(size_t)s * D + d]));
    out[(size_t)s * D + d] = f2bf(ta + tb);
  }
}

extern "C" void kernel_launch(void* const* d_in, const int* in_sizes, int n_in,
                              void* d_out, int out_size, void* d_ws, size_t ws_size,
                              hipStream_t stream) {
  const u16* x = (const u16*)d_in[0];
  const u16* wqkv = (const u16*)d_in[1];
  const u16* wo = (const u16*)d_in[2];
  const u16* wg = (const u16*)d_in[4];
  const u16* wu = (const u16*)d_in[5];
  const u16* wd = (const u16*)d_in[6];
  const u16* gw = (const u16*)d_in[7];
  u16* ws = (u16*)d_ws;
  u16* qkv = ws; u16* o = qkv + S * 1920; u16* g = o + S * D; u16* u = g + S * F; u16* h = u + S * F;
  u16* a = h + S * F; u16* b = a + S * D;
  k_gemm<<<dim3(1920 / 16, S / 16), 256, 0, stream>>>(x, wqkv, qkv, S, 1920, D);
  k_attn<<<S * H / 256, 256, 0, stream>>>(qkv, o);
  k_gemm<<<dim3(D / 16, S / 16), 256, 0, stream>>>(o, wo, a, S, D, D);
  k_gemm<<<dim3(F / 16, S / 16), 256, 0, stream>>>(x, wg, g, S, F, D);
  k_gemm<<<dim3(F / 16, S / 16), 256, 0, stream>>>(x, wu, u, S, F, D);
  k_glu<<<S * F / 256, 256, 0, stream>>>(g, u, h, S * F);
  k_gemm<<<dim3(D / 16, S / 16), 256, 0, stream>>>(h, wd, b, S, D, F);
  k_final<<<S, 64, 0, stream>>>(x, gw, a, b, (u16*)d_out);
}
```

```cpp
#include <hip/hip_runtime.h>
#include <stdint.h>
struct KArgs { const void* x; const void* wqkv; const void* wo; const void* wg; const void* wu; const void* wd; const void* gw; void* out; void* ws; };
__global__ __launch_bounds__(512, 2) void fused_kernel(KArgs a) {
  __shared__ __attribute__((aligned(16))) char smem[163840];
  unsigned tid = threadIdx.x;
  asm volatile(
"s_load_dwordx16 s[64:79], s[0:1], 0\n"
"s_load_dwordx2 s[80:81], s[0:1], 64\n"
"v_lshrrev_b32 v1, 6, v0\n"
"v_and_b32 v0, 63, v0\n"
"v_readfirstlane_b32 s32, v1\n"
"s_and_b32 s33, s2, 7\n"
"s_lshr_b32 s34, s2, 3\n"
"s_getpc_b64 s[96:97]\n"
".Lpc0:\n"
"s_cmp_lg_u32 s32, 7\n"
"s_cbranch_scc1 .Lnoct\n"
"s_mul_i32 s94, s34, 16\n"
"v_add_u32 v2, s94, v0\n"
"v_lshlrev_b32 v2, 7, v2\n"
"v_min_u32 v2, .Lkend-.Lpc0-4, v2\n"
"s_mov_b32 exec_lo, 0xffff\n"
"s_mov_b32 exec_hi, 0\n"
"global_load_dword v223, v2, s[96:97]\n"
"s_mov_b64 exec, -1\n"
".Lnoct:\n"
"s_cmp_ge_u32 s34, 16\n"
"s_cbranch_scc1 .Lqkv\n"
"v_lshrrev_b32 v1, 3, v0\n"
"v_mul_u32_u24 v2, 2560, v1\n"
"v_lshrrev_b32 v4, 1, v1\n"
"v_and_b32 v3, 7, v0\n"
"v_xor_b32 v4, v4, v3\n"
"v_lshl_add_u32 v244, v4, 4, v2\n"
"v_xor_b32 v2, 64, v244\n"
"v_add_u32 v245, 19456, v2\n"
"v_lshrrev_b32 v1, 5, v0\n"
"v_mul_u32_u24 v1, 8, v1\n"
"v_bfe_u32 v2, v0, 3, 2\n"
"v_add_u32 v1, v1, v2\n"
"v_mul_u32_u24 v1, 3072, v1\n"
"v_bfe_u32 v2, v0, 1, 2\n"
"v_lshrrev_b32 v3, 4, v0\n"
"v_xor_b32 v2, v2, v3\n"
"v_lshl_add_u32 v1, v2, 5, v1\n"
"v_and_b32 v2, 1, v0\n"
"v_lshl_add_u32 v248, v2, 4, v1\n"
"s_lshr_b32 s35, s33, 2\n"
"s_lshl_b32 s35, s35, 2\n"
"s_and_b32 s36, s34, 3\n"
"s_add_u32 s39, s35, s36\n"
"s_and_b32 s35, s33, 3\n"
"s_lshl_b32 s35, s35, 2\n"
"s_lshr_b32 s37, s34, 2\n"
"s_add_u32 s40, s35, s37\n"
"s_lshr_b32 s35, s32, 1\n"
"s_lshl_b32 s35, s35, 6\n"
"s_and_b32 s36, s32, 1\n"
"s_add_u32 s35, s35, s36\n"
"s_lshl_b32 s37, s39, 8\n"
"s_add_u32 s35, s35, s37\n"
"s_mul_i32 s45, s35, 1280\n"
"s_and_b32 s37, s40, 1\n"
"s_mul_i32 s35, s40, 192\n"
"s_lshl_b32 s38, s37, 6\n"
"s_add_u32 s46, s35, s38\n"
"s_xor_b32 s37, s37, 1\n"
"s_lshl_b32 s38, s37, 7\n"
"s_add_u32 s47, s35, s38\n"
"s_bfe_u32 s35, s32, 0x10001\n"
"s_lshl_b32 s35, s35, 2\n"
"s_add_u32 s35, s35, s36\n"
"s_mul_i32 s35, s35, 12288\n"
"s_add_u32 s46, s46, s35\n"
"s_lshl_b32 s48, s32, 11\n"
"s_mul_i32 s50, s32, 12288\n"
"v_mov_b32 v254, v0\n"
"s_waitcnt lgkmcnt(0)\n"
"s_mov_b32 s4, s64\n"
"s_and_b32 s5, s65, 0xffff\n"
"s_mov_b32 s6, 0x280000\n"
"s_mov_b32 s7, 0x20000\n"
"s_cmp_lt_u32 s32, 4\n"
"s_cselect_b32 s8, s70, s72\n"
"s_cselect_b32 s9, s71, s73\n"
"s_and_b32 s9, s9, 0xffff\n"
"s_mov_b32 s10, 0x1e0000\n"
"s_mov_b32 s11, 0x20000\n"
"s_add_u32 m0, s48, 0x18000\n"
"s_add_u32 s43, s46, 0x0\n"
"buffer_load_dwordx4 v248, s[8:11], s43 offen lds\n"
"s_add_u32 s44, s46, 0x17c00\n"
"buffer_load_dwordx4 v248, s[8:11], s44 offen offset:1024 lds\n"
"s_add_u32 m0, s50, 0x0\n"
"s_add_u32 s41, s45, 0x0\n"
"buffer_load_dwordx4 v244, s[4:7], s41 offen lds\n"
"buffer_load_dwordx4 v245, s[4:7], s41 offen offset:1024 lds\n"
"s_add_u32 s42, s45, 0x9800\n"
"buffer_load_dwordx4 v244, s[4:7], s42 offen offset:2048 lds\n"
"buffer_load_dwordx4 v245, s[4:7], s42 offen offset:3072 lds\n"
"s_barrier\n"
"s_add_u32 m0, s48, 0x1c000\n"
"s_add_u32 s43, s46, 0x30000\n"
"buffer_load_dwordx4 v248, s[8:11], s43 offen lds\n"
"s_add_u32 s44, s46, 0x47c00\n"
"buffer_load_dwordx4 v248, s[8:11], s44 offen offset:1024 lds\n"
"s_add_u32 m0, s50, 0x1000\n"
"s_add_u32 s41, s45, 0x80\n"
"buffer_load_dwordx4 v244, s[4:7], s41 offen lds\n"
"buffer_load_dwordx4 v245, s[4:7], s41 offen offset:1024 lds\n"
"s_add_u32 s42, s45, 0x9880\n"
"buffer_load_dwordx4 v244, s[4:7], s42 offen offset:2048 lds\n"
"buffer_load_dwordx4 v245, s[4:7], s42 offen offset:3072 lds\n"
"s_mov_b32 s60, s64\n"
"s_mov_b32 s61, s65\n"
"s_mov_b32 s62, 0x280000\n"
"s_mov_b32 s82, 40\n"
"s_cmp_ge_u32 s32, 2\n"
"s_cselect_b32 s60, s66, s60\n"
"s_cselect_b32 s61, s67, s61\n"
"s_cselect_b32 s62, 0x258000, s62\n"
"s_cselect_b32 s82, 38, s82\n"
"s_cmp_ge_u32 s32, 4\n"
"s_cselect_b32 s60, s70, s60\n"
"s_cselect_b32 s61, s71, s61\n"
"s_cselect_b32 s62, 0x1e0000, s62\n"
"s_cselect_b32 s82, 60, s82\n"
"s_cmp_ge_u32 s32, 5\n"
"s_cselect_b32 s60, s72, s60\n"
"s_cselect_b32 s61, s73, s61\n"
"s_cselect_b32 s62, 0x1e0000, s62\n"
"s_cselect_b32 s82, 60, s82\n"
"s_cmp_ge_u32 s32, 6\n"
"s_cselect_b32 s60, s74, s60\n"
"s_cselect_b32 s61, s75, s61\n"
"s_cselect_b32 s62, 0x1e0000, s62\n"
"s_cselect_b32 s82, 60, s82\n"
"s_cmp_ge_u32 s32, 7\n"
"s_cselect_b32 s60, s68, s60\n"
"s_cselect_b32 s61, s69, s61\n"
"s_cselect_b32 s62, 0xc8000, s62\n"
"s_cselect_b32 s82, 25, s82\n"
"s_and_b32 s61, s61, 0xffff\n"
"s_mov_b32 s63, 0x20000\n"
"s_lshl_b32 s83, s2, 1\n"
"s_and_b32 s98, s32, 1\n"
"s_add_u32 s83, s83, s98\n"
"s_cmp_lt_u32 s32, 4\n"
"s_cselect_b32 s83, s83, s2\n"
"s_mul_i32 s83, s83, s82\n"
"s_lshl_b32 s83, s83, 7\n"
"v_lshlrev_b32 v156, 7, v254\n"
"v_mov_b32 v157, 0x40000000\n"
"v_cmp_gt_u32 vcc, s82, v254\n"
"s_nop 1\n"
"v_cndmask_b32 v156, v157, v156, vcc\n"
"buffer_load_dword v157, v156, s[60:63], s83 offen\n"
"v_and_b32 v160, 15, v254\n"
"v_lshrrev_b32 v161, 1, v160\n"
"v_lshrrev_b32 v162, 4, v254\n"
"v_xor_b32 v161, v161, v162\n"
"v_lshlrev_b32 v160, 7, v160\n"
"v_lshl_add_u32 v160, v161, 4, v160\n"
"v_add_u32 v246, s50, v160\n"
"v_xor_b32 v247, 64, v246\n"
"v_bfe_u32 v160, v254, 2, 3\n"
"v_lshrrev_b32 v161, 1, v160\n"
"v_lshlrev_b32 v160, 7, v160\n"
"v_lshl_add_u32 v160, v161, 5, v160\n"
"v_and_b32 v161, 3, v254\n"
"v_lshl_add_u32 v160, v161, 3, v160\n"
"v_lshrrev_b32 v161, 5, v254\n"
"v_mul_u32_u24 v161, 4096, v161\n"
"v_add_u32 v160, v160, v161\n"
"v_add_u32 v240, 0x18000, v160\n"
"v_xor_b32 v241, 32, v240\n"
"v_xor_b32 v242, 64, v240\n"
"v_xor_b32 v243, 96, v240\n"
"s_waitcnt vmcnt(7)\n"
"s_barrier\n"
"ds_read_b128 v[0:3], v246 offset:0\n"
"ds_read_b128 v[4:7], v246 offset:2048\n"
"ds_read_b128 v[8:11], v247 offset:0\n"
"ds_read_b128 v[12:15], v247 offset:2048\n"
"ds_read_b64_tr_b16 v[224:225], v240 offset:0\n"
"ds_read_b64_tr_b16 v[226:227], v240 offset:2048\n"
"ds_read_b64_tr_b16 v[228:229], v241 offset:0\n"
"ds_read_b64_tr_b16 v[230:231], v241 offset:2048\n"
"ds_read_b64_tr_b16 v[232:233], v242 offset:0\n"
"ds_read_b64_tr_b16 v[234:235], v242 offset:2048\n"
"s_waitcnt lgkmcnt(4)\n"
"v_mfma_f32_16x16x32_bf16 v[160:163], v[224:227], v[0:3], 0\n"
"ds_read_b64_tr_b16 v[236:237], v243 offset:0\n"
"v_mfma_f32_16x16x32_bf16 v[164:167], v[224:227], v[4:7], 0\n"
"ds_read_b64_tr_b16 v[238:239], v243 offset:2048\n"
"s_add_u32 m0, s48, 0x20000\n"
"s_add_u32 s43, s46, 0x60000\n"
"buffer_load_dwordx4 v248, s[8:11], s43 offen lds\n"
"s_waitcnt lgkmcnt(4)\n"
"v_mfma_f32_16x16x32_bf16 v[168:171], v[228:231], v[0:3], 0\n"
"ds_read_b64_tr_b16 v[224:225], v240 offset:8192\n"
"v_mfma_f32_16x16x32_bf16 v[172:175], v[228:231], v[4:7], 0\n"
"ds_read_b64_tr_b16 v[226:227], v240 offset:10240\n"
"s_waitcnt lgkmcnt(4)\n"
"v_mfma_f32_16x16x32_bf16 v[176:179], v[232:235], v[0:3], 0\n"
"ds_read_b64_tr_b16 v[228:229], v241 offset:8192\n"
"v_mfma_f32_16x16x32_bf16 v[180:183], v[232:235], v[4:7], 0\n"
"ds_read_b64_tr_b16 v[230:231], v241 offset:10240\n"
"s_add_u32 s44, s46, 0x77c00\n"
"buffer_load_dwordx4 v248, s[8:11], s44 offen offset:1024 lds\n"
"s_waitcnt lgkmcnt(4)\n"
"v_mfma_f32_16x16x32_bf16 v[184:187], v[236:239], v[0:3], 0\n"
"ds_read_b64_tr_b16 v[232:233], v242 offset:8192\n"
"v_mfma_f32_16x16x32_bf16 v[188:191], v[236:239], v[4:7], 0\n"
"ds_read_b64_tr_b16 v[234:235], v242 offset:10240\n"
"s_waitcnt lgkmcnt(4)\n"
"v_mfma_f32_16x16x32_bf16 v[192:195], v[224:227], v[0:3], 0\n"
"ds_read_b64_tr_b16 v[236:237], v243 offset:8192\n"
"v_mfma_f32_16x16x32_bf16 v[196:199], v[224:227], v[4:7], 0\n"
"ds_read_b64_tr_b16 v[238:239], v243 offset:10240\n"
"s_add_u32 m0, s50, 0x2000\n"
"s_add_u32 s41, s45, 0x100\n"
"buffer_load_dwordx4 v244, s[4:7], s41 offen lds\n"
"s_waitcnt lgkmcnt(4)\n"
"v_mfma_f32_16x16x32_bf16 v[200:203], v[228:231], v[0:3], 0\n"
"ds_read_b64_tr_b16 v[224:225], v240 offset:1024\n"
"v_mfma_f32_16x16x32_bf16 v[204:207], v[228:231], v[4:7], 0\n"
"ds_read_b64_tr_b16 v[226:227], v240 offset:3072\n"
"s_waitcnt lgkmcnt(4)\n"
"v_mfma_f32_16x16x32_bf16 v[208:211], v[232:235], v[0:3], 0\n"
"ds_read_b64_tr_b16 v[228:229], v241 offset:1024\n"
"v_mfma_f32_16x16x32_bf16 v[212:215], v[232:235], v[4:7], 0\n"
"ds_read_b64_tr_b16 v[230:231], v241 offset:3072\n"
"buffer_load_dwordx4 v245, s[4:7], s41 offen offset:1024 lds\n"
"s_waitcnt lgkmcnt(4)\n"
"v_mfma_f32_16x16x32_bf16 v[216:219], v[236:239], v[0:3], 0\n"
"ds_read_b64_tr_b16 v[232:233], v242 offset:1024\n"
"v_mfma_f32_16x16x32_bf16 v[220:223], v[236:239], v[4:7], 0\n"
"ds_read_b64_tr_b16 v[234:235], v242 offset:3072\n"
"s_waitcnt vmcnt(5)\n"
"s_barrier\n"
"s_waitcnt lgkmcnt(4)\n"
"v_mfma_f32_16x16x32_bf16 v[160:163], v[224:227], v[8:11], v[160:163]\n"
"ds_read_b64_tr_b16 v[236:237], v243 offset:1024\n"
"ds_read_b128 v[16:19], v246 offset:4096\n"
"v_mfma_f32_16x16x32_bf16 v[164:167], v[224:227], v[12:15], v[164:167]\n"
"ds_read_b64_tr_b16 v[238:239], v243 offset:3072\n"
"s_add_u32 s42, s45, 0x9900\n"
"buffer_load_dwordx4 v244, s[4:7], s42 offen offset:2048 lds\n"
"ds_read_b128 v[20:23], v246 offset:6144\n"
"ds_read_b128 v[24:27], v247 offset:4096\n"
"s_waitcnt lgkmcnt(7)\n"
"v_mfma_f32_16x16x32_bf16 v[168:171], v[228:231], v[8:11], v[168:171]\n"
"ds_read_b64_tr_b16 v[224:225], v240 offset:9216\n"
"ds_read_b128 v[28:31], v247 offset:6144\n"
"v_lshlrev_b32 v253, 4, v254\n"
"v_mfma_f32_16x16x32_bf16 v[172:175], v[228:231], v[12:15], v[172:175]\n"
"ds_read_b64_tr_b16 v[226:227], v240 offset:11264\n"
"v_lshrrev_b32 v144, 2, v254\n"
"v_mul_u32_u24 v145, 3072, v144\n"
"s_waitcnt lgkmcnt(8)\n"
"v_mfma_f32_16x16x32_bf16 v[176:179], v[232:235], v[8:11], v[176:179]\n"
"ds_read_b64_tr_b16 v[228:229], v241 offset:9216\n"
"v_lshrrev_b32 v144, 3, v144\n"
"v_bfe_u32 v146, v254, 1, 1\n"
"v_mfma_f32_16x16x32_bf16 v[180:183], v[232:235], v[12:15], v[180:183]\n"
"ds_read_b64_tr_b16 v[230:231], v241 offset:11264\n"
"buffer_load_dwordx4 v245, s[4:7], s42 offen offset:3072 lds\n"
"v_xor_b32 v144, v144, v146\n"
"v_lshl_add_u32 v145, v144, 5, v145\n"
"s_waitcnt lgkmcnt(7)\n"
"v_mfma_f32_16x16x32_bf16 v[184:187], v[236:239], v[8:11], v[184:187]\n"
"ds_read_b64_tr_b16 v[232:233], v242 offset:9216\n"
"v_and_b32 v144, 1, v254\n"
"v_lshl_add_u32 v249, v144, 4, v145\n"
"v_mfma_f32_16x16x32_bf16 v[188:191], v[236:239], v[12:15], v[188:191]\n"
"ds_read_b64_tr_b16 v[234:235], v242 offset:11264\n"
"s_and_b32 s35, s32, 3\n"
"s_mul_i32 s35, s35, 49152\n"
"s_waitcnt lgkmcnt(4)\n"
"v_mfma_f32_16x16x32_bf16 v[192:195], v[224:227], v[8:11], v[192:195]\n"
"ds_read_b64_tr_b16 v[236:237], v243 offset:9216\n"
"s_add_u32 s47, s47, s35\n"
"s_bfe_u32 s35, s32, 0x10001\n"
"s_lshl_b32 s35, s35, 12\n"
"v_mfma_f32_16x16x32_bf16 v[196:199], v[224:227], v[12:15], v[196:199]\n"
"ds_read_b64_tr_b16 v[238:239], v243 offset:11264\n"
"s_add_u32 m0, s48, 0x24000\n"
"s_add_u32 s43, s46, 0x90000\n"
"buffer_load_dwordx4 v248, s[8:11], s43 offen lds\n"
"s_lshr_b32 s36, s32, 2\n"
"s_lshl_b32 s36, s36, 11\n"
"s_waitcnt lgkmcnt(4)\n"
"v_mfma_f32_16x16x32_bf16 v[200:203], v[228:231], v[8:11], v[200:203]\n"
"ds_read_b64_tr_b16 v[224:225], v240 offset:16384\n"
"s_add_u32 s35, s35, s36\n"
"s_and_b32 s36, s32, 1\n"
"v_mfma_f32_16x16x32_bf16 v[204:207], v[228:231], v[12:15], v[204:207]\n"
"ds_read_b64_tr_b16 v[226:227], v240 offset:18432\n"
"s_lshl_b32 s36, s36, 10\n"
"s_add_u32 s49, s35, s36\n"
"s_waitcnt lgkmcnt(4)\n"
"v_mfma_f32_16x16x32_bf16 v[208:211], v[232:235], v[8:11], v[208:211]\n"
"ds_read_b64_tr_b16 v[228:229], v241 offset:16384\n"
"s_lshl_b32 s35, s39, 4\n"
"s_add_u32 s52, s35, s40\n"
"v_mfma_f32_16x16x32_bf16 v[212:215], v[232:235], v[12:15], v[212:215]\n"
"ds_read_b64_tr_b16 v[230:231], v241 offset:18432\n"
"s_add_u32 s44, s46, 0xa7c00\n"
"buffer_load_dwordx4 v248, s[8:11], s44 offen offset:1024 lds\n"
"s_lshl_b32 s52, s52, 7\n"
"s_lshl_b32 s36, s32, 1\n"
"s_waitcnt lgkmcnt(4)\n"
"v_mfma_f32_16x16x32_bf16 v[216:219], v[236:239], v[8:11], v[216:219]\n"
"ds_read_b64_tr_b16 v[232:233], v242 offset:16384\n"
"s_add_u32 s35, s35, s36\n"
"s_mul_i32 s51, s35, 69632\n"
"v_mfma_f32_16x16x32_bf16 v[220:223], v[236:239], v[12:15], v[220:223]\n"
"ds_read_b64_tr_b16 v[234:235], v242 offset:18432\n"
"s_lshl_b32 s35, s40, 10\n"
"s_add_u32 s51, s51, s35\n"
"s_mov_b32 s12, s80\n"
"s_waitcnt lgkmcnt(4)\n"
"v_mfma_f32_16x16x32_bf16 v[160:163], v[224:227], v[16:19], v[160:163]\n"
"ds_read_b64_tr_b16 v[236:237], v243 offset:16384\n"
"s_and_b32 s13, s81, 0xffff\n"
"v_mfma_f32_16x16x32_bf16 v[164:167], v[224:227], v[20:23], v[164:167]\n"
"ds_read_b64_tr_b16 v[238:239], v243 offset:18432\n"
"s_add_u32 m0, s50, 0x0\n"
"s_add_u32 s41, s45, 0x180\n"
"buffer_load_dwordx4 v244, s[4:7], s41 offen lds\n"
"s_mov_b32 s14, 0xa00000\n"
"s_mov_b32 s15, 0x20000\n"
"s_waitcnt lgkmcnt(4)\n"
"v_mfma_f32_16x16x32_bf16 v[168:171], v[228:231], v[16:19], v[168:171]\n"
"ds_read_b64_tr_b16 v[224:225], v240 offset:24576\n"
"s_mov_b32 s16, s68\n"
"s_and_b32 s17, s69, 0xffff\n"
"v_mfma_f32_16x16x32_bf16 v[172:175], v[228:231], v[20:23], v[172:175]\n"
"ds_read_b64_tr_b16 v[226:227], v240 offset:26624\n"
"s_mov_b32 s18, 0xc8000\n"
"s_waitcnt lgkmcnt(4)\n"
"v_mfma_f32_16x16x32_bf16 v[176:179], v[232:235], v[16:19], v[176:179]\n"
"ds_read_b64_tr_b16 v[228:229], v241 offset:24576\n"
"s_mov_b32 s19, 0x20000\n"
"s_mov_b32 s20, s74\n"
"v_mfma_f32_16x16x32_bf16 v[180:183], v[232:235], v[20:23], v[180:183]\n"
"ds_read_b64_tr_b16 v[230:231], v241 offset:26624\n"
"buffer_load_dwordx4 v245, s[4:7], s41 offen offset:1024 lds\n"
"s_and_b32 s21, s75, 0xffff\n"
"s_mov_b32 s22, 0x1e0000\n"
"s_waitcnt lgkmcnt(4)\n"
"v_mfma_f32_16x16x32_bf16 v[184:187], v[236:239], v[16:19], v[184:187]\n"
"ds_read_b64_tr_b16 v[232:233], v242 offset:24576\n"
"s_mov_b32 s23, 0x20000\n"
"s_mov_b32 s24, s78\n"
"v_mfma_f32_16x16x32_bf16 v[188:191], v[236:239], v[20:23], v[188:191]\n"
"ds_read_b64_tr_b16 v[234:235], v242 offset:26624\n"
"s_and_b32 s25, s79, 0xffff\n"
"s_waitcnt lgkmcnt(4)\n"
"v_mfma_f32_16x16x32_bf16 v[192:195], v[224:227], v[16:19], v[192:195]\n"
"ds_read_b64_tr_b16 v[236:237], v243 offset:24576\n"
"s_mov_b32 s26, 0x280000\n"
"s_mov_b32 s27, 0x20000\n"
"v_mfma_f32_16x16x32_bf16 v[196:199], v[224:227], v[20:23], v[196:199]\n"
"ds_read_b64_tr_b16 v[238:239], v243 offset:26624\n"
"s_add_u32 s42, s45, 0x9980\n"
"buffer_load_dwordx4 v244, s[4:7], s42 offen offset:2048 lds\n"
"s_sub_u32 s35, s34, 8\n"
"s_lshr_b32 s36, s35, 2\n"
"s_waitcnt lgkmcnt(4)\n"
"v_mfma_f32_16x16x32_bf16 v[200:203], v[228:231], v[16:19], v[200:203]\n"
"ds_read_b64_tr_b16 v[224:225], v240 offset:17408\n"
"s_add_u32 s36, s36, 8\n"
"s_and_b32 s35, s35, 3\n"
"v_mfma_f32_16x16x32_bf16 v[204:207], v[228:231], v[20:23], v[204:207]\n"
"ds_read_b64_tr_b16 v[226:227], v240 offset:19456\n"
"s_lshl_b32 s35, s35, 2\n"
"s_cmp_lt_u32 s34, 8\n"
"s_cselect_b32 s91, s34, s36\n"
"s_cselect_b32 s35, 0, s35\n"
"s_waitcnt lgkmcnt(4)\n"
"v_mfma_f32_16x16x32_bf16 v[208:211], v[232:235], v[16:19], v[208:211]\n"
"ds_read_b64_tr_b16 v[228:229], v241 offset:17408\n"
"s_lshl_b32 s90, s33, 4\n"
"v_mfma_f32_16x16x32_bf16 v[212:215], v[232:235], v[20:23], v[212:215]\n"
"ds_read_b64_tr_b16 v[230:231], v241 offset:19456\n"
"buffer_load_dwordx4 v245, s[4:7], s42 offen offset:3072 lds\n"
"s_waitcnt lgkmcnt(4)\n"
"v_mfma_f32_16x16x32_bf16 v[216:219], v[236:239], v[16:19], v[216:219]\n"
"ds_read_b64_tr_b16 v[232:233], v242 offset:17408\n"
"s_add_u32 s90, s90, s35\n"
"s_and_b32 s35, s32, 3\n"
"v_mfma_f32_16x16x32_bf16 v[220:223], v[236:239], v[20:23], v[220:223]\n"
"ds_read_b64_tr_b16 v[234:235], v242 offset:19456\n"
"s_lshr_b32 s36, s32, 2\n"
"s_lshl_b32 s91, s91, 7\n"
"s_mul_i32 s38, s36, 25600\n"
"s_waitcnt vmcnt(6)\n"
"s_barrier\n"
"s_waitcnt lgkmcnt(4)\n"
"v_mfma_f32_16x16x32_bf16 v[160:163], v[224:227], v[24:27], v[160:163]\n"
"ds_read_b64_tr_b16 v[236:237], v243 offset:17408\n"
"ds_read_b128 v[32:35], v246 offset:8192\n"
"v_mfma_f32_16x16x32_bf16 v[164:167], v[224:227], v[28:31], v[164:167]\n"
"ds_read_b64_tr_b16 v[238:239], v243 offset:19456\n"
"s_add_u32 m0, s48, 0x18000\n"
"s_add_u32 s43, s46, 0xc0000\n"
"buffer_load_dwordx4 v248, s[8:11], s43 offen lds\n"
"ds_read_b128 v[36:39], v246 offset:10240\n"
"ds_read_b128 v[40:43], v247 offset:8192\n"
"s_waitcnt lgkmcnt(7)\n"
"v_mfma_f32_16x16x32_bf16 v[168:171], v[228:231], v[24:27], v[168:171]\n"
"ds_read_b64_tr_b16 v[224:225], v240 offset:25600\n"
"ds_read_b128 v[44:47], v247 offset:10240\n"
"s_mul_i32 s85, s35, 51200\n"
"v_mfma_f32_16x16x32_bf16 v[172:175], v[228:231], v[28:31], v[172:175]\n"
"ds_read_b64_tr_b16 v[226:227], v240 offset:27648\n"
"s_add_u32 s85, s85, s38\n"
"s_add_u32 s85, s85, s91\n"
"s_waitcnt lgkmcnt(8)\n"
"v_mfma_f32_16x16x32_bf16 v[176:179], v[232:235], v[24:27], v[176:179]\n"
"ds_read_b64_tr_b16 v[228:229], v241 offset:25600\n"
"s_mul_i32 s38, s36, 10240\n"
"s_add_u32 s38, s38, s91\n"
"v_mfma_f32_16x16x32_bf16 v[180:183], v[232:235], v[28:31], v[180:183]\n"
"ds_read_b64_tr_b16 v[230:231], v241 offset:27648\n"
"s_add_u32 s44, s46, 0xd7c00\n"
"buffer_load_dwordx4 v248, s[8:11], s44 offen offset:1024 lds\n"
"s_mul_i32 s87, s35, 122880\n"
"s_add_u32 s87, s87, s38\n"
"s_waitcnt lgkmcnt(7)\n"
"v_mfma_f32_16x16x32_bf16 v[184:187], v[236:239], v[24:27], v[184:187]\n"
"ds_read_b64_tr_b16 v[232:233], v242 offset:25600\n"
"s_bitcmp1_b32 s35, 0\n"
"s_cselect_b32 s38, 40960, 0\n"
"s_add_u32 s84, s87, s38\n"
"v_mfma_f32_16x16x32_bf16 v[188:191], v[236:239], v[28:31], v[188:191]\n"
"ds_read_b64_tr_b16 v[234:235], v242 offset:27648\n"
"s_bitcmp1_b32 s35, 0\n"
"s_cselect_b32 s38, 0, 81920\n"
"s_waitcnt lgkmcnt(4)\n"
"v_mfma_f32_16x16x32_bf16 v[192:195], v[224:227], v[24:27], v[192:195]\n"
"ds_read_b64_tr_b16 v[236:237], v243 offset:25600\n"
"s_add_u32 s87, s87, s38\n"
"s_lshl_b32 s86, s35, 1\n"
"v_mfma_f32_16x16x32_bf16 v[196:199], v[224:227], v[28:31], v[196:199]\n"
"ds_read_b64_tr_b16 v[238:239], v243 offset:27648\n"
"s_add_u32 m0, s50, 0x1000\n"
"s_add_u32 s41, s45, 0x200\n"
"buffer_load_dwordx4 v244, s[4:7], s41 offen lds\n"
"s_add_u32 s86, s86, s36\n"
"s_mul_i32 s86, s86, 102400\n"
"s_waitcnt lgkmcnt(4)\n"
"v_mfma_f32_16x16x32_bf16 v[200:203], v[228:231], v[24:27], v[200:203]\n"
"ds_read_b64_tr_b16 v[224:225], v240 offset:32768\n"
"s_add_u32 s86, s86, s91\n"
"s_add_u32 s86, s86, 20480\n"
"v_mfma_f32_16x16x32_bf16 v[204:207], v[228:231], v[28:31], v[204:207]\n"
"ds_read_b64_tr_b16 v[226:227], v240 offset:34816\n"
"s_lshl_b32 s88, s35, 12\n"
"s_lshl_b32 s37, s36, 11\n"
"s_waitcnt lgkmcnt(4)\n"
"v_mfma_f32_16x16x32_bf16 v[208:211], v[232:235], v[24:27], v[208:211]\n"
"ds_read_b64_tr_b16 v[228:229], v241 offset:32768\n"
"s_add_u32 s88, s88, s37\n"
"s_mul_i32 s37, s36, 2\n"
"v_mfma_f32_16x16x32_bf16 v[212:215], v[232:235], v[28:31], v[212:215]\n"
"ds_read_b64_tr_b16 v[230:231], v241 offset:34816\n"
"buffer_load_dwordx4 v245, s[4:7], s41 offen offset:1024 lds\n"
"s_add_u32 s90, s90, s37\n"
"s_mul_i32 s89, s90, 69632\n"
"s_waitcnt lgkmcnt(4)\n"
"v_mfma_f32_16x16x32_bf16 v[216:219], v[236:239], v[24:27], v[216:219]\n"
"ds_read_b64_tr_b16 v[232:233], v242 offset:32768\n"
"s_lshl_b32 s37, s35, 10\n"
"s_add_u32 s89, s89, s37\n"
"v_mfma_f32_16x16x32_bf16 v[220:223], v[236:239], v[28:31], v[220:223]\n"
"ds_read_b64_tr_b16 v[234:235], v242 offset:34816\n"
"v_lshrrev_b32 v144, 5, v254\n"
"v_mul_u32_u24 v144, 4, v144\n"
"v_bfe_u32 v145, v254, 3, 2\n"
"s_waitcnt lgkmcnt(4)\n"
"v_mfma_f32_16x16x32_bf16 v[160:163], v[224:227], v[32:35], v[160:163]\n"
"ds_read_b64_tr_b16 v[236:237], v243 offset:32768\n"
"v_add_u32 v144, v144, v145\n"
"v_mfma_f32_16x16x32_bf16 v[164:167], v[224:227], v[36:39], v[164:167]\n"
"ds_read_b64_tr_b16 v[238:239], v243 offset:34816\n"
"s_add_u32 s42, s45, 0x9a00\n"
"buffer_load_dwordx4 v244, s[4:7], s42 offen offset:2048 lds\n"
"v_mul_u32_u24 v144, 1280, v144\n"
"v_bfe_u32 v145, v254, 1, 2\n"
"s_waitcnt lgkmcnt(4)\n"
"v_mfma_f32_16x16x32_bf16 v[168:171], v[228:231], v[32:35], v[168:171]\n"
"ds_read_b64_tr_b16 v[224:225], v240 offset:40960\n"
"v_lshrrev_b32 v146, 4, v254\n"
"v_xor_b32 v145, v145, v146\n"
"v_mfma_f32_16x16x32_bf16 v[172:175], v[228:231], v[36:39], v[172:175]\n"
"ds_read_b64_tr_b16 v[226:227], v240 offset:43008\n"
"v_lshl_add_u32 v144, v145, 5, v144\n"
"s_waitcnt lgkmcnt(4)\n"
"v_mfma_f32_16x16x32_bf16 v[176:179], v[232:235], v[32:35], v[176:179]\n"
"ds_read_b64_tr_b16 v[228:229], v241 offset:40960\n"
"v_and_b32 v145, 1, v254\n"
"v_lshl_add_u32 v250, v145, 4, v144\n"
"v_mfma_f32_16x16x32_bf16 v[180:183], v[232:235], v[36:39], v[180:183]\n"
"ds_read_b64_tr_b16 v[230:231], v241 offset:43008\n"
"buffer_load_dwordx4 v245, s[4:7], s42 offen offset:3072 lds\n"
"v_lshrrev_b32 v144, 5, v254\n"
"v_mul_u32_u24 v144, 8, v144\n"
"s_waitcnt lgkmcnt(4)\n"
"v_mfma_f32_16x16x32_bf16 v[184:187], v[236:239], v[32:35], v[184:187]\n"
"ds_read_b64_tr_b16 v[232:233], v242 offset:40960\n"
"v_bfe_u32 v145, v254, 3, 2\n"
"v_add_u32 v144, v144, v145\n"
"v_mfma_f32_16x16x32_bf16 v[188:191], v[236:239], v[36:39], v[188:191]\n"
"ds_read_b64_tr_b16 v[234:235], v242 offset:43008\n"
"v_mul_u32_u24 v144, 1280, v144\n"
"v_bfe_u32 v145, v254, 1, 2\n"
"s_waitcnt lgkmcnt(4)\n"
"v_mfma_f32_16x16x32_bf16 v[192:195], v[224:227], v[32:35], v[192:195]\n"
"ds_read_b64_tr_b16 v[236:237], v243 offset:40960\n"
"v_lshrrev_b32 v146, 4, v254\n"
"v_xor_b32 v145, v145, v146\n"
"v_mfma_f32_16x16x32_bf16 v[196:199], v[224:227], v[36:39], v[196:199]\n"
"ds_read_b64_tr_b16 v[238:239], v243 offset:43008\n"
"s_add_u32 m0, s50, 0x2000\n"
"s_add_u32 s41, s45, 0x280\n"
"buffer_load_dwordx4 v244, s[4:7], s41 offen lds\n"
"v_lshl_add_u32 v144, v145, 5, v144\n"
"v_and_b32 v145, 1, v254\n"
"s_waitcnt lgkmcnt(4)\n"
"v_mfma_f32_16x16x32_bf16 v[200:203], v[228:231], v[32:35], v[200:203]\n"
"ds_read_b64_tr_b16 v[224:225], v240 offset:33792\n"
"v_lshl_add_u32 v251, v145, 4, v144\n"
"v_lshrrev_b32 v144, 5, v254\n"
"v_mfma_f32_16x16x32_bf16 v[204:207], v[228:231], v[36:39], v[204:207]\n"
"ds_read_b64_tr_b16 v[226:227], v240 offset:35840\n"
"v_mul_u32_u24 v144, 40, v144\n"
"s_waitcnt lgkmcnt(4)\n"
"v_mfma_f32_16x16x32_bf16 v[208:211], v[232:235], v[32:35], v[208:211]\n"
"ds_read_b64_tr_b16 v[228:229], v241 offset:33792\n"
"v_bfe_u32 v145, v254, 3, 2\n"
"v_add_u32 v144, v144, v145\n"
"v_mfma_f32_16x16x32_bf16 v[212:215], v[232:235], v[36:39], v[212:215]\n"
"ds_read_b64_tr_b16 v[230:231], v241 offset:35840\n"
"buffer_load_dwordx4 v245, s[4:7], s41 offen offset:1024 lds\n"
"v_mul_u32_u24 v144, 1280, v144\n"
"v_bfe_u32 v145, v254, 1, 2\n"
"s_waitcnt lgkmcnt(4)\n"
"v_mfma_f32_16x16x32_bf16 v[216:219], v[236:239], v[32:35], v[216:219]\n"
"ds_read_b64_tr_b16 v[232:233], v242 offset:33792\n"
"v_lshrrev_b32 v146, 4, v254\n"
"v_xor_b32 v145, v145, v146\n"
"v_mfma_f32_16x16x32_bf16 v[220:223], v[236:239], v[36:39], v[220:223]\n"
"ds_read_b64_tr_b16 v[234:235], v242 offset:35840\n"
"v_lshl_add_u32 v144, v145, 5, v144\n"
"v_and_b32 v145, 1, v254\n"
"v_lshl_add_u32 v252, v145, 4, v144\n"
"s_waitcnt vmcnt(8)\n"
"s_barrier\n"
"s_waitcnt lgkmcnt(4)\n"
"v_mfma_f32_16x16x32_bf16 v[160:163], v[224:227], v[40:43], v[160:163]\n"
"ds_read_b64_tr_b16 v[236:237], v243 offset:33792\n"
"s_add_u32 s42, s45, 0x9a80\n"
"buffer_load_dwordx4 v244, s[4:7], s42 offen offset:2048 lds\n"
"v_mfma_f32_16x16x32_bf16 v[164:167], v[224:227], v[44:47], v[164:167]\n"
"ds_read_b64_tr_b16 v[238:239], v243 offset:35840\n"
"ds_read_b128 v[48:51], v246 offset:0\n"
"s_waitcnt lgkmcnt(5)\n"
"v_mfma_f32_16x16x32_bf16 v[168:171], v[228:231], v[40:43], v[168:171]\n"
"ds_read_b64_tr_b16 v[224:225], v240 offset:41984\n"
"v_mfma_f32_16x16x32_bf16 v[172:175], v[228:231], v[44:47], v[172:175]\n"
"ds_read_b64_tr_b16 v[226:227], v240 offset:44032\n"
"s_waitcnt lgkmcnt(5)\n"
"v_mfma_f32_16x16x32_bf16 v[176:179], v[232:235], v[40:43], v[176:179]\n"
"ds_read_b64_tr_b16 v[228:229], v241 offset:41984\n"
"buffer_load_dwordx4 v245, s[4:7], s42 offen offset:3072 lds\n"
"ds_read_b128 v[52:55], v246 offset:2048\n"
"v_mfma_f32_16x16x32_bf16 v[180:183], v[232:235], v[44:47], v[180:183]\n"
"ds_read_b64_tr_b16 v[230:231], v241 offset:44032\n"
"s_waitcnt lgkmcnt(6)\n"
"v_mfma_f32_16x16x32_bf16 v[184:187], v[236:239], v[40:43], v[184:187]\n"
"ds_read_b64_tr_b16 v[232:233], v242 offset:41984\n"
"v_mfma_f32_16x16x32_bf16 v[188:191], v[236:239], v[44:47], v[188:191]\n"
"ds_read_b64_tr_b16 v[234:235], v242 offset:44032\n"
"s_add_u32 m0, s48, 0x1c000\n"
"s_add_u32 s43, s46, 0xf0000\n"
"buffer_load_dwordx4 v248, s[8:11], s43 offen lds\n"
"s_waitcnt lgkmcnt(5)\n"
"v_mfma_f32_16x16x32_bf16 v[192:195], v[224:227], v[40:43], v[192:195]\n"
"ds_read_b64_tr_b16 v[236:237], v243 offset:41984\n"
"ds_read_b128 v[56:59], v247 offset:0\n"
"v_mfma_f32_16x16x32_bf16 v[196:199], v[224:227], v[44:47], v[196:199]\n"
"ds_read_b64_tr_b16 v[238:239], v243 offset:44032\n"
"s_waitcnt lgkmcnt(5)\n"
"v_mfma_f32_16x16x32_bf16 v[200:203], v[228:231], v[40:43], v[200:203]\n"
"ds_read_b64_tr_b16 v[224:225], v240 offset:49152\n"
"s_add_u32 s44, s46, 0x107c00\n"
"buffer_load_dwordx4 v248, s[8:11], s44 offen offset:1024 lds\n"
"v_mfma_f32_16x16x32_bf16 v[204:207], v[228:231], v[44:47], v[204:207]\n"
"ds_read_b64_tr_b16 v[226:227], v240 offset:51200\n"
"s_waitcnt lgkmcnt(5)\n"
"v_mfma_f32_16x16x32_bf16 v[208:211], v[232:235], v[40:43], v[208:211]\n"
"ds_read_b64_tr_b16 v[228:229], v241 offset:49152\n"
"ds_read_b128 v[60:63], v247 offset:2048\n"
"v_mfma_f32_16x16x32_bf16 v[212:215], v[232:235], v[44:47], v[212:215]\n"
"ds_read_b64_tr_b16 v[230:231], v241 offset:51200\n"
"s_mov_b32 s94, 0x892000\n"
"buffer_load_dwordx2 v[158:159], off, s[12:15], s94 sc1\n"
"s_waitcnt lgkmcnt(5)\n"
"v_mfma_f32_16x16x32_bf16 v[216:219], v[236:239], v[40:43], v[216:219]\n"
"ds_read_b64_tr_b16 v[232:233], v242 offset:49152\n"
"v_mfma_f32_16x16x32_bf16 v[220:223], v[236:239], v[44:47], v[220:223]\n"
"ds_read_b64_tr_b16 v[234:235], v242 offset:51200\n"
"s_waitcnt lgkmcnt(5)\n"
"v_mfma_f32_16x16x32_bf16 v[160:163], v[224:227], v[48:51], v[160:163]\n"
"ds_read_b64_tr_b16 v[236:237], v243 offset:49152\n"
"v_mfma_f32_16x16x32_bf16 v[164:167], v[224:227], v[52:55], v[164:167]\n"
"ds_read_b64_tr_b16 v[238:239], v243 offset:51200\n"
"s_add_u32 m0, s50, 0x0\n"
"s_add_u32 s41, s45, 0x300\n"
"buffer_load_dwordx4 v244, s[4:7], s41 offen lds\n"
"s_waitcnt lgkmcnt(4)\n"
"v_mfma_f32_16x16x32_bf16 v[168:171], v[228:231], v[48:51], v[168:171]\n"
"ds_read_b64_tr_b16 v[224:225], v240 offset:57344\n"
"v_mfma_f32_16x16x32_bf16 v[172:175], v[228:231], v[52:55], v[172:175]\n"
"ds_read_b64_tr_b16 v[226:227], v240 offset:59392\n"
"s_waitcnt lgkmcnt(4)\n"
"v_mfma_f32_16x16x32_bf16 v[176:179], v[232:235], v[48:51], v[176:179]\n"
"ds_read_b64_tr_b16 v[228:229], v241 offset:57344\n"
"v_mfma_f32_16x16x32_bf16 v[180:183], v[232:235], v[52:55], v[180:183]\n"
"ds_read_b64_tr_b16 v[230:231], v241 offset:59392\n"
"buffer_load_dwordx4 v245, s[4:7], s41 offen offset:1024 lds\n"
"s_waitcnt lgkmcnt(4)\n"
"v_mfma_f32_16x16x32_bf16 v[184:187], v[236:239], v[48:51], v[184:187]\n"
"ds_read_b64_tr_b16 v[232:233], v242 offset:57344\n"
"v_mfma_f32_16x16x32_bf16 v[188:191], v[236:239], v[52:55], v[188:191]\n"
"ds_read_b64_tr_b16 v[234:235], v242 offset:59392\n"
"s_waitcnt lgkmcnt(4)\n"
"v_mfma_f32_16x16x32_bf16 v[192:195], v[224:227], v[48:51], v[192:195]\n"
"ds_read_b64_tr_b16 v[236:237], v243 offset:57344\n"
"v_mfma_f32_16x16x32_bf16 v[196:199], v[224:227], v[52:55], v[196:199]\n"
"ds_read_b64_tr_b16 v[238:239], v243 offset:59392\n"
"s_add_u32 s42, s45, 0x9b00\n"
"buffer_load_dwordx4 v244, s[4:7], s42 offen offset:2048 lds\n"
"s_waitcnt lgkmcnt(4)\n"
"v_mfma_f32_16x16x32_bf16 v[200:203], v[228:231], v[48:51], v[200:203]\n"
"ds_read_b64_tr_b16 v[224:225], v240 offset:50176\n"
"v_mfma_f32_16x16x32_bf16 v[204:207], v[228:231], v[52:55], v[204:207]\n"
"ds_read_b64_tr_b16 v[226:227], v240 offset:52224\n"
"s_waitcnt lgkmcnt(4)\n"
"v_mfma_f32_16x16x32_bf16 v[208:211], v[232:235], v[48:51], v[208:211]\n"
"ds_read_b64_tr_b16 v[228:229], v241 offset:50176\n"
"v_mfma_f32_16x16x32_bf16 v[212:215], v[232:235], v[52:55], v[212:215]\n"
"ds_read_b64_tr_b16 v[230:231], v241 offset:52224\n"
"buffer_load_dwordx4 v245, s[4:7], s42 offen offset:3072 lds\n"
"s_waitcnt lgkmcnt(4)\n"
"v_mfma_f32_16x16x32_bf16 v[216:219], v[236:239], v[48:51], v[216:219]\n"
"ds_read_b64_tr_b16 v[232:233], v242 offset:50176\n"
"v_mfma_f32_16x16x32_bf16 v[220:223], v[236:239], v[52:55], v[220:223]\n"
"ds_read_b64_tr_b16 v[234:235], v242 offset:52224\n"
"s_waitcnt vmcnt(11)\n"
"s_barrier\n"
"s_waitcnt lgkmcnt(4)\n"
"v_mfma_f32_16x16x32_bf16 v[160:163], v[224:227], v[56:59], v[160:163]\n"
"ds_read_b64_tr_b16 v[236:237], v243 offset:50176\n"
"ds_read_b128 v[64:67], v246 offset:4096\n"
"v_mfma_f32_16x16x32_bf16 v[164:167], v[224:227], v[60:63], v[164:167]\n"
"ds_read_b64_tr_b16 v[238:239], v243 offset:52224\n"
"s_waitcnt lgkmcnt(5)\n"
"v_mfma_f32_16x16x32_bf16 v[168:171], v[228:231], v[56:59], v[168:171]\n"
"ds_read_b64_tr_b16 v[224:225], v240 offset:58368\n"
"s_add_u32 m0, s48, 0x20000\n"
"s_add_u32 s43, s46, 0x120000\n"
"buffer_load_dwordx4 v248, s[8:11], s43 offen lds\n"
"v_mfma_f32_16x16x32_bf16 v[172:175], v[228:231], v[60:63], v[172:175]\n"
"ds_read_b64_tr_b16 v[226:227], v240 offset:60416\n"
"s_waitcnt lgkmcnt(5)\n"
"v_mfma_f32_16x16x32_bf16 v[176:179], v[232:235], v[56:59], v[176:179]\n"
"ds_read_b64_tr_b16 v[228:229], v241 offset:58368\n"
"ds_read_b128 v[68:71], v246 offset:6144\n"
"v_mfma_f32_16x16x32_bf16 v[180:183], v[232:235], v[60:63], v[180:183]\n"
"ds_read_b64_tr_b16 v[230:231], v241 offset:60416\n"
"s_waitcnt lgkmcnt(5)\n"
"v_mfma_f32_16x16x32_bf16 v[184:187], v[236:239], v[56:59], v[184:187]\n"
"ds_read_b64_tr_b16 v[232:233], v242 offset:58368\n"
"v_mfma_f32_16x16x32_bf16 v[188:191], v[236:239], v[60:63], v[188:191]\n"
"ds_read_b64_tr_b16 v[234:235], v242 offset:60416\n"
"s_waitcnt lgkmcnt(5)\n"
"v_mfma_f32_16x16x32_bf16 v[192:195], v[224:227], v[56:59], v[192:195]\n"
"ds_read_b64_tr_b16 v[236:237], v243 offset:58368\n"
"ds_read_b128 v[72:75], v247 offset:4096\n"
"v_mfma_f32_16x16x32_bf16 v[196:199], v[224:227], v[60:63], v[196:199]\n"
"ds_read_b64_tr_b16 v[238:239], v243 offset:60416\n"
"s_waitcnt lgkmcnt(5)\n"
"v_mfma_f32_16x16x32_bf16 v[200:203], v[228:231], v[56:59], v[200:203]\n"
"ds_read_b64_tr_b16 v[224:225], v240 offset:0\n"
"s_add_u32 s44, s46, 0x137c00\n"
"buffer_load_dwordx4 v248, s[8:11], s44 offen offset:1024 lds\n"
"v_mfma_f32_16x16x32_bf16 v[204:207], v[228:231], v[60:63], v[204:207]\n"
"ds_read_b64_tr_b16 v[226:227], v240 offset:2048\n"
"s_waitcnt lgkmcnt(5)\n"
"v_mfma_f32_16x16x32_bf16 v[208:211], v[232:235], v[56:59], v[208:211]\n"
"ds_read_b64_tr_b16 v[228:229], v241 offset:0\n"
"ds_read_b128 v[76:79], v247 offset:6144\n"
"v_mfma_f32_16x16x32_bf16 v[212:215], v[232:235], v[60:63], v[212:215]\n"
"ds_read_b64_tr_b16 v[230:231], v241 offset:2048\n"
"s_waitcnt lgkmcnt(5)\n"
"v_mfma_f32_16x16x32_bf16 v[216:219], v[236:239], v[56:59], v[216:219]\n"
"ds_read_b64_tr_b16 v[232:233], v242 offset:0\n"
"v_mfma_f32_16x16x32_bf16 v[220:223], v[236:239], v[60:63], v[220:223]\n"
"ds_read_b64_tr_b16 v[234:235], v242 offset:2048\n"
"s_waitcnt lgkmcnt(5)\n"
"v_mfma_f32_16x16x32_bf16 v[160:163], v[224:227], v[64:67], v[160:163]\n"
"ds_read_b64_tr_b16 v[236:237], v243 offset:0\n"
"v_mfma_f32_16x16x32_bf16 v[164:167], v[224:227], v[68:71], v[164:167]\n"
"ds_read_b64_tr_b16 v[238:239], v243 offset:2048\n"
"s_add_u32 m0, s50, 0x1000\n"
"s_add_u32 s41, s45, 0x380\n"
"buffer_load_dwordx4 v244, s[4:7], s41 offen lds\n"
"s_waitcnt lgkmcnt(4)\n"
"v_mfma_f32_16x16x32_bf16 v[168:171], v[228:231], v[64:67], v[168:171]\n"
"ds_read_b64_tr_b16 v[224:225], v240 offset:8192\n"
"v_mfma_f32_16x16x32_bf16 v[172:175], v[228:231], v[68:71], v[172:175]\n"
"ds_read_b64_tr_b16 v[226:227], v240 offset:10240\n"
"s_waitcnt lgkmcnt(4)\n"
"v_mfma_f32_16x16x32_bf16 v[176:179], v[232:235], v[64:67], v[176:179]\n"
"ds_read_b64_tr_b16 v[228:229], v241 offset:8192\n"
"v_mfma_f32_16x16x32_bf16 v[180:183], v[232:235], v[68:71], v[180:183]\n"
"ds_read_b64_tr_b16 v[230:231], v241 offset:10240\n"
"buffer_load_dwordx4 v245, s[4:7], s41 offen offset:1024 lds\n"
"s_waitcnt lgkmcnt(4)\n"
"v_mfma_f32_16x16x32_bf16 v[184:187], v[236:239], v[64:67], v[184:187]\n"
"ds_read_b64_tr_b16 v[232:233], v242 offset:8192\n"
"v_mfma_f32_16x16x32_bf16 v[188:191], v[236:239], v[68:71], v[188:191]\n"
"ds_read_b64_tr_b16 v[234:235], v242 offset:10240\n"
"s_waitcnt lgkmcnt(4)\n"
"v_mfma_f32_16x16x32_bf16 v[192:195], v[224:227], v[64:67], v[192:195]\n"
"ds_read_b64_tr_b16 v[236:237], v243 offset:8192\n"
"v_mfma_f32_16x16x32_bf16 v[196:199], v[224:227], v[68:71], v[196:199]\n"
"ds_read_b64_tr_b16 v[238:239], v243 offset:10240\n"
"s_add_u32 s42, s45, 0x9b80\n"
"buffer_load_dwordx4 v244, s[4:7], s42 offen offset:2048 lds\n"
"s_waitcnt lgkmcnt(4)\n"
"v_mfma_f32_16x16x32_bf16 v[200:203], v[228:231], v[64:67], v[200:203]\n"
"ds_read_b64_tr_b16 v[224:225], v240 offset:1024\n"
"v_mfma_f32_16x16x32_bf16 v[204:207], v[228:231], v[68:71], v[204:207]\n"
"ds_read_b64_tr_b16 v[226:227], v240 offset:3072\n"
"s_waitcnt lgkmcnt(4)\n"
"v_mfma_f32_16x16x32_bf16 v[208:211], v[232:235], v[64:67], v[208:211]\n"
"ds_read_b64_tr_b16 v[228:229], v241 offset:1024\n"
"v_mfma_f32_16x16x32_bf16 v[212:215], v[232:235], v[68:71], v[212:215]\n"
"ds_read_b64_tr_b16 v[230:231], v241 offset:3072\n"
"buffer_load_dwordx4 v245, s[4:7], s42 offen offset:3072 lds\n"
"s_waitcnt lgkmcnt(4)\n"
"v_mfma_f32_16x16x32_bf16 v[216:219], v[236:239], v[64:67], v[216:219]\n"
"ds_read_b64_tr_b16 v[232:233], v242 offset:1024\n"
"v_mfma_f32_16x16x32_bf16 v[220:223], v[236:239], v[68:71], v[220:223]\n"
"ds_read_b64_tr_b16 v[234:235], v242 offset:3072\n"
"s_waitcnt vmcnt(11)\n"
"s_barrier\n"
"s_waitcnt lgkmcnt(4)\n"
"v_mfma_f32_16x16x32_bf16 v[160:163], v[224:227], v[72:75], v[160:163]\n"
"ds_read_b64_tr_b16 v[236:237], v243 offset:1024\n"
"ds_read_b128 v[80:83], v246 offset:8192\n"
"v_mfma_f32_16x16x32_bf16 v[164:167], v[224:227], v[76:79], v[164:167]\n"
"ds_read_b64_tr_b16 v[238:239], v243 offset:3072\n"
"s_waitcnt lgkmcnt(5)\n"
"v_mfma_f32_16x16x32_bf16 v[168:171], v[228:231], v[72:75], v[168:171]\n"
"ds_read_b64_tr_b16 v[224:225], v240 offset:9216\n"
"s_add_u32 m0, s48, 0x24000\n"
"s_add_u32 s43, s46, 0x150000\n"
"buffer_load_dwordx4 v248, s[8:11], s43 offen lds\n"
"v_mfma_f32_16x16x32_bf16 v[172:175], v[228:231], v[76:79], v[172:175]\n"
"ds_read_b64_tr_b16 v[226:227], v240 offset:11264\n"
"s_waitcnt lgkmcnt(5)\n"
"v_mfma_f32_16x16x32_bf16 v[176:179], v[232:235], v[72:75], v[176:179]\n"
"ds_read_b64_tr_b16 v[228:229], v241 offset:9216\n"
"ds_read_b128 v[84:87], v246 offset:10240\n"
"v_mfma_f32_16x16x32_bf16 v[180:183], v[232:235], v[76:79], v[180:183]\n"
"ds_read_b64_tr_b16 v[230:231], v241 offset:11264\n"
"s_waitcnt lgkmcnt(5)\n"
"v_mfma_f32_16x16x32_bf16 v[184:187], v[236:239], v[72:75], v[184:187]\n"
"ds_read_b64_tr_b16 v[232:233], v242 offset:9216\n"
"v_mfma_f32_16x16x32_bf16 v[188:191], v[236:239], v[76:79], v[188:191]\n"
"ds_read_b64_tr_b16 v[234:235], v242 offset:11264\n"
"s_waitcnt lgkmcnt(5)\n"
"v_mfma_f32_16x16x32_bf16 v[192:195], v[224:227], v[72:75], v[192:195]\n"
"ds_read_b64_tr_b16 v[236:237], v243 offset:9216\n"
"ds_read_b128 v[88:91], v247 offset:8192\n"
"v_mfma_f32_16x16x32_bf16 v[196:199], v[224:227], v[76:79], v[196:199]\n"
"ds_read_b64_tr_b16 v[238:239], v243 offset:11264\n"
"s_waitcnt lgkmcnt(5)\n"
"v_mfma_f32_16x16x32_bf16 v[200:203], v[228:231], v[72:75], v[200:203]\n"
"ds_read_b64_tr_b16 v[224:225], v240 offset:16384\n"
"s_add_u32 s44, s46, 0x167c00\n"
"buffer_load_dwordx4 v248, s[8:11], s44 offen offset:1024 lds\n"
"v_mfma_f32_16x16x32_bf16 v[204:207], v[228:231], v[76:79], v[204:207]\n"
"ds_read_b64_tr_b16 v[226:227], v240 offset:18432\n"
"s_waitcnt lgkmcnt(5)\n"
"v_mfma_f32_16x16x32_bf16 v[208:211], v[232:235], v[72:75], v[208:211]\n"
"ds_read_b64_tr_b16 v[228:229], v241 offset:16384\n"
"ds_read_b128 v[92:95], v247 offset:10240\n"
"v_mfma_f32_16x16x32_bf16 v[212:215], v[232:235], v[76:79], v[212:215]\n"
"ds_read_b64_tr_b16 v[230:231], v241 offset:18432\n"
"s_waitcnt lgkmcnt(5)\n"
"v_mfma_f32_16x16x32_bf16 v[216:219], v[236:239], v[72:75], v[216:219]\n"
"ds_read_b64_tr_b16 v[232:233], v242 offset:16384\n"
"v_mfma_f32_16x16x32_bf16 v[220:223], v[236:239], v[76:79], v[220:223]\n"
"ds_read_b64_tr_b16 v[234:235], v242 offset:18432\n"
"s_waitcnt vmcnt(12)\n"
"v_readfirstlane_b32 s58, v158\n"
"v_readfirstlane_b32 s59, v159\n"
"s_nop 0\n"
"s_mul_i32 s56, s58, 0x7F4A7C15\n"
"s_mul_hi_u32 s57, s58, 0x7F4A7C15\n"
"s_mul_i32 s58, s58, 0x9E3779B9\n"
"s_add_u32 s57, s57, s58\n"
"s_mul_i32 s59, s59, 0x7F4A7C15\n"
"s_add_u32 s57, s57, s59\n"
"s_add_u32 s56, s56, 0xD9B4E019\n"
"s_addc_u32 s57, s57, 0x632BE59B\n"
"s_lshr_b32 s35, s34, 1\n"
"s_and_b32 s36, s34, 1\n"
"s_add_u32 s35, s35, s36\n"
"s_cmp_lt_u32 s34, 16\n"
"s_cselect_b32 s35, s34, s35\n"
"s_cmp_lt_u32 s34, 8\n"
"s_cselect_b32 s36, 2, 0\n"
"s_add_u32 s35, s35, s36\n"
"s_add_u32 s0, s35, 0\n"
"s_and_b32 s0, s0, 3\n"
"s_add_u32 s1, s35, 1\n"
"s_and_b32 s1, s1, 3\n"
"s_add_u32 s58, s35, 2\n"
"s_and_b32 s58, s58, 3\n"
"s_add_u32 s59, s35, 3\n"
"s_and_b32 s59, s59, 3\n"
"s_waitcnt lgkmcnt(5)\n"
"v_mfma_f32_16x16x32_bf16 v[160:163], v[224:227], v[80:83], v[160:163]\n"
"ds_read_b64_tr_b16 v[236:237], v243 offset:16384\n"
"v_mfma_f32_16x16x32_bf16 v[164:167], v[224:227], v[84:87], v[164:167]\n"
"ds_read_b64_tr_b16 v[238:239], v243 offset:18432\n"
"s_add_u32 m0, s50, 0x2000\n"
"s_add_u32 s41, s45, 0x400\n"
"buffer_load_dwordx4 v244, s[4:7], s41 offen lds\n"
"s_waitcnt lgkmcnt(4)\n"
"v_mfma_f32_16x16x32_bf16 v[168:171], v[228:231], v[80:83], v[168:171]\n"
"ds_read_b64_tr_b16 v[224:225], v240 offset:24576\n"
"v_mfma_f32_16x16x32_bf16 v[172:175], v[228:231], v[84:87], v[172:175]\n"
"ds_read_b64_tr_b16 v[226:227], v240 offset:26624\n"
"s_waitcnt lgkmcnt(4)\n"
"v_mfma_f32_16x16x32_bf16 v[176:179], v[232:235], v[80:83], v[176:179]\n"
"ds_read_b64_tr_b16 v[228:229], v241 offset:24576\n"
"v_mfma_f32_16x16x32_bf16 v[180:183], v[232:235], v[84:87], v[180:183]\n"
"ds_read_b64_tr_b16 v[230:231], v241 offset:26624\n"
"buffer_load_dwordx4 v245, s[4:7], s41 offen offset:1024 lds\n"
"s_waitcnt lgkmcnt(4)\n"
"v_mfma_f32_16x16x32_bf16 v[184:187], v[236:239], v[80:83], v[184:187]\n"
"ds_read_b64_tr_b16 v[232:233], v242 offset:24576\n"
"v_mfma_f32_16x16x32_bf16 v[188:191], v[236:239], v[84:87], v[188:191]\n"
"ds_read_b64_tr_b16 v[234:235], v242 offset:26624\n"
"s_waitcnt lgkmcnt(4)\n"
"v_mfma_f32_16x16x32_bf16 v[192:195], v[224:227], v[80:83], v[192:195]\n"
"ds_read_b64_tr_b16 v[236:237], v243 offset:24576\n"
"v_mfma_f32_16x16x32_bf16 v[196:199], v[224:227], v[84:87], v[196:199]\n"
"ds_read_b64_tr_b16 v[238:239], v243 offset:26624\n"
"s_add_u32 s42, s45, 0x9c00\n"
"buffer_load_dwordx4 v244, s[4:7], s42 offen offset:2048 lds\n"
"s_waitcnt lgkmcnt(4)\n"
"v_mfma_f32_16x16x32_bf16 v[200:203], v[228:231], v[80:83], v[200:203]\n"
"ds_read_b64_tr_b16 v[224:225], v240 offset:17408\n"
"v_mfma_f32_16x16x32_bf16 v[204:207], v[228:231], v[84:87], v[204:207]\n"
"ds_read_b64_tr_b16 v[226:227], v240 offset:19456\n"
"s_waitcnt lgkmcnt(4)\n"
"v_mfma_f32_16x16x32_bf16 v[208:211], v[232:235], v[80:83], v[208:211]\n"
"ds_read_b64_tr_b16 v[228:229], v241 offset:17408\n"
"v_mfma_f32_16x16x32_bf16 v[212:215], v[232:235], v[84:87], v[212:215]\n"
"ds_read_b64_tr_b16 v[230:231], v241 offset:19456\n"
"buffer_load_dwordx4 v245, s[4:7], s42 offen offset:3072 lds\n"
"s_waitcnt lgkmcnt(4)\n"
"v_mfma_f32_16x16x32_bf16 v[216:219], v[236:239], v[80:83], v[216:219]\n"
"ds_read_b64_tr_b16 v[232:233], v242 offset:17408\n"
"v_mfma_f32_16x16x32_bf16 v[220:223], v[236:239], v[84:87], v[220:223]\n"
"ds_read_b64_tr_b16 v[234:235], v242 offset:19456\n"
"s_waitcnt vmcnt(10)\n"
"s_barrier\n"
"s_waitcnt lgkmcnt(4)\n"
"v_mfma_f32_16x16x32_bf16 v[160:163], v[224:227], v[88:91], v[160:163]\n"
"ds_read_b64_tr_b16 v[236:237], v243 offset:17408\n"
"ds_read_b128 v[96:99], v246 offset:0\n"
"v_mfma_f32_16x16x32_bf16 v[164:167], v[224:227], v[92:95], v[164:167]\n"
"ds_read_b64_tr_b16 v[238:239], v243 offset:19456\n"
"s_waitcnt lgkmcnt(5)\n"
"v_mfma_f32_16x16x32_bf16 v[168:171], v[228:231], v[88:91], v[168:171]\n"
"ds_read_b64_tr_b16 v[224:225], v240 offset:25600\n"
"s_add_u32 m0, s48, 0x18000\n"
"s_add_u32 s43, s46, 0x180000\n"
"buffer_load_dwordx4 v248, s[8:11], s43 offen lds\n"
"v_mfma_f32_16x16x32_bf16 v[172:175], v[228:231], v[92:95], v[172:175]\n"
"ds_read_b64_tr_b16 v[226:227], v240 offset:27648\n"
"s_waitcnt lgkmcnt(5)\n"
"v_mfma_f32_16x16x32_bf16 v[176:179], v[232:235], v[88:91], v[176:179]\n"
"ds_read_b64_tr_b16 v[228:229], v241 offset:25600\n"
"ds_read_b128 v[100:103], v246 offset:2048\n"
"v_mfma_f32_16x16x32_bf16 v[180:183], v[232:235], v[92:95], v[180:183]\n"
"ds_read_b64_tr_b16 v[230:231], v241 offset:27648\n"
"s_waitcnt lgkmcnt(5)\n"
"v_mfma_f32_16x16x32_bf16 v[184:187], v[236:239], v[88:91], v[184:187]\n"
"ds_read_b64_tr_b16 v[232:233], v242 offset:25600\n"
"v_mfma_f32_16x16x32_bf16 v[188:191], v[236:239], v[92:95], v[188:191]\n"
"ds_read_b64_tr_b16 v[234:235], v242 offset:27648\n"
"s_waitcnt lgkmcnt(5)\n"
"v_mfma_f32_16x16x32_bf16 v[192:195], v[224:227], v[88:91], v[192:195]\n"
"ds_read_b64_tr_b16 v[236:237], v243 offset:25600\n"
"ds_read_b128 v[104:107], v247 offset:0\n"
"v_mfma_f32_16x16x32_bf16 v[196:199], v[224:227], v[92:95], v[196:199]\n"
"ds_read_b64_tr_b16 v[238:239], v243 offset:27648\n"
"s_waitcnt lgkmcnt(5)\n"
"v_mfma_f32_16x16x32_bf16 v[200:203], v[228:231], v[88:91], v[200:203]\n"
"ds_read_b64_tr_b16 v[224:225], v240 offset:32768\n"
"s_add_u32 s44, s46, 0x197c00\n"
"buffer_load_dwordx4 v248, s[8:11], s44 offen offset:1024 lds\n"
"v_mfma_f32_16x16x32_bf16 v[204:207], v[228:231], v[92:95], v[204:207]\n"
"ds_read_b64_tr_b16 v[226:227], v240 offset:34816\n"
"s_waitcnt lgkmcnt(5)\n"
"v_mfma_f32_16x16x32_bf16 v[208:211], v[232:235], v[88:91], v[208:211]\n"
"ds_read_b64_tr_b16 v[228:229], v240 offset:40960\n"
"ds_read_b128 v[108:111], v247 offset:2048\n"
"v_mfma_f32_16x16x32_bf16 v[212:215], v[232:235], v[92:95], v[212:215]\n"
"ds_read_b64_tr_b16 v[230:231], v240 offset:43008\n"
"s_waitcnt lgkmcnt(5)\n"
"v_mfma_f32_16x16x32_bf16 v[216:219], v[236:239], v[88:91], v[216:219]\n"
"ds_read_b64_tr_b16 v[232:233], v241 offset:32768\n"
"v_mfma_f32_16x16x32_bf16 v[220:223], v[236:239], v[92:95], v[220:223]\n"
"ds_read_b64_tr_b16 v[234:235], v241 offset:34816\n"
"s_waitcnt vmcnt(6)\n"
"s_barrier\n"
"s_waitcnt lgkmcnt(5)\n"
"v_mfma_f32_16x16x32_bf16 v[160:163], v[224:227], v[96:99], v[160:163]\n"
"ds_read_b64_tr_b16 v[236:237], v241 offset:40960\n"
"s_add_u32 m0, s48, 0x1c000\n"
"s_add_u32 s43, s46, 0x1b0000\n"
"buffer_load_dwordx4 v248, s[8:11], s43 offen lds\n"
"v_mfma_f32_16x16x32_bf16 v[164:167], v[224:227], v[100:103], v[164:167]\n"
"ds_read_b64_tr_b16 v[238:239], v241 offset:43008\n"
"ds_read_b128 v[112:115], v246 offset:4096\n"
"s_waitcnt lgkmcnt(5)\n"
"v_mfma_f32_16x16x32_bf16 v[192:195], v[228:231], v[96:99], v[192:195]\n"
"ds_read_b64_tr_b16 v[224:225], v240 offset:33792\n"
"v_mfma_f32_16x16x32_bf16 v[196:199], v[228:231], v[100:103], v[196:199]\n"
"ds_read_b64_tr_b16 v[226:227], v240 offset:35840\n"
"s_add_u32 s44, s46, 0x1c7c00\n"
"buffer_load_dwordx4 v248, s[8:11], s44 offen offset:1024 lds\n"
"s_waitcnt lgkmcnt(5)\n"
"v_mfma_f32_16x16x32_bf16 v[168:171], v[232:235], v[96:99], v[168:171]\n"
"ds_read_b64_tr_b16 v[228:229], v240 offset:41984\n"
"ds_read_b128 v[116:119], v246 offset:6144\n"
"v_mfma_f32_16x16x32_bf16 v[172:175], v[232:235], v[100:103], v[172:175]\n"
"ds_read_b64_tr_b16 v[230:231], v240 offset:44032\n"
"s_waitcnt lgkmcnt(6)\n"
"v_mfma_f32_16x16x32_bf16 v[200:203], v[236:239], v[96:99], v[200:203]\n"
"ds_read_b64_tr_b16 v[232:233], v241 offset:33792\n"
"s_add_u32 m0, s50, 0x0\n"
"s_add_u32 s41, s45, 0x480\n"
"buffer_load_dwordx4 v244, s[4:7], s41 offen lds\n"
"v_mfma_f32_16x16x32_bf16 v[204:207], v[236:239], v[100:103], v[204:207]\n"
"ds_read_b64_tr_b16 v[234:235], v241 offset:35840\n"
"s_waitcnt lgkmcnt(5)\n"
"v_mfma_f32_16x16x32_bf16 v[160:163], v[224:227], v[104:107], v[160:163]\n"
"ds_read_b64_tr_b16 v[236:237], v241 offset:41984\n"
"buffer_load_dwordx4 v245, s[4:7], s41 offen offset:1024 lds\n"
"v_mfma_f32_16x16x32_bf16 v[164:167], v[224:227], v[108:111], v[164:167]\n"
"ds_read_b64_tr_b16 v[238:239], v241 offset:44032\n"
"ds_read_b128 v[120:123], v247 offset:4096\n"
"s_waitcnt lgkmcnt(5)\n"
"v_mfma_f32_16x16x32_bf16 v[192:195], v[228:231], v[104:107], v[192:195]\n"
"ds_read_b64_tr_b16 v[224:225], v240 offset:49152\n"
"v_mfma_f32_16x16x32_bf16 v[196:199], v[228:231], v[108:111], v[196:199]\n"
"ds_read_b64_tr_b16 v[226:227], v240 offset:51200\n"
"s_add_u32 s42, s45, 0x9c80\n"
"buffer_load_dwordx4 v244, s[4:7], s42 offen offset:2048 lds\n"
"s_waitcnt lgkmcnt(5)\n"
"v_mfma_f32_16x16x32_bf16 v[168:171], v[232:235], v[104:107], v[168:171]\n"
"ds_read_b64_tr_b16 v[228:229], v240 offset:57344\n"
"ds_read_b128 v[124:127], v247 offset:6144\n"
"v_mfma_f32_16x16x32_bf16 v[172:175], v[232:235], v[108:111], v[172:175]\n"
"ds_read_b64_tr_b16 v[230:231], v240 offset:59392\n"
"s_waitcnt lgkmcnt(6)\n"
"v_mfma_f32_16x16x32_bf16 v[200:203], v[236:239], v[104:107], v[200:203]\n"
"ds_read_b64_tr_b16 v[232:233], v241 offset:49152\n"
"buffer_load_dwordx4 v245, s[4:7], s42 offen offset:3072 lds\n"
"v_mfma_f32_16x16x32_bf16 v[204:207], v[236:239], v[108:111], v[204:207]\n"
"ds_read_b64_tr_b16 v[234:235], v241 offset:51200\n"
"s_waitcnt vmcnt(6)\n"
"s_barrier\n"
"s_waitcnt lgkmcnt(5)\n"
"v_mfma_f32_16x16x32_bf16 v[160:163], v[224:227], v[112:115], v[160:163]\n"
"ds_read_b64_tr_b16 v[236:237], v241 offset:57344\n"
"ds_read_b128 v[128:131], v246 offset:8192\n"
"v_mfma_f32_16x16x32_bf16 v[164:167], v[224:227], v[116:119], v[164:167]\n"
"ds_read_b64_tr_b16 v[238:239], v241 offset:59392\n"
"s_waitcnt lgkmcnt(5)\n"
"v_mfma_f32_16x16x32_bf16 v[192:195], v[228:231], v[112:115], v[192:195]\n"
"ds_read_b64_tr_b16 v[224:225], v240 offset:50176\n"
"v_mfma_f32_16x16x32_bf16 v[196:199], v[228:231], v[116:119], v[196:199]\n"
"ds_read_b64_tr_b16 v[226:227], v240 offset:52224\n"
"s_waitcnt lgkmcnt(5)\n"
"v_mfma_f32_16x16x32_bf16 v[168:171], v[232:235], v[112:115], v[168:171]\n"
"ds_read_b64_tr_b16 v[228:229], v240 offset:58368\n"
"ds_read_b128 v[132:135], v246 offset:10240\n"
"v_mfma_f32_16x16x32_bf16 v[172:175], v[232:235], v[116:119], v[172:175]\n"
"ds_read_b64_tr_b16 v[230:231], v240 offset:60416\n"
"s_waitcnt lgkmcnt(5)\n"
"v_mfma_f32_16x16x32_bf16 v[200:203], v[236:239], v[112:115], v[200:203]\n"
"ds_read_b64_tr_b16 v[232:233], v241 offset:50176\n"
"v_mfma_f32_16x16x32_bf16 v[204:207], v[236:239], v[116:119], v[204:207]\n"
"ds_read_b64_tr_b16 v[234:235], v241 offset:52224\n"
"s_waitcnt lgkmcnt(5)\n"
"v_mfma_f32_16x16x32_bf16 v[160:163], v[224:227], v[120:123], v[160:163]\n"
"ds_read_b64_tr_b16 v[236:237], v241 offset:58368\n"
"ds_read_b128 v[136:139], v247 offset:8192\n"
"v_mfma_f32_16x16x32_bf16 v[164:167], v[224:227], v[124:127], v[164:167]\n"
"ds_read_b64_tr_b16 v[238:239], v241 offset:60416\n"
"s_waitcnt lgkmcnt(5)\n"
"v_mfma_f32_16x16x32_bf16 v[192:195], v[228:231], v[120:123], v[192:195]\n"
"ds_read_b64_tr_b16 v[224:225], v240 offset:0\n"
"v_mfma_f32_16x16x32_bf16 v[196:199], v[228:231], v[124:127], v[196:199]\n"
"ds_read_b64_tr_b16 v[226:227], v240 offset:2048\n"
"s_waitcnt lgkmcnt(5)\n"
"v_mfma_f32_16x16x32_bf16 v[168:171], v[232:235], v[120:123], v[168:171]\n"
"ds_read_b64_tr_b16 v[228:229], v240 offset:8192\n"
"ds_read_b128 v[140:143], v247 offset:10240\n"
"v_mfma_f32_16x16x32_bf16 v[172:175], v[232:235], v[124:127], v[172:175]\n"
"ds_read_b64_tr_b16 v[230:231], v240 offset:10240\n"
"s_waitcnt lgkmcnt(5)\n"
"v_mfma_f32_16x16x32_bf16 v[200:203], v[236:239], v[120:123], v[200:203]\n"
"ds_read_b64_tr_b16 v[232:233], v241 offset:0\n"
"v_mfma_f32_16x16x32_bf16 v[204:207], v[236:239], v[124:127], v[204:207]\n"
"ds_read_b64_tr_b16 v[234:235], v241 offset:2048\n"
"s_waitcnt vmcnt(0)\n"
"s_barrier\n"
"s_waitcnt lgkmcnt(5)\n"
"v_mfma_f32_16x16x32_bf16 v[160:163], v[224:227], v[128:131], v[160:163]\n"
"ds_read_b64_tr_b16 v[236:237], v241 offset:8192\n"
"ds_read_b128 v[144:147], v246 offset:0\n"
"v_mfma_f32_16x16x32_bf16 v[164:167], v[224:227], v[132:135], v[164:167]\n"
"ds_read_b64_tr_b16 v[238:239], v241 offset:10240\n"
"s_waitcnt lgkmcnt(5)\n"
"v_mfma_f32_16x16x32_bf16 v[192:195], v[228:231], v[128:131], v[192:195]\n"
"ds_read_b64_tr_b16 v[224:225], v240 offset:1024\n"
"v_mfma_f32_16x16x32_bf16 v[196:199], v[228:231], v[132:135], v[196:199]\n"
"ds_read_b64_tr_b16 v[226:227], v240 offset:3072\n"
"s_waitcnt lgkmcnt(5)\n"
"v_mfma_f32_16x16x32_bf16 v[168:171], v[232:235], v[128:131], v[168:171]\n"
"ds_read_b64_tr_b16 v[228:229], v240 offset:9216\n"
"ds_read_b128 v[148:151], v246 offset:2048\n"
"v_mfma_f32_16x16x32_bf16 v[172:175], v[232:235], v[132:135], v[172:175]\n"
"ds_read_b64_tr_b16 v[230:231], v240 offset:11264\n"
"s_waitcnt lgkmcnt(5)\n"
"v_mfma_f32_16x16x32_bf16 v[200:203], v[236:239], v[128:131], v[200:203]\n"
"ds_read_b64_tr_b16 v[232:233], v241 offset:1024\n"
"v_mfma_f32_16x16x32_bf16 v[204:207], v[236:239], v[132:135], v[204:207]\n"
"ds_read_b64_tr_b16 v[234:235], v241 offset:3072\n"
"s_waitcnt lgkmcnt(5)\n"
"v_mfma_f32_16x16x32_bf16 v[160:163], v[224:227], v[136:139], v[160:163]\n"
"ds_read_b64_tr_b16 v[236:237], v241 offset:9216\n"
"ds_read_b128 v[152:155], v247 offset:0\n"
"v_mfma_f32_16x16x32_bf16 v[164:167], v[224:227], v[140:143], v[164:167]\n"
"ds_read_b64_tr_b16 v[238:239], v241 offset:11264\n"
"s_waitcnt lgkmcnt(5)\n"
"v_mfma_f32_16x16x32_bf16 v[192:195], v[228:231], v[136:139], v[192:195]\n"
"ds_read_b64_tr_b16 v[224:225], v240 offset:16384\n"
"v_mfma_f32_16x16x32_bf16 v[196:199], v[228:231], v[140:143], v[196:199]\n"
"ds_read_b64_tr_b16 v[226:227], v240 offset:18432\n"
"s_waitcnt lgkmcnt(5)\n"
"v_mfma_f32_16x16x32_bf16 v[168:171], v[232:235], v[136:139], v[168:171]\n"
"ds_read_b64_tr_b16 v[228:229], v240 offset:24576\n"
"ds_read_b128 v[156:159], v247 offset:2048\n"
"v_mfma_f32_16x16x32_bf16 v[172:175], v[232:235], v[140:143], v[172:175]\n"
"ds_read_b64_tr_b16 v[230:231], v240 offset:26624\n"
"s_waitcnt lgkmcnt(5)\n"
"v_mfma_f32_16x16x32_bf16 v[200:203], v[236:239], v[136:139], v[200:203]\n"
"ds_read_b64_tr_b16 v[232:233], v241 offset:16384\n"
"v_mfma_f32_16x16x32_bf16 v[204:207], v[236:239], v[140:143], v[204:207]\n"
"ds_read_b64_tr_b16 v[234:235], v241 offset:18432\n"
"s_waitcnt lgkmcnt(5)\n"
"v_mfma_f32_16x16x32_bf16 v[160:163], v[224:227], v[144:147], v[160:163]\n"
"ds_read_b64_tr_b16 v[236:237], v241 offset:24576\n"
"v_mfma_f32_16x16x32_bf16 v[164:167], v[224:227], v[148:151], v[164:167]\n"
"ds_read_b64_tr_b16 v[238:239], v241 offset:26624\n"
"s_waitcnt lgkmcnt(4)\n"
"v_mfma_f32_16x16x32_bf16 v[192:195], v[228:231], v[144:147], v[192:195]\n"
"ds_read_b64_tr_b16 v[224:225], v240 offset:17408\n"
"v_mfma_f32_16x16x32_bf16 v[196:199], v[228:231], v[148:151], v[196:199]\n"
"ds_read_b64_tr_b16 v[226:227], v240 offset:19456\n"
"s_waitcnt lgkmcnt(4)\n"
"v_mfma_f32_16x16x32_bf16 v[168:171], v[232:235], v[144:147], v[168:171]\n"
"ds_read_b64_tr_b16 v[228:229], v240 offset:25600\n"
"v_mfma_f32_16x16x32_bf16 v[172:175], v[232:235], v[148:151], v[172:175]\n"
"ds_read_b64_tr_b16 v[230:231], v240 offset:27648\n"
"s_waitcnt lgkmcnt(4)\n"
"v_mfma_f32_16x16x32_bf16 v[200:203], v[236:239], v[144:147], v[200:203]\n"
"ds_read_b64_tr_b16 v[232:233], v241 offset:17408\n"
"v_mfma_f32_16x16x32_bf16 v[204:207], v[236:239], v[148:151], v[204:207]\n"
"ds_read_b64_tr_b16 v[234:235], v241 offset:19456\n"
"s_waitcnt lgkmcnt(4)\n"
"v_mfma_f32_16x16x32_bf16 v[160:163], v[224:227], v[152:155], v[160:163]\n"
"ds_read_b64_tr_b16 v[236:237], v241 offset:25600\n"
"v_mfma_f32_16x16x32_bf16 v[164:167], v[224:227], v[156:159], v[164:167]\n"
"ds_read_b64_tr_b16 v[238:239], v241 offset:27648\n"
"s_waitcnt lgkmcnt(4)\n"
"v_mfma_f32_16x16x32_bf16 v[192:195], v[228:231], v[152:155], v[192:195]\n"
"ds_read_b64_tr_b16 v[224:225], v242 offset:32768\n"
"v_mfma_f32_16x16x32_bf16 v[196:199], v[228:231], v[156:159], v[196:199]\n"
"ds_read_b64_tr_b16 v[226:227], v242 offset:34816\n"
"s_waitcnt lgkmcnt(4)\n"
"v_mfma_f32_16x16x32_bf16 v[168:171], v[232:235], v[152:155], v[168:171]\n"
"ds_read_b64_tr_b16 v[228:229], v242 offset:40960\n"
"v_mfma_f32_16x16x32_bf16 v[172:175], v[232:235], v[156:159], v[172:175]\n"
"ds_read_b64_tr_b16 v[230:231], v242 offset:43008\n"
"s_waitcnt lgkmcnt(4)\n"
"v_mfma_f32_16x16x32_bf16 v[200:203], v[236:239], v[152:155], v[200:203]\n"
"ds_read_b64_tr_b16 v[232:233], v243 offset:32768\n"
"v_mfma_f32_16x16x32_bf16 v[204:207], v[236:239], v[156:159], v[204:207]\n"
"ds_read_b64_tr_b16 v[234:235], v243 offset:34816\n"
"s_barrier\n"
"s_waitcnt lgkmcnt(4)\n"
"v_mfma_f32_16x16x32_bf16 v[176:179], v[224:227], v[96:99], v[176:179]\n"
"ds_read_b64_tr_b16 v[236:237], v243 offset:40960\n"
"s_add_u32 m0, s49, 0x0\n"
"s_add_u32 s43, s47, 0x0\n"
"buffer_load_dwordx4 v249, s[8:11], s43 offen lds\n"
"v_bfe_u32 v240, v254, 4, 1\n"
"v_mfma_f32_16x16x32_bf16 v[180:183], v[224:227], v[100:103], v[180:183]\n"
"ds_read_b64_tr_b16 v[238:239], v243 offset:43008\n"
"v_bfe_u32 v241, v254, 2, 2\n"
"v_lshl_add_u32 v241, v240, 3, v241\n"
"s_waitcnt lgkmcnt(4)\n"
"v_mfma_f32_16x16x32_bf16 v[208:211], v[228:231], v[96:99], v[208:211]\n"
"ds_read_b64_tr_b16 v[224:225], v242 offset:33792\n"
"v_lshlrev_b32 v241, 6, v241\n"
"v_lshl_add_u32 v241, v240, 5, v241\n"
"v_mfma_f32_16x16x32_bf16 v[212:215], v[228:231], v[100:103], v[212:215]\n"
"ds_read_b64_tr_b16 v[226:227], v242 offset:35840\n"
"v_and_b32 v240, 3, v254\n"
"s_waitcnt lgkmcnt(4)\n"
"v_mfma_f32_16x16x32_bf16 v[184:187], v[232:235], v[96:99], v[184:187]\n"
"ds_read_b64_tr_b16 v[228:229], v242 offset:41984\n"
"s_add_u32 m0, s49, 0x2000\n"
"s_add_u32 s43, s47, 0x30000\n"
"buffer_load_dwordx4 v249, s[8:11], s43 offen lds\n"
"v_lshl_add_u32 v241, v240, 3, v241\n"
"v_lshrrev_b32 v240, 5, v254\n"
"v_mfma_f32_16x16x32_bf16 v[188:191], v[232:235], v[100:103], v[188:191]\n"
"ds_read_b64_tr_b16 v[230:231], v242 offset:44032\n"
"v_lshl_add_u32 v240, v240, 10, v241\n"
"v_xor_b32 v241, 32, v240\n"
"s_waitcnt lgkmcnt(4)\n"
"v_mfma_f32_16x16x32_bf16 v[216:219], v[236:239], v[96:99], v[216:219]\n"
"ds_read_b64_tr_b16 v[232:233], v243 offset:33792\n"
"v_mul_f32 v244, 0xbfb8aa3b, v160\n"
"v_mul_f32 v245, 0xbfb8aa3b, v161\n"
"v_mfma_f32_16x16x32_bf16 v[220:223], v[236:239], v[100:103], v[220:223]\n"
"ds_read_b64_tr_b16 v[234:235], v243 offset:35840\n"
"s_add_u32 m0, s49, 0x4000\n"
"s_add_u32 s43, s47, 0x60000\n"
"buffer_load_dwordx4 v249, s[8:11], s43 offen lds\n"
"v_mul_f32 v246, 0xbfb8aa3b, v162\n"
"s_waitcnt lgkmcnt(4)\n"
"v_mfma_f32_16x16x32_bf16 v[176:179], v[224:227], v[104:107], v[176:179]\n"
"ds_read_b64_tr_b16 v[236:237], v243 offset:41984\n"
"v_mul_f32 v247, 0xbfb8aa3b, v163\n"
"v_exp_f32 v244, v244\n"
"v_mfma_f32_16x16x32_bf16 v[180:183], v[224:227], v[108:111], v[180:183]\n"
"ds_read_b64_tr_b16 v[238:239], v243 offset:44032\n"
"v_mul_f32 v192, v160, v192\n"
"v_exp_f32 v245, v245\n"
"s_waitcnt lgkmcnt(4)\n"
"v_mfma_f32_16x16x32_bf16 v[208:211], v[228:231], v[104:107], v[208:211]\n"
"ds_read_b64_tr_b16 v[224:225], v242 offset:49152\n"
"s_add_u32 m0, s49, 0x6000\n"
"s_add_u32 s43, s47, 0x90000\n"
"buffer_load_dwordx4 v249, s[8:11], s43 offen lds\n"
"v_mul_f32 v193, v161, v193\n"
"v_exp_f32 v246, v246\n"
"v_mfma_f32_16x16x32_bf16 v[212:215], v[228:231], v[108:111], v[212:215]\n"
"ds_read_b64_tr_b16 v[226:227], v242 offset:51200\n"
"v_mul_f32 v194, v162, v194\n"
"v_exp_f32 v247, v247\n"
"s_waitcnt lgkmcnt(4)\n"
"v_mfma_f32_16x16x32_bf16 v[184:187], v[232:235], v[104:107], v[184:187]\n"
"ds_read_b64_tr_b16 v[228:229], v242 offset:57344\n"
"v_mul_f32 v195, v163, v195\n"
"v_add_f32 v244, 1.0, v244\n"
"v_mfma_f32_16x16x32_bf16 v[188:191], v[232:235], v[108:111], v[188:191]\n"
"ds_read_b64_tr_b16 v[230:231], v242 offset:59392\n"
"s_add_u32 m0, s49, 0x8000\n"
"s_add_u32 s43, s47, 0xc0000\n"
"buffer_load_dwordx4 v249, s[8:11], s43 offen lds\n"
"v_add_f32 v245, 1.0, v245\n"
"s_waitcnt lgkmcnt(4)\n"
"v_mfma_f32_16x16x32_bf16 v[216:219], v[236:239], v[104:107], v[216:219]\n"
"ds_read_b64_tr_b16 v[232:233], v243 offset:49152\n"
"v_add_f32 v246, 1.0, v246\n"
"v_add_f32 v247, 1.0, v247\n"
"v_mfma_f32_16x16x32_bf16 v[220:223], v[236:239], v[108:111], v[220:223]\n"
"ds_read_b64_tr_b16 v[234:235], v243 offset:51200\n"
"v_rcp_f32 v244, v244\n"
"v_rcp_f32 v245, v245\n"
"v_rcp_f32 v246, v246\n"
"s_waitcnt lgkmcnt(4)\n"
"v_mfma_f32_16x16x32_bf16 v[176:179], v[224:227], v[112:115], v[176:179]\n"
"ds_read_b64_tr_b16 v[236:237], v243 offset:57344\n"
"v_rcp_f32 v247, v247\n"
"v_mul_f32 v192, v244, v192\n"
"v_mfma_f32_16x16x32_bf16 v[180:183], v[224:227], v[116:119], v[180:183]\n"
"ds_read_b64_tr_b16 v[238:239], v243 offset:59392\n"
"s_add_u32 m0, s88, 0x14000\n"
"s_mul_i32 s96, s0, 491520\n"
"s_add_u32 s96, s96, s84\n"
"buffer_load_dwordx4 v250, s[20:23], s96 offen lds\n"
"v_mul_f32 v193, v245, v193\n"
"s_waitcnt lgkmcnt(4)\n"
"v_mfma_f32_16x16x32_bf16 v[208:211], v[228:231], v[112:115], v[208:211]\n"
"ds_read_b64_tr_b16 v[224:225], v242 offset:50176\n"
"v_mul_f32 v194, v246, v194\n"
"v_mul_f32 v195, v247, v195\n"
"v_mfma_f32_16x16x32_bf16 v[212:215], v[228:231], v[116:119], v[212:215]\n"
"ds_read_b64_tr_b16 v[226:227], v242 offset:52224\n"
"v_mul_f32 v244, 0xbfb8aa3b, v168\n"
"s_waitcnt lgkmcnt(4)\n"
"v_mfma_f32_16x16x32_bf16 v[184:187], v[232:235], v[112:115], v[184:187]\n"
"ds_read_b64_tr_b16 v[228:229], v242 offset:58368\n"
"v_mul_f32 v245, 0xbfb8aa3b, v169\n"
"v_mul_f32 v246, 0xbfb8aa3b, v170\n"
"v_mfma_f32_16x16x32_bf16 v[188:191], v[232:235], v[116:119], v[188:191]\n"
"ds_read_b64_tr_b16 v[230:231], v242 offset:60416\n"
"s_add_u32 s97, s96, 0x4c00\n"
"buffer_load_dwordx4 v250, s[20:23], s97 offen offset:1024 lds\n"
"v_mul_f32 v247, 0xbfb8aa3b, v171\n"
"v_exp_f32 v244, v244\n"
"s_waitcnt lgkmcnt(4)\n"
"v_mfma_f32_16x16x32_bf16 v[216:219], v[236:239], v[112:115], v[216:219]\n"
"ds_read_b64_tr_b16 v[232:233], v243 offset:50176\n"
"v_mul_f32 v200, v168, v200\n"
"v_exp_f32 v245, v245\n"
"v_mfma_f32_16x16x32_bf16 v[220:223], v[236:239], v[116:119], v[220:223]\n"
"ds_read_b64_tr_b16 v[234:235], v243 offset:52224\n"
"v_mul_f32 v201, v169, v201\n"
"v_exp_f32 v246, v246\n"
"s_waitcnt lgkmcnt(4)\n"
"v_mfma_f32_16x16x32_bf16 v[176:179], v[224:227], v[120:123], v[176:179]\n"
"ds_read_b64_tr_b16 v[236:237], v243 offset:58368\n"
"v_mul_f32 v202, v170, v202\n"
"v_exp_f32 v247, v247\n"
"v_mfma_f32_16x16x32_bf16 v[180:183], v[224:227], v[124:127], v[180:183]\n"
"ds_read_b64_tr_b16 v[238:239], v243 offset:60416\n"
"s_add_u32 m0, s88, 0x10000\n"
"s_mul_i32 s96, s1, 491520\n"
"s_add_u32 s96, s96, s84\n"
"buffer_load_dwordx4 v250, s[20:23], s96 offen lds\n"
"v_mul_f32 v203, v171, v203\n"
"v_add_f32 v244, 1.0, v244\n"
"s_waitcnt lgkmcnt(4)\n"
"v_mfma_f32_16x16x32_bf16 v[208:211], v[228:231], v[120:123], v[208:211]\n"
"ds_read_b64_tr_b16 v[224:225], v242 offset:0\n"
"v_add_f32 v245, 1.0, v245\n"
"v_add_f32 v246, 1.0, v246\n"
"v_mfma_f32_16x16x32_bf16 v[212:215], v[228:231], v[124:127], v[212:215]\n"
"ds_read_b64_tr_b16 v[226:227], v242 offset:2048\n"
"v_add_f32 v247, 1.0, v247\n"
"s_waitcnt lgkmcnt(4)\n"
"v_mfma_f32_16x16x32_bf16 v[184:187], v[232:235], v[120:123], v[184:187]\n"
"ds_read_b64_tr_b16 v[228:229], v242 offset:8192\n"
"v_rcp_f32 v244, v244\n"
"v_rcp_f32 v245, v245\n"
"v_mfma_f32_16x16x32_bf16 v[188:191], v[232:235], v[124:127], v[188:191]\n"
"ds_read_b64_tr_b16 v[230:231], v242 offset:10240\n"
"s_add_u32 s97, s96, 0x4c00\n"
"buffer_load_dwordx4 v250, s[20:23], s97 offen offset:1024 lds\n"
"v_rcp_f32 v246, v246\n"
"v_rcp_f32 v247, v247\n"
"s_waitcnt lgkmcnt(4)\n"
"v_mfma_f32_16x16x32_bf16 v[216:219], v[236:239], v[120:123], v[216:219]\n"
"ds_read_b64_tr_b16 v[232:233], v243 offset:0\n"
"v_mul_f32 v200, v244, v200\n"
"v_mul_f32 v201, v245, v201\n"
"v_mfma_f32_16x16x32_bf16 v[220:223], v[236:239], v[124:127], v[220:223]\n"
"ds_read_b64_tr_b16 v[234:235], v243 offset:2048\n"
"v_mul_f32 v202, v246, v202\n"
"v_mul_f32 v203, v247, v203\n"
"v_cvt_pk_bf16_f32 v168, v192, v193\n"
"s_waitcnt lgkmcnt(4)\n"
"v_mfma_f32_16x16x32_bf16 v[176:179], v[224:227], v[128:131], v[176:179]\n"
"ds_read_b64_tr_b16 v[236:237], v243 offset:8192\n"
"v_cvt_pk_bf16_f32 v169, v194, v195\n"
"v_cvt_pk_bf16_f32 v170, v200, v201\n"
"v_mfma_f32_16x16x32_bf16 v[180:183], v[224:227], v[132:135], v[180:183]\n"
"ds_read_b64_tr_b16 v[238:239], v243 offset:10240\n"
"v_cvt_pk_bf16_f32 v171, v202, v203\n"
"s_waitcnt lgkmcnt(4)\n"
"v_mfma_f32_16x16x32_bf16 v[208:211], v[228:231], v[128:131], v[208:211]\n"
"ds_read_b64_tr_b16 v[224:225], v242 offset:1024\n"
"s_add_u32 m0, s88, 0xc000\n"
"s_mul_i32 s96, s58, 491520\n"
"s_add_u32 s96, s96, s84\n"
"buffer_load_dwordx4 v250, s[20:23], s96 offen lds\n"
"s_add_u32 s35, s51, 0x0\n"
"v_mfma_f32_16x16x32_bf16 v[212:215], v[228:231], v[132:135], v[212:215]\n"
"ds_read_b64_tr_b16 v[226:227], v242 offset:3072\n"
"buffer_store_dwordx4 v[168:171], v253, s[12:15], s35 offen sc1\n"
"v_mul_f32 v244, 0xbfb8aa3b, v164\n"
"s_waitcnt lgkmcnt(4)\n"
"v_mfma_f32_16x16x32_bf16 v[184:187], v[232:235], v[128:131], v[184:187]\n"
"ds_read_b64_tr_b16 v[228:229], v242 offset:9216\n"
"v_mul_f32 v245, 0xbfb8aa3b, v165\n"
"v_mul_f32 v246, 0xbfb8aa3b, v166\n"
"v_mfma_f32_16x16x32_bf16 v[188:191], v[232:235], v[132:135], v[188:191]\n"
"ds_read_b64_tr_b16 v[230:231], v242 offset:11264\n"
"v_mul_f32 v247, 0xbfb8aa3b, v167\n"
"v_exp_f32 v244, v244\n"
"s_waitcnt lgkmcnt(4)\n"
"v_mfma_f32_16x16x32_bf16 v[216:219], v[236:239], v[128:131], v[216:219]\n"
"ds_read_b64_tr_b16 v[232:233], v243 offset:1024\n"
"v_mul_f32 v196, v164, v196\n"
"v_exp_f32 v245, v245\n"
"v_mfma_f32_16x16x32_bf16 v[220:223], v[236:239], v[132:135], v[220:223]\n"
"ds_read_b64_tr_b16 v[234:235], v243 offset:3072\n"
"v_mul_f32 v197, v165, v197\n"
"s_waitcnt lgkmcnt(4)\n"
"v_mfma_f32_16x16x32_bf16 v[176:179], v[224:227], v[136:139], v[176:179]\n"
"ds_read_b64_tr_b16 v[236:237], v243 offset:9216\n"
"v_exp_f32 v246, v246\n"
"v_mul_f32 v198, v166, v198\n"
"v_mfma_f32_16x16x32_bf16 v[180:183], v[224:227], v[140:143], v[180:183]\n"
"ds_read_b64_tr_b16 v[238:239], v243 offset:11264\n"
"v_exp_f32 v247, v247\n"
"v_mul_f32 v199, v167, v199\n"
"s_waitcnt lgkmcnt(4)\n"
"v_mfma_f32_16x16x32_bf16 v[208:211], v[228:231], v[136:139], v[208:211]\n"
"ds_read_b64_tr_b16 v[224:225], v242 offset:16384\n"
"s_add_u32 s97, s96, 0x4c00\n"
"buffer_load_dwordx4 v250, s[20:23], s97 offen offset:1024 lds\n"
"v_add_f32 v244, 1.0, v244\n"
"v_add_f32 v245, 1.0, v245\n"
"v_mfma_f32_16x16x32_bf16 v[212:215], v[228:231], v[140:143], v[212:215]\n"
"ds_read_b64_tr_b16 v[226:227], v242 offset:18432\n"
"v_add_f32 v246, 1.0, v246\n"
"v_add_f32 v247, 1.0, v247\n"
"s_waitcnt lgkmcnt(4)\n"
"v_mfma_f32_16x16x32_bf16 v[184:187], v[232:235], v[136:139], v[184:187]\n"
"ds_read_b64_tr_b16 v[228:229], v242 offset:24576\n"
"v_rcp_f32 v244, v244\n"
"v_rcp_f32 v245, v245\n"
"v_mfma_f32_16x16x32_bf16 v[188:191], v[232:235], v[140:143], v[188:191]\n"
"ds_read_b64_tr_b16 v[230:231], v242 offset:26624\n"
"v_rcp_f32 v246, v246\n"
"s_waitcnt lgkmcnt(4)\n"
"v_mfma_f32_16x16x32_bf16 v[216:219], v[236:239], v[136:139], v[216:219]\n"
"ds_read_b64_tr_b16 v[232:233], v243 offset:16384\n"
"v_rcp_f32 v247, v247\n"
"v_mul_f32 v196, v244, v196\n"
"v_mfma_f32_16x16x32_bf16 v[220:223], v[236:239], v[140:143], v[220:223]\n"
"ds_read_b64_tr_b16 v[234:235], v243 offset:18432\n"
"v_mul_f32 v197, v245, v197\n"
"v_mul_f32 v198, v246, v198\n"
"v_mul_f32 v199, v247, v199\n"
"s_waitcnt vmcnt(11)\n"
"s_barrier\n"
"s_waitcnt lgkmcnt(4)\n"
"v_mfma_f32_16x16x32_bf16 v[176:179], v[224:227], v[144:147], v[176:179]\n"
"ds_read_b64_tr_b16 v[236:237], v243 offset:24576\n"
"v_mul_f32 v244, 0xbfb8aa3b, v172\n"
"v_mul_f32 v245, 0xbfb8aa3b, v173\n"
"v_mfma_f32_16x16x32_bf16 v[180:183], v[224:227], v[148:151], v[180:183]\n"
"ds_read_b64_tr_b16 v[238:239], v243 offset:26624\n"
"v_mul_f32 v246, 0xbfb8aa3b, v174\n"
"s_waitcnt lgkmcnt(4)\n"
"v_mfma_f32_16x16x32_bf16 v[208:211], v[228:231], v[144:147], v[208:211]\n"
"ds_read_b64_tr_b16 v[224:225], v242 offset:17408\n"
"v_mul_f32 v247, 0xbfb8aa3b, v175\n"
"v_exp_f32 v244, v244\n"
"v_mfma_f32_16x16x32_bf16 v[212:215], v[228:231], v[148:151], v[212:215]\n"
"ds_read_b64_tr_b16 v[226:227], v242 offset:19456\n"
"v_mul_f32 v204, v172, v204\n"
"s_waitcnt lgkmcnt(4)\n"
"v_mfma_f32_16x16x32_bf16 v[184:187], v[232:235], v[144:147], v[184:187]\n"
"ds_read_b64_tr_b16 v[228:229], v242 offset:25600\n"
"v_exp_f32 v245, v245\n"
"v_mul_f32 v205, v173, v205\n"
"v_mfma_f32_16x16x32_bf16 v[188:191], v[232:235], v[148:151], v[188:191]\n"
"ds_read_b64_tr_b16 v[230:231], v242 offset:27648\n"
"v_exp_f32 v246, v246\n"
"v_mul_f32 v206, v174, v206\n"
"s_waitcnt lgkmcnt(4)\n"
"v_mfma_f32_16x16x32_bf16 v[216:219], v[236:239], v[144:147], v[216:219]\n"
"ds_read_b64_tr_b16 v[232:233], v243 offset:17408\n"
"v_exp_f32 v247, v247\n"
"v_mul_f32 v207, v175, v207\n"
"v_mfma_f32_16x16x32_bf16 v[220:223], v[236:239], v[148:151], v[220:223]\n"
"ds_read_b64_tr_b16 v[234:235], v243 offset:19456\n"
"v_add_f32 v244, 1.0, v244\n"
"v_add_f32 v245, 1.0, v245\n"
"s_waitcnt lgkmcnt(4)\n"
"v_mfma_f32_16x16x32_bf16 v[176:179], v[224:227], v[152:155], v[176:179]\n"
"ds_read_b64_tr_b16 v[236:237], v243 offset:25600\n"
"v_add_f32 v246, 1.0, v246\n"
"v_add_f32 v247, 1.0, v247\n"
"v_mfma_f32_16x16x32_bf16 v[180:183], v[224:227], v[156:159], v[180:183]\n"
"ds_read_b64_tr_b16 v[238:239], v243 offset:27648\n"
"v_rcp_f32 v244, v244\n"
"v_rcp_f32 v245, v245\n"
"s_waitcnt lgkmcnt(4)\n"
"v_mfma_f32_16x16x32_bf16 v[208:211], v[228:231], v[152:155], v[208:211]\n"
"ds_read_b64_tr_b16 v[224:225], v240 offset:0\n"
"v_rcp_f32 v246, v246\n"
"v_rcp_f32 v247, v247\n"
"v_mfma_f32_16x16x32_bf16 v[212:215], v[228:231], v[156:159], v[212:215]\n"
"ds_read_b64_tr_b16 v[226:227], v240 offset:256\n"
"v_mul_f32 v204, v244, v204\n"
"s_waitcnt lgkmcnt(4)\n"
"v_mfma_f32_16x16x32_bf16 v[184:187], v[232:235], v[152:155], v[184:187]\n"
"ds_read_b64_tr_b16 v[228:229], v241 offset:0\n"
"v_mul_f32 v205, v245, v205\n"
"v_mul_f32 v206, v246, v206\n"
"v_mfma_f32_16x16x32_bf16 v[188:191], v[232:235], v[156:159], v[188:191]\n"
"ds_read_b64_tr_b16 v[230:231], v241 offset:256\n"
"v_mul_f32 v207, v247, v207\n"
"v_cvt_pk_bf16_f32 v172, v196, v197\n"
"s_waitcnt lgkmcnt(4)\n"
"v_mfma_f32_16x16x32_bf16 v[216:219], v[236:239], v[152:155], v[216:219]\n"
"ds_read_b64_tr_b16 v[232:233], v240 offset:2048\n"
"v_cvt_pk_bf16_f32 v173, v198, v199\n"
"v_cvt_pk_bf16_f32 v174, v204, v205\n"
"v_mfma_f32_16x16x32_bf16 v[220:223], v[236:239], v[156:159], v[220:223]\n"
"ds_read_b64_tr_b16 v[234:235], v240 offset:2304\n"
"v_cvt_pk_bf16_f32 v175, v206, v207\n"
"s_add_u32 s35, s51, 0x11000\n"
"buffer_store_dwordx4 v[172:175], v253, s[12:15], s35 offen sc1\n"
"s_waitcnt lgkmcnt(4)\n"
"v_mfma_f32_16x16x32_bf16 v[160:163], v[224:227], v[0:3], 0\n"
"ds_read_b64_tr_b16 v[236:237], v241 offset:2048\n"
"v_mul_f32 v244, 0xbfb8aa3b, v176\n"
"v_mul_f32 v245, 0xbfb8aa3b, v177\n"
"v_mfma_f32_16x16x32_bf16 v[164:167], v[224:227], v[4:7], 0\n"
"ds_read_b64_tr_b16 v[238:239], v241 offset:2304\n"
"v_mul_f32 v246, 0xbfb8aa3b, v178\n"
"s_waitcnt lgkmcnt(4)\n"
"v_mfma_f32_16x16x32_bf16 v[168:171], v[228:231], v[0:3], 0\n"
"ds_read_b64_tr_b16 v[224:225], v240 offset:4096\n"
"v_mul_f32 v247, 0xbfb8aa3b, v179\n"
"v_exp_f32 v244, v244\n"
"v_mul_f32 v208, v176, v208\n"
"v_mfma_f32_16x16x32_bf16 v[172:175], v[228:231], v[4:7], 0\n"
"ds_read_b64_tr_b16 v[226:227], v240 offset:4352\n"
"v_exp_f32 v245, v245\n"
"v_mul_f32 v209, v177, v209\n"
"s_waitcnt lgkmcnt(4)\n"
"v_mfma_f32_16x16x32_bf16 v[192:195], v[232:235], v[0:3], 0\n"
"ds_read_b64_tr_b16 v[228:229], v241 offset:4096\n"
"v_exp_f32 v246, v246\n"
"v_mul_f32 v210, v178, v210\n"
"v_mfma_f32_16x16x32_bf16 v[196:199], v[232:235], v[4:7], 0\n"
"ds_read_b64_tr_b16 v[230:231], v241 offset:4352\n"
"v_exp_f32 v247, v247\n"
"v_mul_f32 v211, v179, v211\n"
"s_waitcnt lgkmcnt(4)\n"
"v_mfma_f32_16x16x32_bf16 v[200:203], v[236:239], v[0:3], 0\n"
"ds_read_b64_tr_b16 v[232:233], v240 offset:6144\n"
"v_add_f32 v244, 1.0, v244\n"
"v_add_f32 v245, 1.0, v245\n"
"v_add_f32 v246, 1.0, v246\n"
"v_mfma_f32_16x16x32_bf16 v[204:207], v[236:239], v[4:7], 0\n"
"ds_read_b64_tr_b16 v[234:235], v240 offset:6400\n"
"v_add_f32 v247, 1.0, v247\n"
"v_rcp_f32 v244, v244\n"
"v_rcp_f32 v245, v245\n"
"s_waitcnt vmcnt(11)\n"
"s_barrier\n"
"s_waitcnt lgkmcnt(4)\n"
"v_mfma_f32_16x16x32_bf16 v[160:163], v[224:227], v[8:11], v[160:163]\n"
"ds_read_b64_tr_b16 v[236:237], v241 offset:6144\n"
"v_rcp_f32 v246, v246\n"
"v_rcp_f32 v247, v247\n"
"v_mfma_f32_16x16x32_bf16 v[164:167], v[224:227], v[12:15], v[164:167]\n"
"ds_read_b64_tr_b16 v[238:239], v241 offset:6400\n"
"v_mul_f32 v208, v244, v208\n"
"s_waitcnt lgkmcnt(4)\n"
"v_mfma_f32_16x16x32_bf16 v[168:171], v[228:231], v[8:11], v[168:171]\n"
"ds_read_b64_tr_b16 v[224:225], v240 offset:8192\n"
"s_add_u32 m0, s49, 0xa000\n"
"s_add_u32 s43, s47, 0xf0000\n"
"buffer_load_dwordx4 v249, s[8:11], s43 offen lds\n"
"v_mul_f32 v209, v245, v209\n"
"v_mul_f32 v210, v246, v210\n"
"v_mul_f32 v211, v247, v211\n"
"v_mfma_f32_16x16x32_bf16 v[172:175], v[228:231], v[12:15], v[172:175]\n"
"ds_read_b64_tr_b16 v[226:227], v240 offset:8448\n"
"v_mul_f32 v244, 0xbfb8aa3b, v184\n"
"v_mul_f32 v245, 0xbfb8aa3b, v185\n"
"s_waitcnt lgkmcnt(4)\n"
"v_mfma_f32_16x16x32_bf16 v[192:195], v[232:235], v[8:11], v[192:195]\n"
"ds_read_b64_tr_b16 v[228:229], v241 offset:8192\n"
"v_mul_f32 v246, 0xbfb8aa3b, v186\n"
"v_mul_f32 v247, 0xbfb8aa3b, v187\n"
"v_mfma_f32_16x16x32_bf16 v[196:199], v[232:235], v[12:15], v[196:199]\n"
"ds_read_b64_tr_b16 v[230:231], v241 offset:8448\n"
"v_exp_f32 v244, v244\n"
"v_mul_f32 v216, v184, v216\n"
"s_waitcnt lgkmcnt(4)\n"
"v_mfma_f32_16x16x32_bf16 v[200:203], v[236:239], v[8:11], v[200:203]\n"
"ds_read_b64_tr_b16 v[232:233], v240 offset:10240\n"
"v_exp_f32 v245, v245\n"
"v_mul_f32 v217, v185, v217\n"
"v_exp_f32 v246, v246\n"
"v_mfma_f32_16x16x32_bf16 v[204:207], v[236:239], v[12:15], v[204:207]\n"
"ds_read_b64_tr_b16 v[234:235], v240 offset:10496\n"
"v_mul_f32 v218, v186, v218\n"
"v_exp_f32 v247, v247\n"
"v_mul_f32 v219, v187, v219\n"
"s_waitcnt lgkmcnt(4)\n"
"v_mfma_f32_16x16x32_bf16 v[160:163], v[224:227], v[16:19], v[160:163]\n"
"ds_read_b64_tr_b16 v[236:237], v241 offset:10240\n"
"v_add_f32 v244, 1.0, v244\n"
"v_add_f32 v245, 1.0, v245\n"
"v_mfma_f32_16x16x32_bf16 v[164:167], v[224:227], v[20:23], v[164:167]\n"
"ds_read_b64_tr_b16 v[238:239], v241 offset:10496\n"
"s_add_u32 m0, s88, 0x18000\n"
"s_mul_i32 s96, s59, 491520\n"
"s_add_u32 s96, s96, s84\n"
"buffer_load_dwordx4 v250, s[20:23], s96 offen lds\n"
"v_add_f32 v246, 1.0, v246\n"
"s_waitcnt lgkmcnt(4)\n"
"v_mfma_f32_16x16x32_bf16 v[168:171], v[228:231], v[16:19], v[168:171]\n"
"ds_read_b64_tr_b16 v[224:225], v240 offset:12288\n"
"v_add_f32 v247, 1.0, v247\n"
"v_rcp_f32 v244, v244\n"
"v_rcp_f32 v245, v245\n"
"v_mfma_f32_16x16x32_bf16 v[172:175], v[228:231], v[20:23], v[172:175]\n"
"ds_read_b64_tr_b16 v[226:227], v240 offset:12544\n"
"v_rcp_f32 v246, v246\n"
"v_rcp_f32 v247, v247\n"
"s_waitcnt lgkmcnt(4)\n"
"v_mfma_f32_16x16x32_bf16 v[192:195], v[232:235], v[16:19], v[192:195]\n"
"ds_read_b64_tr_b16 v[228:229], v241 offset:12288\n"
"v_mul_f32 v216, v244, v216\n"
"v_mul_f32 v217, v245, v217\n"
"v_mfma_f32_16x16x32_bf16 v[196:199], v[232:235], v[20:23], v[196:199]\n"
"ds_read_b64_tr_b16 v[230:231], v241 offset:12544\n"
"s_add_u32 s97, s96, 0x4c00\n"
"buffer_load_dwordx4 v250, s[20:23], s97 offen offset:1024 lds\n"
"v_mul_f32 v218, v246, v218\n"
"v_mul_f32 v219, v247, v219\n"
"s_waitcnt lgkmcnt(4)\n"
"v_mfma_f32_16x16x32_bf16 v[200:203], v[236:239], v[16:19], v[200:203]\n"
"ds_read_b64_tr_b16 v[232:233], v240 offset:14336\n"
"v_cvt_pk_bf16_f32 v176, v208, v209\n"
"v_cvt_pk_bf16_f32 v177, v210, v211\n"
"v_cvt_pk_bf16_f32 v178, v216, v217\n"
"v_mfma_f32_16x16x32_bf16 v[204:207], v[236:239], v[20:23], v[204:207]\n"
"ds_read_b64_tr_b16 v[234:235], v240 offset:14592\n"
"v_cvt_pk_bf16_f32 v179, v218, v219\n"
"s_add_u32 s35, s51, 0x4000\n"
"buffer_store_dwordx4 v[176:179], v253, s[12:15], s35 offen sc1\n"
"s_waitcnt vmcnt(14)\n"
"s_waitcnt vmcnt(4)\n"
"s_barrier\n"
"s_waitcnt lgkmcnt(4)\n"
"v_mfma_f32_16x16x32_bf16 v[160:163], v[224:227], v[24:27], v[160:163]\n"
"ds_read_b64_tr_b16 v[236:237], v241 offset:14336\n"
"v_mul_f32 v244, 0xbfb8aa3b, v180\n"
"v_mul_f32 v245, 0xbfb8aa3b, v181\n"
"v_mfma_f32_16x16x32_bf16 v[164:167], v[224:227], v[28:31], v[164:167]\n"
"ds_read_b64_tr_b16 v[238:239], v241 offset:14592\n"
"s_cmp_lg_u32 s32, 0\n"
"s_cbranch_scc1 .Lpub1\n"
"v_mov_b32 v0, s56\n"
"v_mov_b32 v1, s57\n"
"s_add_u32 s94, s52, 0x882000\n"
"s_mov_b64 exec, 1\n"
"buffer_store_dwordx2 v[0:1], off, s[12:15], s94 sc1\n"
"s_mov_b64 exec, -1\n"
".Lpub1:\n"
"v_mul_f32 v246, 0xbfb8aa3b, v182\n"
"s_waitcnt lgkmcnt(4)\n"
"v_mfma_f32_16x16x32_bf16 v[168:171], v[228:231], v[24:27], v[168:171]\n"
"ds_read_b64_tr_b16 v[224:225], v240 offset:16384\n"
"v_mul_f32 v247, 0xbfb8aa3b, v183\n"
"v_exp_f32 v244, v244\n"
"v_mul_f32 v212, v180, v212\n"
"v_mfma_f32_16x16x32_bf16 v[172:175], v[228:231], v[28:31], v[172:175]\n"
"ds_read_b64_tr_b16 v[226:227], v240 offset:16640\n"
"v_exp_f32 v245, v245\n"
"v_mul_f32 v213, v181, v213\n"
"s_waitcnt lgkmcnt(4)\n"
"v_mfma_f32_16x16x32_bf16 v[192:195], v[232:235], v[24:27], v[192:195]\n"
"ds_read_b64_tr_b16 v[228:229], v241 offset:16384\n"
"v_exp_f32 v246, v246\n"
"v_mul_f32 v214, v182, v214\n"
"v_mfma_f32_16x16x32_bf16 v[196:199], v[232:235], v[28:31], v[196:199]\n"
"ds_read_b64_tr_b16 v[230:231], v241 offset:16640\n"
"s_add_u32 m0, s49, 0x0\n"
"s_add_u32 s43, s47, 0x120000\n"
"buffer_load_dwordx4 v249, s[8:11], s43 offen lds\n"
"v_exp_f32 v247, v247\n"
"v_mul_f32 v215, v183, v215\n"
"s_waitcnt lgkmcnt(4)\n"
"v_mfma_f32_16x16x32_bf16 v[200:203], v[236:239], v[24:27], v[200:203]\n"
"ds_read_b64_tr_b16 v[232:233], v240 offset:18432\n"
"v_add_f32 v244, 1.0, v244\n"
"v_add_f32 v245, 1.0, v245\n"
"v_add_f32 v246, 1.0, v246\n"
"v_mfma_f32_16x16x32_bf16 v[204:207], v[236:239], v[28:31], v[204:207]\n"
"ds_read_b64_tr_b16 v[234:235], v240 offset:18688\n"
"v_add_f32 v247, 1.0, v247\n"
"v_rcp_f32 v244, v244\n"
"v_rcp_f32 v245, v245\n"
"s_waitcnt lgkmcnt(4)\n"
"v_mfma_f32_16x16x32_bf16 v[160:163], v[224:227], v[32:35], v[160:163]\n"
"ds_read_b64_tr_b16 v[236:237], v241 offset:18432\n"
"v_rcp_f32 v246, v246\n"
"v_rcp_f32 v247, v247\n"
"v_mfma_f32_16x16x32_bf16 v[164:167], v[224:227], v[36:39], v[164:167]\n"
"ds_read_b64_tr_b16 v[238:239], v241 offset:18688\n"
"s_add_u32 m0, s88, 0x1c000\n"
"s_mul_i32 s96, s0, 491520\n"
"s_add_u32 s96, s96, s84\n"
"s_add_u32 s96, s96, 0xa000\n"
"buffer_load_dwordx4 v250, s[20:23], s96 offen lds\n"
"v_mul_f32 v212, v244, v212\n"
"s_waitcnt lgkmcnt(4)\n"
"v_mfma_f32_16x16x32_bf16 v[168:171], v[228:231], v[32:35], v[168:171]\n"
"ds_read_b64_tr_b16 v[224:225], v240 offset:20480\n"
"v_mul_f32 v213, v245, v213\n"
"v_mul_f32 v214, v246, v214\n"
"v_mul_f32 v215, v247, v215\n"
"v_mfma_f32_16x16x32_bf16 v[172:175], v[228:231], v[36:39], v[172:175]\n"
"ds_read_b64_tr_b16 v[226:227], v240 offset:20736\n"
"v_mul_f32 v244, 0xbfb8aa3b, v188\n"
"v_mul_f32 v245, 0xbfb8aa3b, v189\n"
"s_waitcnt lgkmcnt(4)\n"
"v_mfma_f32_16x16x32_bf16 v[192:195], v[232:235], v[32:35], v[192:195]\n"
"ds_read_b64_tr_b16 v[228:229], v241 offset:20480\n"
"v_mul_f32 v246, 0xbfb8aa3b, v190\n"
"v_mul_f32 v247, 0xbfb8aa3b, v191\n"
"v_mfma_f32_16x16x32_bf16 v[196:199], v[232:235], v[36:39], v[196:199]\n"
"ds_read_b64_tr_b16 v[230:231], v241 offset:20736\n"
"s_add_u32 s97, s96, 0x4c00\n"
"buffer_load_dwordx4 v250, s[20:23], s97 offen offset:1024 lds\n"
"v_exp_f32 v244, v244\n"
"v_mul_f32 v220, v188, v220\n"
"s_waitcnt lgkmcnt(4)\n"
"v_mfma_f32_16x16x32_bf16 v[200:203], v[236:239], v[32:35], v[200:203]\n"
"ds_read_b64_tr_b16 v[232:233], v240 offset:22528\n"
"v_exp_f32 v245, v245\n"
"v_mul_f32 v221, v189, v221\n"
"v_exp_f32 v246, v246\n"
"v_mfma_f32_16x16x32_bf16 v[204:207], v[236:239], v[36:39], v[204:207]\n"
"ds_read_b64_tr_b16 v[234:235], v240 offset:22784\n"
"v_mul_f32 v222, v190, v222\n"
"v_exp_f32 v247, v247\n"
"v_mul_f32 v223, v191, v223\n"
"s_barrier\n"
"s_waitcnt lgkmcnt(4)\n"
"v_mfma_f32_16x16x32_bf16 v[160:163], v[224:227], v[40:43], v[160:163]\n"
"ds_read_b64_tr_b16 v[236:237], v241 offset:22528\n"
"v_add_f32 v244, 1.0, v244\n"
"v_add_f32 v245, 1.0, v245\n"
"v_mfma_f32_16x16x32_bf16 v[164:167], v[224:227], v[44:47], v[164:167]\n"
"ds_read_b64_tr_b16 v[238:239], v241 offset:22784\n"
"v_add_f32 v246, 1.0, v246\n"
"s_waitcnt lgkmcnt(4)\n"
"v_mfma_f32_16x16x32_bf16 v[168:171], v[228:231], v[40:43], v[168:171]\n"
"ds_read_b64_tr_b16 v[224:225], v240 offset:24576\n"
"s_add_u32 m0, s49, 0x2000\n"
"s_add_u32 s43, s47, 0x150000\n"
"buffer_load_dwordx4 v249, s[8:11], s43 offen lds\n"
"v_add_f32 v247, 1.0, v247\n"
"v_rcp_f32 v244, v244\n"
"v_rcp_f32 v245, v245\n"
"v_mfma_f32_16x16x32_bf16 v[172:175], v[228:231], v[44:47], v[172:175]\n"
"ds_read_b64_tr_b16 v[226:227], v240 offset:24832\n"
"v_rcp_f32 v246, v246\n"
"v_rcp_f32 v247, v247\n"
"s_waitcnt lgkmcnt(4)\n"
"v_mfma_f32_16x16x32_bf16 v[192:195], v[232:235], v[40:43], v[192:195]\n"
"ds_read_b64_tr_b16 v[228:229], v241 offset:24576\n"
"v_mul_f32 v220, v244, v220\n"
"v_mul_f32 v221, v245, v221\n"
"v_mfma_f32_16x16x32_bf16 v[196:199], v[232:235], v[44:47], v[196:199]\n"
"ds_read_b64_tr_b16 v[230:231], v241 offset:24832\n"
"v_mul_f32 v222, v246, v222\n"
"v_mul_f32 v223, v247, v223\n"
"s_waitcnt lgkmcnt(4)\n"
"v_mfma_f32_16x16x32_bf16 v[200:203], v[236:239], v[40:43], v[200:203]\n"
"ds_read_b64_tr_b16 v[232:233], v240 offset:26624\n"
"v_cvt_pk_bf16_f32 v180, v212, v213\n"
"v_cvt_pk_bf16_f32 v181, v214, v215\n"
"v_cvt_pk_bf16_f32 v182, v220, v221\n"
"v_mfma_f32_16x16x32_bf16 v[204:207], v[236:239], v[44:47], v[204:207]\n"
"ds_read_b64_tr_b16 v[234:235], v240 offset:26880\n"
"v_cvt_pk_bf16_f32 v183, v222, v223\n"
"s_add_u32 s35, s51, 0x15000\n"
"buffer_store_dwordx4 v[180:183], v253, s[12:15], s35 offen sc1\n"
"s_waitcnt lgkmcnt(4)\n"
"v_mfma_f32_16x16x32_bf16 v[160:163], v[224:227], v[48:51], v[160:163]\n"
"ds_read_b64_tr_b16 v[236:237], v241 offset:26624\n"
"v_mfma_f32_16x16x32_bf16 v[164:167], v[224:227], v[52:55], v[164:167]\n"
"ds_read_b64_tr_b16 v[238:239], v241 offset:26880\n"
"s_add_u32 m0, s88, 0x20000\n"
"s_mul_i32 s96, s1, 491520\n"
"s_add_u32 s96, s96, s84\n"
"s_add_u32 s96, s96, 0xa000\n"
"buffer_load_dwordx4 v250, s[20:23], s96 offen lds\n"
"s_waitcnt lgkmcnt(4)\n"
"v_mfma_f32_16x16x32_bf16 v[168:171], v[228:231], v[48:51], v[168:171]\n"
"ds_read_b64_tr_b16 v[224:225], v240 offset:28672\n"
"v_mfma_f32_16x16x32_bf16 v[172:175], v[228:231], v[52:55], v[172:175]\n"
"ds_read_b64_tr_b16 v[226:227], v240 offset:28928\n"
"s_waitcnt lgkmcnt(4)\n"
"v_mfma_f32_16x16x32_bf16 v[192:195], v[232:235], v[48:51], v[192:195]\n"
"ds_read_b64_tr_b16 v[228:229], v241 offset:28672\n"
"v_mfma_f32_16x16x32_bf16 v[196:199], v[232:235], v[52:55], v[196:199]\n"
"ds_read_b64_tr_b16 v[230:231], v241 offset:28928\n"
"s_add_u32 s97, s96, 0x4c00\n"
"buffer_load_dwordx4 v250, s[20:23], s97 offen offset:1024 lds\n"
"s_waitcnt lgkmcnt(4)\n"
"v_mfma_f32_16x16x32_bf16 v[200:203], v[236:239], v[48:51], v[200:203]\n"
"ds_read_b64_tr_b16 v[232:233], v240 offset:30720\n"
"v_mfma_f32_16x16x32_bf16 v[204:207], v[236:239], v[52:55], v[204:207]\n"
"ds_read_b64_tr_b16 v[234:235], v240 offset:30976\n"
"s_barrier\n"
"s_waitcnt lgkmcnt(4)\n"
"v_mfma_f32_16x16x32_bf16 v[160:163], v[224:227], v[56:59], v[160:163]\n"
"ds_read_b64_tr_b16 v[236:237], v241 offset:30720\n"
"v_mfma_f32_16x16x32_bf16 v[164:167], v[224:227], v[60:63], v[164:167]\n"
"ds_read_b64_tr_b16 v[238:239], v241 offset:30976\n"
"s_waitcnt lgkmcnt(4)\n"
"v_mfma_f32_16x16x32_bf16 v[168:171], v[228:231], v[56:59], v[168:171]\n"
"ds_read_b64_tr_b16 v[224:225], v240 offset:32768\n"
"s_add_u32 m0, s49, 0x4000\n"
"s_add_u32 s43, s47, 0x180000\n"
"buffer_load_dwordx4 v249, s[8:11], s43 offen lds\n"
"v_mfma_f32_16x16x32_bf16 v[172:175], v[228:231], v[60:63], v[172:175]\n"
"ds_read_b64_tr_b16 v[226:227], v240 offset:33024\n"
"s_waitcnt lgkmcnt(4)\n"
"v_mfma_f32_16x16x32_bf16 v[192:195], v[232:235], v[56:59], v[192:195]\n"
"ds_read_b64_tr_b16 v[228:229], v241 offset:32768\n"
"v_mfma_f32_16x16x32_bf16 v[196:199], v[232:235], v[60:63], v[196:199]\n"
"ds_read_b64_tr_b16 v[230:231], v241 offset:33024\n"
"s_waitcnt lgkmcnt(4)\n"
"v_mfma_f32_16x16x32_bf16 v[200:203], v[236:239], v[56:59], v[200:203]\n"
"ds_read_b64_tr_b16 v[232:233], v240 offset:34816\n"
"v_mfma_f32_16x16x32_bf16 v[204:207], v[236:239], v[60:63], v[204:207]\n"
"ds_read_b64_tr_b16 v[234:235], v240 offset:35072\n"
"s_waitcnt lgkmcnt(4)\n"
"v_mfma_f32_16x16x32_bf16 v[160:163], v[224:227], v[64:67], v[160:163]\n"
"ds_read_b64_tr_b16 v[236:237], v241 offset:34816\n"
"v_mfma_f32_16x16x32_bf16 v[164:167], v[224:227], v[68:71], v[164:167]\n"
"ds_read_b64_tr_b16 v[238:239], v241 offset:35072\n"
"s_add_u32 m0, s88, 0x24000\n"
"s_mul_i32 s96, s58, 491520\n"
"s_add_u32 s96, s96, s84\n"
"s_add_u32 s96, s96, 0xa000\n"
"buffer_load_dwordx4 v250, s[20:23], s96 offen lds\n"
"s_waitcnt lgkmcnt(4)\n"
"v_mfma_f32_16x16x32_bf16 v[168:171], v[228:231], v[64:67], v[168:171]\n"
"ds_read_b64_tr_b16 v[224:225], v240 offset:36864\n"
"v_mfma_f32_16x16x32_bf16 v[172:175], v[228:231], v[68:71], v[172:175]\n"
"ds_read_b64_tr_b16 v[226:227], v240 offset:37120\n"
"s_waitcnt lgkmcnt(4)\n"
"v_mfma_f32_16x16x32_bf16 v[192:195], v[232:235], v[64:67], v[192:195]\n"
"ds_read_b64_tr_b16 v[228:229], v241 offset:36864\n"
"v_mfma_f32_16x16x32_bf16 v[196:199], v[232:235], v[68:71], v[196:199]\n"
"ds_read_b64_tr_b16 v[230:231], v241 offset:37120\n"
"s_add_u32 s97, s96, 0x4c00\n"
"buffer_load_dwordx4 v250, s[20:23], s97 offen offset:1024 lds\n"
"s_waitcnt lgkmcnt(4)\n"
"v_mfma_f32_16x16x32_bf16 v[200:203], v[236:239], v[64:67], v[200:203]\n"
"ds_read_b64_tr_b16 v[232:233], v240 offset:38912\n"
"v_mfma_f32_16x16x32_bf16 v[204:207], v[236:239], v[68:71], v[204:207]\n"
"ds_read_b64_tr_b16 v[234:235], v240 offset:39168\n"
"s_waitcnt vmcnt(13)\n"
"s_waitcnt vmcnt(5)\n"
"s_barrier\n"
"s_waitcnt lgkmcnt(4)\n"
"v_mfma_f32_16x16x32_bf16 v[160:163], v[224:227], v[72:75], v[160:163]\n"
"ds_read_b64_tr_b16 v[236:237], v241 offset:38912\n"
"v_mfma_f32_16x16x32_bf16 v[164:167], v[224:227], v[76:79], v[164:167]\n"
"ds_read_b64_tr_b16 v[238:239], v241 offset:39168\n"
"s_cmp_lg_u32 s32, 0\n"
"s_cbranch_scc1 .Lpub2\n"
"v_mov_b32 v184, s56\n"
"v_mov_b32 v185, s57\n"
"s_add_u32 s94, s52, 0x88e000\n"
"s_mov_b64 exec, 1\n"
"buffer_store_dwordx2 v[184:185], off, s[12:15], s94 sc1\n"
"s_mov_b64 exec, -1\n"
".Lpub2:\n"
"s_waitcnt lgkmcnt(4)\n"
"v_mfma_f32_16x16x32_bf16 v[168:171], v[228:231], v[72:75], v[168:171]\n"
"ds_read_b64_tr_b16 v[224:225], v240 offset:40960\n"
"v_mfma_f32_16x16x32_bf16 v[172:175], v[228:231], v[76:79], v[172:175]\n"
"ds_read_b64_tr_b16 v[226:227], v240 offset:41216\n"
"s_waitcnt lgkmcnt(4)\n"
"v_mfma_f32_16x16x32_bf16 v[192:195], v[232:235], v[72:75], v[192:195]\n"
"ds_read_b64_tr_b16 v[228:229], v241 offset:40960\n"
"v_mfma_f32_16x16x32_bf16 v[196:199], v[232:235], v[76:79], v[196:199]\n"
"ds_read_b64_tr_b16 v[230:231], v241 offset:41216\n"
"s_add_u32 m0, s49, 0x6000\n"
"s_add_u32 s43, s47, 0x1b0000\n"
"buffer_load_dwordx4 v249, s[8:11], s43 offen lds\n"
"s_waitcnt lgkmcnt(4)\n"
"v_mfma_f32_16x16x32_bf16 v[200:203], v[236:239], v[72:75], v[200:203]\n"
"ds_read_b64_tr_b16 v[232:233], v240 offset:43008\n"
"v_mfma_f32_16x16x32_bf16 v[204:207], v[236:239], v[76:79], v[204:207]\n"
"ds_read_b64_tr_b16 v[234:235], v240 offset:43264\n"
"s_waitcnt lgkmcnt(4)\n"
"v_mfma_f32_16x16x32_bf16 v[160:163], v[224:227], v[80:83], v[160:163]\n"
"ds_read_b64_tr_b16 v[236:237], v241 offset:43008\n"
"v_mfma_f32_16x16x32_bf16 v[164:167], v[224:227], v[84:87], v[164:167]\n"
"ds_read_b64_tr_b16 v[238:239], v241 offset:43264\n"
"s_waitcnt lgkmcnt(4)\n"
"v_mfma_f32_16x16x32_bf16 v[168:171], v[228:231], v[80:83], v[168:171]\n"
"ds_read_b64_tr_b16 v[224:225], v240 offset:45056\n"
"s_cmp_lg_u32 s32, 7\n"
"s_cbranch_scc1 .Lpi3\n"
"v_and_b32 v248, 0xf0, v253\n"
"s_lshl_b32 s95, s33, 11\n"
"v_lshlrev_b32 v248, 3, v248\n"
"s_add_u32 s95, s95, 0x882000\n"
"buffer_load_dwordx2 v[40:41], v248, s[12:15], s95 offen sc1\n"
".Lpi3:\n"
"v_mfma_f32_16x16x32_bf16 v[172:175], v[228:231], v[84:87], v[172:175]\n"
"ds_read_b64_tr_b16 v[226:227], v240 offset:45312\n"
"s_waitcnt lgkmcnt(4)\n"
"v_mfma_f32_16x16x32_bf16 v[192:195], v[232:235], v[80:83], v[192:195]\n"
"ds_read_b64_tr_b16 v[228:229], v241 offset:45056\n"
"v_mfma_f32_16x16x32_bf16 v[196:199], v[232:235], v[84:87], v[196:199]\n"
"ds_read_b64_tr_b16 v[230:231], v241 offset:45312\n"
"s_waitcnt lgkmcnt(4)\n"
"v_mfma_f32_16x16x32_bf16 v[200:203], v[236:239], v[80:83], v[200:203]\n"
"ds_read_b64_tr_b16 v[232:233], v240 offset:47104\n"
"v_mfma_f32_16x16x32_bf16 v[204:207], v[236:239], v[84:87], v[204:207]\n"
"ds_read_b64_tr_b16 v[234:235], v240 offset:47360\n"
"s_barrier\n"
"s_waitcnt lgkmcnt(4)\n"
"v_mfma_f32_16x16x32_bf16 v[160:163], v[224:227], v[88:91], v[160:163]\n"
"ds_read_b64_tr_b16 v[236:237], v241 offset:47104\n"
"v_mfma_f32_16x16x32_bf16 v[164:167], v[224:227], v[92:95], v[164:167]\n"
"ds_read_b64_tr_b16 v[238:239], v241 offset:47360\n"
"s_waitcnt lgkmcnt(4)\n"
"v_mfma_f32_16x16x32_bf16 v[168:171], v[228:231], v[88:91], v[168:171]\n"
"ds_read_b64_tr_b16 v[224:225], v240 offset:0\n"
"v_mfma_f32_16x16x32_bf16 v[172:175], v[228:231], v[92:95], v[172:175]\n"
"ds_read_b64_tr_b16 v[226:227], v240 offset:256\n"
"s_waitcnt lgkmcnt(4)\n"
"v_mfma_f32_16x16x32_bf16 v[192:195], v[232:235], v[88:91], v[192:195]\n"
"ds_read_b64_tr_b16 v[228:229], v241 offset:0\n"
"v_mfma_f32_16x16x32_bf16 v[196:199], v[232:235], v[92:95], v[196:199]\n"
"ds_read_b64_tr_b16 v[230:231], v241 offset:256\n"
"s_waitcnt lgkmcnt(4)\n"
"v_mfma_f32_16x16x32_bf16 v[200:203], v[236:239], v[88:91], v[200:203]\n"
"ds_read_b64_tr_b16 v[232:233], v240 offset:2048\n"
"v_mfma_f32_16x16x32_bf16 v[204:207], v[236:239], v[92:95], v[204:207]\n"
"ds_read_b64_tr_b16 v[234:235], v240 offset:2304\n"
"s_waitcnt lgkmcnt(4)\n"
"v_mfma_f32_16x16x32_bf16 v[160:163], v[224:227], v[96:99], v[160:163]\n"
"ds_read_b64_tr_b16 v[236:237], v241 offset:2048\n"
"v_mfma_f32_16x16x32_bf16 v[164:167], v[224:227], v[100:103], v[164:167]\n"
"ds_read_b64_tr_b16 v[238:239], v241 offset:2304\n"
"s_waitcnt lgkmcnt(4)\n"
"v_mfma_f32_16x16x32_bf16 v[168:171], v[228:231], v[96:99], v[168:171]\n"
"ds_read_b64_tr_b16 v[224:225], v240 offset:4096\n"
"v_mfma_f32_16x16x32_bf16 v[172:175], v[228:231], v[100:103], v[172:175]\n"
"ds_read_b64_tr_b16 v[226:227], v240 offset:4352\n"
"s_waitcnt lgkmcnt(4)\n"
"v_mfma_f32_16x16x32_bf16 v[192:195], v[232:235], v[96:99], v[192:195]\n"
"ds_read_b64_tr_b16 v[228:229], v241 offset:4096\n"
"v_mfma_f32_16x16x32_bf16 v[196:199], v[232:235], v[100:103], v[196:199]\n"
"ds_read_b64_tr_b16 v[230:231], v241 offset:4352\n"
"s_waitcnt lgkmcnt(4)\n"
"v_mfma_f32_16x16x32_bf16 v[200:203], v[236:239], v[96:99], v[200:203]\n"
"ds_read_b64_tr_b16 v[232:233], v240 offset:6144\n"
"v_mfma_f32_16x16x32_bf16 v[204:207], v[236:239], v[100:103], v[204:207]\n"
"ds_read_b64_tr_b16 v[234:235], v240 offset:6400\n"
"s_cmp_lg_u32 s32, 7\n"
"s_cbranch_scc1 .Lok4\n"
"s_waitcnt vmcnt(0)\n"
"v_cmp_ne_u64 vcc, s[56:57], v[40:41]\n"
"s_nop 1\n"
"s_cmp_eq_u64 vcc, 0\n"
"s_cbranch_scc1 .Lok4\n"
"s_mov_b32 s94, 0\n"
"s_lshl_b32 s95, s33, 11\n"
"s_add_u32 s95, s95, 0x882000\n"
".Lagain5:\n"
"s_sleep 1\n"
"buffer_load_dwordx2 v[40:41], v248, s[12:15], s95 offen sc1\n"
"s_add_u32 s94, s94, 1\n"
"s_waitcnt vmcnt(0)\n"
"v_cmp_ne_u64 vcc, s[56:57], v[40:41]\n"
"s_nop 1\n"
"s_cmp_eq_u64 vcc, 0\n"
"s_cbranch_scc1 .Lok4\n"
"s_cmp_lt_u32 s94, 0x8000\n"
"s_cbranch_scc1 .Lagain5\n"
".Lok4:\n"
"s_barrier\n"
"s_waitcnt lgkmcnt(4)\n"
"v_mfma_f32_16x16x32_bf16 v[160:163], v[224:227], v[104:107], v[160:163]\n"
"ds_read_b64_tr_b16 v[236:237], v241 offset:6144\n"
"v_mfma_f32_16x16x32_bf16 v[164:167], v[224:227], v[108:111], v[164:167]\n"
"ds_read_b64_tr_b16 v[238:239], v241 offset:6400\n"
"s_lshl_b32 s98, s0, 12\n"
"s_add_u32 s98, s98, s89\n"
"buffer_load_dwordx4 v[0:3], v253, s[12:15], s98 offen sc1\n"
"s_waitcnt lgkmcnt(4)\n"
"v_mfma_f32_16x16x32_bf16 v[168:171], v[228:231], v[104:107], v[168:171]\n"
"ds_read_b64_tr_b16 v[224:225], v240 offset:8192\n"
"v_mfma_f32_16x16x32_bf16 v[172:175], v[228:231], v[108:111], v[172:175]\n"
"ds_read_b64_tr_b16 v[226:227], v240 offset:8448\n"
"s_waitcnt lgkmcnt(4)\n"
"v_mfma_f32_16x16x32_bf16 v[192:195], v[232:235], v[104:107], v[192:195]\n"
"ds_read_b64_tr_b16 v[228:229], v241 offset:8192\n"
"v_mfma_f32_16x16x32_bf16 v[196:199], v[232:235], v[108:111], v[196:199]\n"
"ds_read_b64_tr_b16 v[230:231], v241 offset:8448\n"
"s_add_u32 s98, s98, 0x11000\n"
"buffer_load_dwordx4 v[4:7], v253, s[12:15], s98 offen sc1\n"
"s_waitcnt lgkmcnt(4)\n"
"v_mfma_f32_16x16x32_bf16 v[200:203], v[236:239], v[104:107], v[200:203]\n"
"ds_read_b64_tr_b16 v[232:233], v240 offset:10240\n"
"v_mfma_f32_16x16x32_bf16 v[204:207], v[236:239], v[108:111], v[204:207]\n"
"ds_read_b64_tr_b16 v[234:235], v240 offset:10496\n"
"s_waitcnt lgkmcnt(4)\n"
"v_mfma_f32_16x16x32_bf16 v[160:163], v[224:227], v[112:115], v[160:163]\n"
"ds_read_b64_tr_b16 v[236:237], v241 offset:10240\n"
"v_mfma_f32_16x16x32_bf16 v[164:167], v[224:227], v[116:119], v[164:167]\n"
"ds_read_b64_tr_b16 v[238:239], v241 offset:10496\n"
"s_lshl_b32 s98, s1, 12\n"
"s_add_u32 s98, s98, s89\n"
"buffer_load_dwordx4 v[8:11], v253, s[12:15], s98 offen sc1\n"
"s_waitcnt lgkmcnt(4)\n"
"v_mfma_f32_16x16x32_bf16 v[168:171], v[228:231], v[112:115], v[168:171]\n"
"ds_read_b64_tr_b16 v[224:225], v240 offset:12288\n"
"v_mfma_f32_16x16x32_bf16 v[172:175], v[228:231], v[116:119], v[172:175]\n"
"ds_read_b64_tr_b16 v[226:227], v240 offset:12544\n"
"s_waitcnt lgkmcnt(4)\n"
"v_mfma_f32_16x16x32_bf16 v[192:195], v[232:235], v[112:115], v[192:195]\n"
"ds_read_b64_tr_b16 v[228:229], v241 offset:12288\n"
"v_mfma_f32_16x16x32_bf16 v[196:199], v[232:235], v[116:119], v[196:199]\n"
"ds_read_b64_tr_b16 v[230:231], v241 offset:12544\n"
"s_add_u32 s98, s98, 0x11000\n"
"buffer_load_dwordx4 v[12:15], v253, s[12:15], s98 offen sc1\n"
"s_waitcnt lgkmcnt(4)\n"
"v_mfma_f32_16x16x32_bf16 v[200:203], v[236:239], v[112:115], v[200:203]\n"
"ds_read_b64_tr_b16 v[232:233], v240 offset:14336\n"
"v_mfma_f32_16x16x32_bf16 v[204:207], v[236:239], v[116:119], v[204:207]\n"
"ds_read_b64_tr_b16 v[234:235], v240 offset:14592\n"
"s_waitcnt vmcnt(7)\n"
"s_barrier\n"
"s_waitcnt lgkmcnt(4)\n"
"v_mfma_f32_16x16x32_bf16 v[160:163], v[224:227], v[120:123], v[160:163]\n"
"ds_read_b64_tr_b16 v[236:237], v241 offset:14336\n"
"v_mfma_f32_16x16x32_bf16 v[164:167], v[224:227], v[124:127], v[164:167]\n"
"ds_read_b64_tr_b16 v[238:239], v241 offset:14592\n"
"s_lshl_b32 s98, s58, 12\n"
"s_add_u32 s98, s98, s89\n"
"buffer_load_dwordx4 v[16:19], v253, s[12:15], s98 offen sc1\n"
"s_waitcnt lgkmcnt(4)\n"
"v_mfma_f32_16x16x32_bf16 v[168:171], v[228:231], v[120:123], v[168:171]\n"
"ds_read_b64_tr_b16 v[224:225], v240 offset:16384\n"
"v_mfma_f32_16x16x32_bf16 v[172:175], v[228:231], v[124:127], v[172:175]\n"
"ds_read_b64_tr_b16 v[226:227], v240 offset:16640\n"
"s_waitcnt lgkmcnt(4)\n"
"v_mfma_f32_16x16x32_bf16 v[192:195], v[232:235], v[120:123], v[192:195]\n"
"ds_read_b64_tr_b16 v[228:229], v241 offset:16384\n"
"v_mfma_f32_16x16x32_bf16 v[196:199], v[232:235], v[124:127], v[196:199]\n"
"ds_read_b64_tr_b16 v[230:231], v241 offset:16640\n"
"s_add_u32 s98, s98, 0x11000\n"
"buffer_load_dwordx4 v[20:23], v253, s[12:15], s98 offen sc1\n"
"s_waitcnt lgkmcnt(4)\n"
"v_mfma_f32_16x16x32_bf16 v[200:203], v[236:239], v[120:123], v[200:203]\n"
"ds_read_b64_tr_b16 v[232:233], v240 offset:18432\n"
"v_mfma_f32_16x16x32_bf16 v[204:207], v[236:239], v[124:127], v[204:207]\n"
"ds_read_b64_tr_b16 v[234:235], v240 offset:18688\n"
"s_waitcnt lgkmcnt(4)\n"
"v_mfma_f32_16x16x32_bf16 v[160:163], v[224:227], v[128:131], v[160:163]\n"
"ds_read_b64_tr_b16 v[236:237], v241 offset:18432\n"
"v_mfma_f32_16x16x32_bf16 v[164:167], v[224:227], v[132:135], v[164:167]\n"
"ds_read_b64_tr_b16 v[238:239], v241 offset:18688\n"
"s_lshl_b32 s98, s59, 12\n"
"s_add_u32 s98, s98, s89\n"
"buffer_load_dwordx4 v[24:27], v253, s[12:15], s98 offen sc1\n"
"s_waitcnt lgkmcnt(4)\n"
"v_mfma_f32_16x16x32_bf16 v[168:171], v[228:231], v[128:131], v[168:171]\n"
"ds_read_b64_tr_b16 v[224:225], v240 offset:20480\n"
"v_mfma_f32_16x16x32_bf16 v[172:175], v[228:231], v[132:135], v[172:175]\n"
"ds_read_b64_tr_b16 v[226:227], v240 offset:20736\n"
"s_waitcnt lgkmcnt(4)\n"
"v_mfma_f32_16x16x32_bf16 v[192:195], v[232:235], v[128:131], v[192:195]\n"
"ds_read_b64_tr_b16 v[228:229], v241 offset:20480\n"
"v_mfma_f32_16x16x32_bf16 v[196:199], v[232:235], v[132:135], v[196:199]\n"
"ds_read_b64_tr_b16 v[230:231], v241 offset:20736\n"
"s_add_u32 s98, s98, 0x11000\n"
"buffer_load_dwordx4 v[28:31], v253, s[12:15], s98 offen sc1\n"
"s_waitcnt lgkmcnt(4)\n"
"v_mfma_f32_16x16x32_bf16 v[200:203], v[236:239], v[128:131], v[200:203]\n"
"ds_read_b64_tr_b16 v[232:233], v240 offset:22528\n"
"v_mfma_f32_16x16x32_bf16 v[204:207], v[236:239], v[132:135], v[204:207]\n"
"ds_read_b64_tr_b16 v[234:235], v240 offset:22784\n"
"s_waitcnt vmcnt(8)\n"
"s_barrier\n"
"s_waitcnt lgkmcnt(4)\n"
"v_mfma_f32_16x16x32_bf16 v[160:163], v[224:227], v[136:139], v[160:163]\n"
"ds_read_b64_tr_b16 v[236:237], v241 offset:22528\n"
"v_mfma_f32_16x16x32_bf16 v[164:167], v[224:227], v[140:143], v[164:167]\n"
"ds_read_b64_tr_b16 v[238:239], v241 offset:22784\n"
"s_waitcnt lgkmcnt(4)\n"
"v_mfma_f32_16x16x32_bf16 v[168:171], v[228:231], v[136:139], v[168:171]\n"
"ds_read_b64_tr_b16 v[224:225], v240 offset:24576\n"
"v_mfma_f32_16x16x32_bf16 v[172:175], v[228:231], v[140:143], v[172:175]\n"
"ds_read_b64_tr_b16 v[226:227], v240 offset:24832\n"
"s_waitcnt lgkmcnt(4)\n"
"v_mfma_f32_16x16x32_bf16 v[192:195], v[232:235], v[136:139], v[192:195]\n"
"ds_read_b64_tr_b16 v[228:229], v241 offset:24576\n"
"v_mfma_f32_16x16x32_bf16 v[196:199], v[232:235], v[140:143], v[196:199]\n"
"ds_read_b64_tr_b16 v[230:231], v241 offset:24832\n"
"s_waitcnt lgkmcnt(4)\n"
"v_mfma_f32_16x16x32_bf16 v[200:203], v[236:239], v[136:139], v[200:203]\n"
"ds_read_b64_tr_b16 v[232:233], v240 offset:26624\n"
"v_mfma_f32_16x16x32_bf16 v[204:207], v[236:239], v[140:143], v[204:207]\n"
"ds_read_b64_tr_b16 v[234:235], v240 offset:26880\n"
"s_waitcnt lgkmcnt(4)\n"
"v_mfma_f32_16x16x32_bf16 v[160:163], v[224:227], v[144:147], v[160:163]\n"
"ds_read_b64_tr_b16 v[236:237], v241 offset:26624\n"
"v_mbcnt_lo_u32_b32 v130, -1, 0\n"
"v_mbcnt_hi_u32_b32 v130, -1, v130\n"
"v_mfma_f32_16x16x32_bf16 v[164:167], v[224:227], v[148:151], v[164:167]\n"
"ds_read_b64_tr_b16 v[238:239], v241 offset:26880\n"
"s_and_b32 s35, s32, 3\n"
"s_waitcnt lgkmcnt(4)\n"
"v_mfma_f32_16x16x32_bf16 v[168:171], v[228:231], v[144:147], v[168:171]\n"
"ds_read_b64_tr_b16 v[224:225], v240 offset:28672\n"
"s_cmp_lg_u32 s32, 7\n"
"s_cbranch_scc1 .Lpi6\n"
"v_and_b32 v248, 0xf0, v253\n"
"s_lshl_b32 s95, s33, 11\n"
"v_lshlrev_b32 v248, 3, v248\n"
"s_add_u32 s95, s95, 0x88e000\n"
"buffer_load_dwordx2 v[72:73], v248, s[12:15], s95 offen sc1\n"
".Lpi6:\n"
"s_lshl_b32 s35, s35, 12\n"
"v_bfe_u32 v128, v130, 2, 3\n"
"v_mfma_f32_16x16x32_bf16 v[172:175], v[228:231], v[148:151], v[172:175]\n"
"ds_read_b64_tr_b16 v[226:227], v240 offset:28928\n"
"v_lshrrev_b32 v129, 1, v128\n"
"v_lshlrev_b32 v128, 7, v128\n"
"s_waitcnt lgkmcnt(4)\n"
"v_mfma_f32_16x16x32_bf16 v[192:195], v[232:235], v[144:147], v[192:195]\n"
"ds_read_b64_tr_b16 v[228:229], v241 offset:28672\n"
"v_lshl_add_u32 v128, v129, 5, v128\n"
"v_and_b32 v129, 3, v130\n"
"v_lshl_add_u32 v128, v129, 3, v128\n"
"v_mfma_f32_16x16x32_bf16 v[196:199], v[232:235], v[148:151], v[196:199]\n"
"ds_read_b64_tr_b16 v[230:231], v241 offset:28928\n"
"v_lshrrev_b32 v129, 5, v130\n"
"v_mul_u32_u24 v129, 2048, v129\n"
"s_waitcnt lgkmcnt(4)\n"
"v_mfma_f32_16x16x32_bf16 v[200:203], v[236:239], v[144:147], v[200:203]\n"
"ds_read_b64_tr_b16 v[232:233], v240 offset:30720\n"
"v_add_u32 v128, v128, v129\n"
"v_add_u32 v136, s35, v128\n"
"v_mfma_f32_16x16x32_bf16 v[204:207], v[236:239], v[148:151], v[204:207]\n"
"ds_read_b64_tr_b16 v[234:235], v240 offset:30976\n"
"v_xor_b32 v137, 32, v136\n"
"v_xor_b32 v138, 64, v136\n"
"v_xor_b32 v139, 96, v136\n"
"s_barrier\n"
"s_waitcnt lgkmcnt(4)\n"
"v_mfma_f32_16x16x32_bf16 v[160:163], v[224:227], v[152:155], v[160:163]\n"
"ds_read_b64_tr_b16 v[236:237], v241 offset:30720\n"
"v_mfma_f32_16x16x32_bf16 v[164:167], v[224:227], v[156:159], v[164:167]\n"
"ds_read_b64_tr_b16 v[238:239], v241 offset:30976\n"
"s_waitcnt lgkmcnt(4)\n"
"v_mfma_f32_16x16x32_bf16 v[168:171], v[228:231], v[152:155], v[168:171]\n"
"v_mfma_f32_16x16x32_bf16 v[172:175], v[228:231], v[156:159], v[172:175]\n"
"s_waitcnt lgkmcnt(2)\n"
"v_mfma_f32_16x16x32_bf16 v[192:195], v[232:235], v[152:155], v[192:195]\n"
"v_mfma_f32_16x16x32_bf16 v[196:199], v[232:235], v[156:159], v[196:199]\n"
"s_waitcnt lgkmcnt(0)\n"
"v_mfma_f32_16x16x32_bf16 v[200:203], v[236:239], v[152:155], v[200:203]\n"
"v_mfma_f32_16x16x32_bf16 v[204:207], v[236:239], v[156:159], v[204:207]\n"
"s_cmp_lg_u32 s32, 7\n"
"s_cbranch_scc1 .Lok7\n"
"s_waitcnt vmcnt(0)\n"
"v_cmp_ne_u64 vcc, s[56:57], v[72:73]\n"
"s_nop 1\n"
"s_cmp_eq_u64 vcc, 0\n"
"s_cbranch_scc1 .Lok7\n"
"s_mov_b32 s94, 0\n"
"s_lshl_b32 s95, s33, 11\n"
"s_add_u32 s95, s95, 0x88e000\n"
".Lagain8:\n"
"s_sleep 1\n"
"buffer_load_dwordx2 v[72:73], v248, s[12:15], s95 offen sc1\n"
"s_add_u32 s94, s94, 1\n"
"s_waitcnt vmcnt(0)\n"
"v_cmp_ne_u64 vcc, s[56:57], v[72:73]\n"
"s_nop 1\n"
"s_cmp_eq_u64 vcc, 0\n"
"s_cbranch_scc1 .Lok7\n"
"s_cmp_lt_u32 s94, 0x8000\n"
"s_cbranch_scc1 .Lagain8\n"
".Lok7:\n"
"s_barrier\n"
"v_add_u32 v136, 0x10000, v136\n"
"v_add_u32 v137, 0x10000, v137\n"
"v_add_u32 v138, 0x10000, v138\n"
"v_add_u32 v139, 0x10000, v139\n"
"ds_read_b64_tr_b16 v[224:225], v136 offset:16384\n"
"ds_read_b64_tr_b16 v[226:227], v136 offset:17408\n"
"ds_read_b64_tr_b16 v[228:229], v137 offset:16384\n"
"ds_read_b64_tr_b16 v[230:231], v137 offset:17408\n"
"s_waitcnt lgkmcnt(2)\n"
"s_waitcnt vmcnt(7)\n"
"v_mfma_f32_16x16x32_bf16 v[176:179], v[224:227], v[0:3], 0\n"
"ds_read_b64_tr_b16 v[232:233], v138 offset:16384\n"
"v_mul_f32 v244, 0xbfb8aa3b, v160\n"
"v_mul_f32 v245, 0xbfb8aa3b, v161\n"
"v_mul_f32 v246, 0xbfb8aa3b, v162\n"
"v_mul_f32 v247, 0xbfb8aa3b, v163\n"
"v_exp_f32 v244, v244\n"
"v_mul_f32 v192, v160, v192\n"
"s_waitcnt vmcnt(6)\n"
"v_mfma_f32_16x16x32_bf16 v[208:211], v[224:227], v[4:7], 0\n"
"ds_read_b64_tr_b16 v[234:235], v138 offset:17408\n"
"s_lshl_b32 s98, s0, 12\n"
"s_add_u32 s98, s98, s89\n"
"s_add_u32 s98, s98, 0x4000\n"
"buffer_load_dwordx4 v[32:35], v253, s[12:15], s98 offen sc1\n"
"v_exp_f32 v245, v245\n"
"v_mul_f32 v193, v161, v193\n"
"v_exp_f32 v246, v246\n"
"v_mul_f32 v194, v162, v194\n"
"v_exp_f32 v247, v247\n"
"v_mul_f32 v195, v163, v195\n"
"s_waitcnt lgkmcnt(2)\n"
"v_mfma_f32_16x16x32_bf16 v[180:183], v[228:231], v[0:3], 0\n"
"ds_read_b64_tr_b16 v[236:237], v139 offset:16384\n"
"v_add_f32 v244, 1.0, v244\n"
"v_add_f32 v245, 1.0, v245\n"
"v_add_f32 v246, 1.0, v246\n"
"v_add_f32 v247, 1.0, v247\n"
"v_rcp_f32 v244, v244\n"
"v_rcp_f32 v245, v245\n"
"v_rcp_f32 v246, v246\n"
"v_mfma_f32_16x16x32_bf16 v[212:215], v[228:231], v[4:7], 0\n"
"ds_read_b64_tr_b16 v[238:239], v139 offset:17408\n"
"v_rcp_f32 v247, v247\n"
"v_mul_f32 v192, v244, v192\n"
"v_mul_f32 v193, v245, v193\n"
"v_mul_f32 v194, v246, v194\n"
"v_mul_f32 v195, v247, v195\n"
"v_mul_f32 v244, 0xbfb8aa3b, v168\n"
"v_mul_f32 v245, 0xbfb8aa3b, v169\n"
"s_waitcnt lgkmcnt(2)\n"
"v_mfma_f32_16x16x32_bf16 v[184:187], v[232:235], v[0:3], 0\n"
"ds_read_b64_tr_b16 v[224:225], v136 offset:0\n"
"v_mul_f32 v246, 0xbfb8aa3b, v170\n"
"v_mul_f32 v247, 0xbfb8aa3b, v171\n"
"v_exp_f32 v244, v244\n"
"v_mul_f32 v200, v168, v200\n"
"v_exp_f32 v245, v245\n"
"v_mul_f32 v201, v169, v201\n"
"v_exp_f32 v246, v246\n"
"v_mfma_f32_16x16x32_bf16 v[216:219], v[232:235], v[4:7], 0\n"
"ds_read_b64_tr_b16 v[226:227], v136 offset:1024\n"
"s_add_u32 s98, s98, 0x11000\n"
"buffer_load_dwordx4 v[36:39], v253, s[12:15], s98 offen sc1\n"
"v_mul_f32 v202, v170, v202\n"
"v_exp_f32 v247, v247\n"
"v_mul_f32 v203, v171, v203\n"
"v_add_f32 v244, 1.0, v244\n"
"v_add_f32 v245, 1.0, v245\n"
"v_add_f32 v246, 1.0, v246\n"
"s_waitcnt lgkmcnt(2)\n"
"v_mfma_f32_16x16x32_bf16 v[188:191], v[236:239], v[0:3], 0\n"
"ds_read_b64_tr_b16 v[228:229], v137 offset:0\n"
"v_add_f32 v247, 1.0, v247\n"
"v_rcp_f32 v244, v244\n"
"v_rcp_f32 v245, v245\n"
"v_rcp_f32 v246, v246\n"
"v_rcp_f32 v247, v247\n"
"v_mul_f32 v200, v244, v200\n"
"v_mul_f32 v201, v245, v201\n"
"v_mfma_f32_16x16x32_bf16 v[220:223], v[236:239], v[4:7], 0\n"
"ds_read_b64_tr_b16 v[230:231], v137 offset:1024\n"
"v_mul_f32 v202, v246, v202\n"
"v_mul_f32 v203, v247, v203\n"
"v_cvt_pk_bf16_f32 v160, v192, v193\n"
"v_cvt_pk_bf16_f32 v161, v194, v195\n"
"v_cvt_pk_bf16_f32 v162, v200, v201\n"
"v_cvt_pk_bf16_f32 v163, v202, v203\n"
"s_add_u32 s35, s51, 0xd000\n"
"buffer_store_dwordx4 v[160:163], v253, s[12:15], s35 offen sc1\n"
"s_barrier\n"
"s_waitcnt lgkmcnt(2)\n"
"s_waitcnt vmcnt(8)\n"
"v_mfma_f32_16x16x32_bf16 v[176:179], v[224:227], v[8:11], v[176:179]\n"
"ds_read_b64_tr_b16 v[232:233], v138 offset:0\n"
"s_lshl_b32 s98, s1, 12\n"
"s_add_u32 s98, s98, s89\n"
"s_add_u32 s98, s98, 0x4000\n"
"buffer_load_dwordx4 v[40:43], v253, s[12:15], s98 offen sc1\n"
"v_mul_f32 v244, 0xbfb8aa3b, v164\n"
"v_mul_f32 v245, 0xbfb8aa3b, v165\n"
"v_mul_f32 v246, 0xbfb8aa3b, v166\n"
"v_mul_f32 v247, 0xbfb8aa3b, v167\n"
"v_exp_f32 v244, v244\n"
"v_mul_f32 v196, v164, v196\n"
"s_waitcnt vmcnt(8)\n"
"v_mfma_f32_16x16x32_bf16 v[208:211], v[224:227], v[12:15], v[208:211]\n"
"ds_read_b64_tr_b16 v[234:235], v138 offset:1024\n"
"v_exp_f32 v245, v245\n"
"v_mul_f32 v197, v165, v197\n"
"v_exp_f32 v246, v246\n"
"v_mul_f32 v198, v166, v198\n"
"v_exp_f32 v247, v247\n"
"v_mul_f32 v199, v167, v199\n"
"s_waitcnt lgkmcnt(2)\n"
"v_mfma_f32_16x16x32_bf16 v[180:183], v[228:231], v[8:11], v[180:183]\n"
"ds_read_b64_tr_b16 v[236:237], v139 offset:0\n"
"s_add_u32 s98, s98, 0x11000\n"
"buffer_load_dwordx4 v[44:47], v253, s[12:15], s98 offen sc1\n"
"v_add_f32 v244, 1.0, v244\n"
"v_add_f32 v245, 1.0, v245\n"
"v_add_f32 v246, 1.0, v246\n"
"v_add_f32 v247, 1.0, v247\n"
"v_rcp_f32 v244, v244\n"
"v_rcp_f32 v245, v245\n"
"v_rcp_f32 v246, v246\n"
"v_mfma_f32_16x16x32_bf16 v[212:215], v[228:231], v[12:15], v[212:215]\n"
"ds_read_b64_tr_b16 v[238:239], v139 offset:1024\n"
"s_add_u32 m0, s88, 0x0\n"
"s_mul_i32 s96, s59, 491520\n"
"s_add_u32 s96, s96, s84\n"
"s_add_u32 s96, s96, 0xa000\n"
"buffer_load_dwordx4 v250, s[20:23], s96 offen lds\n"
"v_rcp_f32 v247, v247\n"
"v_mul_f32 v196, v244, v196\n"
"v_mul_f32 v197, v245, v197\n"
"v_mul_f32 v198, v246, v198\n"
"v_mul_f32 v199, v247, v199\n"
"v_mul_f32 v244, 0xbfb8aa3b, v172\n"
"v_mul_f32 v245, 0xbfb8aa3b, v173\n"
"s_waitcnt lgkmcnt(2)\n"
"v_mfma_f32_16x16x32_bf16 v[184:187], v[232:235], v[8:11], v[184:187]\n"
"v_add_u32 v136, 0xffff0000, v136\n"
"v_add_u32 v137, 0xffff0000, v137\n"
"v_add_u32 v138, 0xffff0000, v138\n"
"v_add_u32 v139, 0xffff0000, v139\n"
"ds_read_b64_tr_b16 v[224:225], v136 offset:49152\n"
"v_mul_f32 v246, 0xbfb8aa3b, v174\n"
"v_mul_f32 v247, 0xbfb8aa3b, v175\n"
"v_exp_f32 v244, v244\n"
"v_mul_f32 v204, v172, v204\n"
"v_exp_f32 v245, v245\n"
"v_mul_f32 v205, v173, v205\n"
"v_exp_f32 v246, v246\n"
"v_mfma_f32_16x16x32_bf16 v[216:219], v[232:235], v[12:15], v[216:219]\n"
"ds_read_b64_tr_b16 v[226:227], v136 offset:50176\n"
"s_add_u32 s97, s96, 0x4c00\n"
"buffer_load_dwordx4 v250, s[20:23], s97 offen offset:1024 lds\n"
"v_mul_f32 v206, v174, v206\n"
"v_exp_f32 v247, v247\n"
"v_mul_f32 v207, v175, v207\n"
"v_add_f32 v244, 1.0, v244\n"
"v_add_f32 v245, 1.0, v245\n"
"v_add_f32 v246, 1.0, v246\n"
"s_waitcnt lgkmcnt(2)\n"
"v_mfma_f32_16x16x32_bf16 v[188:191], v[236:239], v[8:11], v[188:191]\n"
"ds_read_b64_tr_b16 v[228:229], v137 offset:49152\n"
"s_cmp_lg_u32 s32, 7\n"
"s_cbranch_scc1 .Lpi9\n"
"v_and_b32 v248, 0xf0, v253\n"
"s_lshl_b32 s95, s33, 11\n"
"v_lshlrev_b32 v248, 3, v248\n"
"s_add_u32 s95, s95, 0x88a000\n"
"buffer_load_dwordx2 v[140:141], v248, s[12:15], s95 offen sc1\n"
".Lpi9:\n"
"v_add_f32 v247, 1.0, v247\n"
"v_rcp_f32 v244, v244\n"
"v_rcp_f32 v245, v245\n"
"v_rcp_f32 v246, v246\n"
"v_rcp_f32 v247, v247\n"
"v_mul_f32 v204, v244, v204\n"
"v_mul_f32 v205, v245, v205\n"
"v_mfma_f32_16x16x32_bf16 v[220:223], v[236:239], v[12:15], v[220:223]\n"
"ds_read_b64_tr_b16 v[230:231], v137 offset:50176\n"
"v_mul_f32 v206, v246, v206\n"
"v_mul_f32 v207, v247, v207\n"
"v_cvt_pk_bf16_f32 v164, v196, v197\n"
"v_cvt_pk_bf16_f32 v165, v198, v199\n"
"v_cvt_pk_bf16_f32 v166, v204, v205\n"
"v_cvt_pk_bf16_f32 v167, v206, v207\n"
"s_add_u32 s35, s51, 0x1e000\n"
"buffer_store_dwordx4 v[164:167], v253, s[12:15], s35 offen sc1\n"
"s_barrier\n"
"s_waitcnt lgkmcnt(2)\n"
"s_waitcnt vmcnt(11)\n"
"v_mfma_f32_16x16x32_bf16 v[176:179], v[224:227], v[16:19], v[176:179]\n"
"ds_read_b64_tr_b16 v[232:233], v138 offset:49152\n"
"s_waitcnt vmcnt(10)\n"
"v_mfma_f32_16x16x32_bf16 v[208:211], v[224:227], v[20:23], v[208:211]\n"
"ds_read_b64_tr_b16 v[234:235], v138 offset:50176\n"
"s_lshl_b32 s98, s58, 12\n"
"s_add_u32 s98, s98, s89\n"
"s_add_u32 s98, s98, 0x4000\n"
"buffer_load_dwordx4 v[48:51], v253, s[12:15], s98 offen sc1\n"
"s_waitcnt lgkmcnt(2)\n"
"v_mfma_f32_16x16x32_bf16 v[180:183], v[228:231], v[16:19], v[180:183]\n"
"ds_read_b64_tr_b16 v[236:237], v139 offset:49152\n"
"v_mfma_f32_16x16x32_bf16 v[212:215], v[228:231], v[20:23], v[212:215]\n"
"ds_read_b64_tr_b16 v[238:239], v139 offset:50176\n"
"s_waitcnt lgkmcnt(2)\n"
"v_mfma_f32_16x16x32_bf16 v[184:187], v[232:235], v[16:19], v[184:187]\n"
"v_add_u32 v136, 0x10000, v136\n"
"v_add_u32 v137, 0x10000, v137\n"
"v_add_u32 v138, 0x10000, v138\n"
"v_add_u32 v139, 0x10000, v139\n"
"ds_read_b64_tr_b16 v[224:225], v136 offset:32768\n"
"v_mfma_f32_16x16x32_bf16 v[216:219], v[232:235], v[20:23], v[216:219]\n"
"ds_read_b64_tr_b16 v[226:227], v136 offset:33792\n"
"s_add_u32 s98, s98, 0x11000\n"
"buffer_load_dwordx4 v[52:55], v253, s[12:15], s98 offen sc1\n"
"s_waitcnt lgkmcnt(2)\n"
"v_mfma_f32_16x16x32_bf16 v[188:191], v[236:239], v[16:19], v[188:191]\n"
"ds_read_b64_tr_b16 v[228:229], v137 offset:32768\n"
"v_mfma_f32_16x16x32_bf16 v[220:223], v[236:239], v[20:23], v[220:223]\n"
"ds_read_b64_tr_b16 v[230:231], v137 offset:33792\n"
"s_cmp_lg_u32 s32, 7\n"
"s_cbranch_scc1 .Lok10\n"
"s_waitcnt vmcnt(3)\n"
"v_cmp_ne_u64 vcc, s[56:57], v[140:141]\n"
"s_nop 1\n"
"s_cmp_eq_u64 vcc, 0\n"
"s_cbranch_scc1 .Lok10\n"
"s_mov_b32 s94, 0\n"
"s_lshl_b32 s95, s33, 11\n"
"s_add_u32 s95, s95, 0x88a000\n"
".Lagain11:\n"
"s_sleep 1\n"
"buffer_load_dwordx2 v[140:141], v248, s[12:15], s95 offen sc1\n"
"s_add_u32 s94, s94, 1\n"
"s_waitcnt vmcnt(0)\n"
"v_cmp_ne_u64 vcc, s[56:57], v[140:141]\n"
"s_nop 1\n"
"s_cmp_eq_u64 vcc, 0\n"
"s_cbranch_scc1 .Lok10\n"
"s_cmp_lt_u32 s94, 0x8000\n"
"s_cbranch_scc1 .Lagain11\n"
".Lok10:\n"
"s_barrier\n"
"s_waitcnt lgkmcnt(2)\n"
"s_waitcnt vmcnt(11)\n"
"v_mfma_f32_16x16x32_bf16 v[176:179], v[224:227], v[24:27], v[176:179]\n"
"ds_read_b64_tr_b16 v[232:233], v138 offset:32768\n"
"s_add_u32 m0, s88, 0x4000\n"
"s_mul_i32 s96, s0, 204800\n"
"s_add_u32 s96, s96, s85\n"
"buffer_load_dwordx4 v251, s[16:19], s96 offen lds\n"
"s_waitcnt vmcnt(11)\n"
"v_mfma_f32_16x16x32_bf16 v[208:211], v[224:227], v[28:31], v[208:211]\n"
"ds_read_b64_tr_b16 v[234:235], v138 offset:33792\n"
"s_waitcnt lgkmcnt(2)\n"
"v_mfma_f32_16x16x32_bf16 v[180:183], v[228:231], v[24:27], v[180:183]\n"
"ds_read_b64_tr_b16 v[236:237], v139 offset:32768\n"
"s_add_u32 s97, s96, 0x1000\n"
"buffer_load_dwordx4 v251, s[16:19], s97 offen offset:1024 lds\n"
"v_mfma_f32_16x16x32_bf16 v[212:215], v[228:231], v[28:31], v[212:215]\n"
"ds_read_b64_tr_b16 v[238:239], v139 offset:33792\n"
"s_waitcnt lgkmcnt(2)\n"
"v_mfma_f32_16x16x32_bf16 v[184:187], v[232:235], v[24:27], v[184:187]\n"
"ds_read_b64_tr_b16 v[224:225], v136 offset:49152\n"
"s_lshl_b32 s98, s59, 12\n"
"s_add_u32 s98, s98, s89\n"
"s_add_u32 s98, s98, 0x4000\n"
"buffer_load_dwordx4 v[56:59], v253, s[12:15], s98 offen sc1\n"
"v_mfma_f32_16x16x32_bf16 v[216:219], v[232:235], v[28:31], v[216:219]\n"
"ds_read_b64_tr_b16 v[226:227], v136 offset:50176\n"
"s_waitcnt lgkmcnt(2)\n"
"v_mfma_f32_16x16x32_bf16 v[188:191], v[236:239], v[24:27], v[188:191]\n"
"ds_read_b64_tr_b16 v[228:229], v137 offset:49152\n"
"s_add_u32 s98, s98, 0x11000\n"
"buffer_load_dwordx4 v[60:63], v253, s[12:15], s98 offen sc1\n"
"v_mfma_f32_16x16x32_bf16 v[220:223], v[236:239], v[28:31], v[220:223]\n"
"ds_read_b64_tr_b16 v[230:231], v137 offset:50176\n"
"s_waitcnt vmcnt(6)\n"
"s_barrier\n"
"s_waitcnt lgkmcnt(2)\n"
"v_mfma_f32_16x16x32_bf16 v[176:179], v[224:227], v[32:35], v[176:179]\n"
"ds_read_b64_tr_b16 v[232:233], v138 offset:49152\n"
"s_cmp_lg_u32 s32, 0\n"
"s_cbranch_scc1 .Lpub12\n"
"v_mov_b32 v142, s56\n"
"v_mov_b32 v143, s57\n"
"s_add_u32 s94, s52, 0x886000\n"
"s_mov_b64 exec, 1\n"
"buffer_store_dwordx2 v[142:143], off, s[12:15], s94 sc1\n"
"s_mov_b64 exec, -1\n"
".Lpub12:\n"
"v_mfma_f32_16x16x32_bf16 v[208:211], v[224:227], v[36:39], v[208:211]\n"
"ds_read_b64_tr_b16 v[234:235], v138 offset:50176\n"
"s_waitcnt lgkmcnt(2)\n"
"v_mfma_f32_16x16x32_bf16 v[180:183], v[228:231], v[32:35], v[180:183]\n"
"ds_read_b64_tr_b16 v[236:237], v139 offset:49152\n"
"v_mfma_f32_16x16x32_bf16 v[212:215], v[228:231], v[36:39], v[212:215]\n"
"ds_read_b64_tr_b16 v[238:239], v139 offset:50176\n"
"s_add_u32 m0, s88, 0x8000\n"
"s_mul_i32 s96, s1, 204800\n"
"s_add_u32 s96, s96, s85\n"
"buffer_load_dwordx4 v251, s[16:19], s96 offen lds\n"
"s_waitcnt lgkmcnt(2)\n"
"v_mfma_f32_16x16x32_bf16 v[184:187], v[232:235], v[32:35], v[184:187]\n"
"v_add_u32 v136, 0x10000, v136\n"
"v_add_u32 v137, 0x10000, v137\n"
"v_add_u32 v138, 0x10000, v138\n"
"v_add_u32 v139, 0x10000, v139\n"
"ds_read_b64_tr_b16 v[224:225], v136 offset:0\n"
"v_mfma_f32_16x16x32_bf16 v[216:219], v[232:235], v[36:39], v[216:219]\n"
"ds_read_b64_tr_b16 v[226:227], v136 offset:1024\n"
"s_waitcnt lgkmcnt(2)\n"
"v_mfma_f32_16x16x32_bf16 v[188:191], v[236:239], v[32:35], v[188:191]\n"
"ds_read_b64_tr_b16 v[228:229], v137 offset:0\n"
"s_add_u32 s97, s96, 0x1000\n"
"buffer_load_dwordx4 v251, s[16:19], s97 offen offset:1024 lds\n"
"v_mfma_f32_16x16x32_bf16 v[220:223], v[236:239], v[36:39], v[220:223]\n"
"ds_read_b64_tr_b16 v[230:231], v137 offset:1024\n"
"s_barrier\n"
"s_waitcnt lgkmcnt(2)\n"
"v_mfma_f32_16x16x32_bf16 v[176:179], v[224:227], v[40:43], v[176:179]\n"
"ds_read_b64_tr_b16 v[232:233], v138 offset:0\n"
"v_lshrrev_b32 v147, 2, v253\n"
"s_lshl_b32 s98, s90, 6\n"
"s_add_u32 s98, s98, 0x880000\n"
"buffer_load_dword v148, v147, s[12:15], s98 offen sc1\n"
"v_mfma_f32_16x16x32_bf16 v[208:211], v[224:227], v[44:47], v[208:211]\n"
"ds_read_b64_tr_b16 v[234:235], v138 offset:1024\n"
"s_waitcnt lgkmcnt(2)\n"
"v_mfma_f32_16x16x32_bf16 v[180:183], v[228:231], v[40:43], v[180:183]\n"
"ds_read_b64_tr_b16 v[236:237], v139 offset:0\n"
"v_mfma_f32_16x16x32_bf16 v[212:215], v[228:231], v[44:47], v[212:215]\n"
"ds_read_b64_tr_b16 v[238:239], v139 offset:1024\n"
"s_lshl_b32 s98, s0, 12\n"
"s_add_u32 s98, s98, s89\n"
"s_add_u32 s98, s98, 0x8000\n"
"buffer_load_dwordx4 v[64:67], v253, s[12:15], s98 offen sc1\n"
"s_waitcnt lgkmcnt(2)\n"
"v_mfma_f32_16x16x32_bf16 v[184:187], v[232:235], v[40:43], v[184:187]\n"
"ds_read_b64_tr_b16 v[224:225], v136 offset:16384\n"
"v_mfma_f32_16x16x32_bf16 v[216:219], v[232:235], v[44:47], v[216:219]\n"
"ds_read_b64_tr_b16 v[226:227], v136 offset:17408\n"
"s_waitcnt lgkmcnt(2)\n"
"v_mfma_f32_16x16x32_bf16 v[188:191], v[236:239], v[40:43], v[188:191]\n"
"ds_read_b64_tr_b16 v[228:229], v137 offset:16384\n"
"s_add_u32 s98, s98, 0x11000\n"
"buffer_load_dwordx4 v[68:71], v253, s[12:15], s98 offen sc1\n"
"v_mfma_f32_16x16x32_bf16 v[220:223], v[236:239], v[44:47], v[220:223]\n"
"ds_read_b64_tr_b16 v[230:231], v137 offset:17408\n"
"s_barrier\n"
"s_waitcnt lgkmcnt(2)\n"
"s_waitcnt vmcnt(10)\n"
"v_mfma_f32_16x16x32_bf16 v[176:179], v[224:227], v[48:51], v[176:179]\n"
"ds_read_b64_tr_b16 v[232:233], v138 offset:16384\n"
"s_add_u32 m0, s88, 0x14000\n"
"s_mul_i32 s96, s58, 204800\n"
"s_add_u32 s96, s96, s85\n"
"buffer_load_dwordx4 v251, s[16:19], s96 offen lds\n"
"s_waitcnt vmcnt(10)\n"
"v_mfma_f32_16x16x32_bf16 v[208:211], v[224:227], v[52:55], v[208:211]\n"
"ds_read_b64_tr_b16 v[234:235], v138 offset:17408\n"
"s_waitcnt lgkmcnt(2)\n"
"v_mfma_f32_16x16x32_bf16 v[180:183], v[228:231], v[48:51], v[180:183]\n"
"ds_read_b64_tr_b16 v[236:237], v139 offset:16384\n"
"s_add_u32 s97, s96, 0x1000\n"
"buffer_load_dwordx4 v251, s[16:19], s97 offen offset:1024 lds\n"
"v_mfma_f32_16x16x32_bf16 v[212:215], v[228:231], v[52:55], v[212:215]\n"
"ds_read_b64_tr_b16 v[238:239], v139 offset:17408\n"
"s_waitcnt lgkmcnt(2)\n"
"v_mfma_f32_16x16x32_bf16 v[184:187], v[232:235], v[48:51], v[184:187]\n"
"v_add_u32 v136, 0xfffe0000, v136\n"
"v_add_u32 v137, 0xfffe0000, v137\n"
"v_add_u32 v138, 0xfffe0000, v138\n"
"v_add_u32 v139, 0xfffe0000, v139\n"
"ds_read_b64_tr_b16 v[224:225], v136 offset:0\n"
"s_lshl_b32 s98, s1, 12\n"
"s_add_u32 s98, s98, s89\n"
"s_add_u32 s98, s98, 0x8000\n"
"buffer_load_dwordx4 v[72:75], v253, s[12:15], s98 offen sc1\n"
"v_mfma_f32_16x16x32_bf16 v[216:219], v[232:235], v[52:55], v[216:219]\n"
"ds_read_b64_tr_b16 v[226:227], v136 offset:1024\n"
"s_waitcnt lgkmcnt(2)\n"
"v_mfma_f32_16x16x32_bf16 v[188:191], v[236:239], v[48:51], v[188:191]\n"
"ds_read_b64_tr_b16 v[228:229], v137 offset:0\n"
"s_add_u32 s98, s98, 0x11000\n"
"buffer_load_dwordx4 v[76:79], v253, s[12:15], s98 offen sc1\n"
"v_mfma_f32_16x16x32_bf16 v[220:223], v[236:239], v[52:55], v[220:223]\n"
"ds_read_b64_tr_b16 v[230:231], v137 offset:1024\n"
"s_waitcnt vmcnt(11)\n"
"s_barrier\n"
"s_waitcnt lgkmcnt(2)\n"
"s_waitcnt vmcnt(10)\n"
"v_mfma_f32_16x16x32_bf16 v[176:179], v[224:227], v[56:59], v[176:179]\n"
"ds_read_b64_tr_b16 v[232:233], v138 offset:0\n"
"s_add_u32 m0, s88, 0x10000\n"
"s_mul_i32 s96, s59, 204800\n"
"s_add_u32 s96, s96, s85\n"
"buffer_load_dwordx4 v251, s[16:19], s96 offen lds\n"
"s_waitcnt vmcnt(10)\n"
"v_mfma_f32_16x16x32_bf16 v[208:211], v[224:227], v[60:63], v[208:211]\n"
"ds_read_b64_tr_b16 v[234:235], v138 offset:1024\n"
"s_waitcnt lgkmcnt(2)\n"
"v_mfma_f32_16x16x32_bf16 v[180:183], v[228:231], v[56:59], v[180:183]\n"
"ds_read_b64_tr_b16 v[236:237], v139 offset:0\n"
"s_add_u32 s97, s96, 0x1000\n"
"buffer_load_dwordx4 v251, s[16:19], s97 offen offset:1024 lds\n"
"v_mfma_f32_16x16x32_bf16 v[212:215], v[228:231], v[60:63], v[212:215]\n"
"ds_read_b64_tr_b16 v[238:239], v139 offset:1024\n"
"s_waitcnt lgkmcnt(2)\n"
"v_mfma_f32_16x16x32_bf16 v[184:187], v[232:235], v[56:59], v[184:187]\n"
"ds_read_b64_tr_b16 v[224:225], v136 offset:16384\n"
"s_lshl_b32 s98, s58, 12\n"
"s_add_u32 s98, s98, s89\n"
"s_add_u32 s98, s98, 0x8000\n"
"buffer_load_dwordx4 v[80:83], v253, s[12:15], s98 offen sc1\n"
"v_mfma_f32_16x16x32_bf16 v[216:219], v[232:235], v[60:63], v[216:219]\n"
"ds_read_b64_tr_b16 v[226:227], v136 offset:17408\n"
"s_waitcnt lgkmcnt(2)\n"
"v_mfma_f32_16x16x32_bf16 v[188:191], v[236:239], v[56:59], v[188:191]\n"
"ds_read_b64_tr_b16 v[228:229], v137 offset:16384\n"
"s_add_u32 s98, s98, 0x11000\n"
"buffer_load_dwordx4 v[84:87], v253, s[12:15], s98 offen sc1\n"
"v_mfma_f32_16x16x32_bf16 v[220:223], v[236:239], v[60:63], v[220:223]\n"
"ds_read_b64_tr_b16 v[230:231], v137 offset:17408\n"
"s_waitcnt vmcnt(11)\n"
"s_barrier\n"
"s_waitcnt lgkmcnt(2)\n"
"s_waitcnt vmcnt(9)\n"
"v_mfma_f32_16x16x32_bf16 v[160:163], v[224:227], v[64:67], 0\n"
"ds_read_b64_tr_b16 v[232:233], v138 offset:16384\n"
"s_add_u32 m0, s88, 0xc000\n"
"s_add_u32 s96, s86, 0x0\n"
"buffer_load_dwordx4 v252, s[16:19], s96 offen lds\n"
"s_waitcnt vmcnt(9)\n"
"v_mfma_f32_16x16x32_bf16 v[192:195], v[224:227], v[68:71], 0\n"
"ds_read_b64_tr_b16 v[234:235], v138 offset:17408\n"
"s_waitcnt lgkmcnt(2)\n"
"v_mfma_f32_16x16x32_bf16 v[164:167], v[228:231], v[64:67], 0\n"
"ds_read_b64_tr_b16 v[236:237], v139 offset:16384\n"
"s_add_u32 s97, s96, 0x6000\n"
"buffer_load_dwordx4 v252, s[16:19], s97 offen offset:1024 lds\n"
"v_mfma_f32_16x16x32_bf16 v[196:199], v[228:231], v[68:71], 0\n"
"ds_read_b64_tr_b16 v[238:239], v139 offset:17408\n"
"s_lshl_b32 s98, s59, 12\n"
"s_add_u32 s98, s98, s89\n"
"s_add_u32 s98, s98, 0x8000\n"
"buffer_load_dwordx4 v[88:91], v253, s[12:15], s98 offen sc1\n"
"s_waitcnt lgkmcnt(2)\n"
"v_mfma_f32_16x16x32_bf16 v[168:171], v[232:235], v[64:67], 0\n"
"ds_read_b64_tr_b16 v[224:225], v136 offset:32768\n"
"v_mfma_f32_16x16x32_bf16 v[200:203], v[232:235], v[68:71], 0\n"
"ds_read_b64_tr_b16 v[226:227], v136 offset:33792\n"
"s_add_u32 s98, s98, 0x11000\n"
"buffer_load_dwordx4 v[92:95], v253, s[12:15], s98 offen sc1\n"
"s_waitcnt lgkmcnt(2)\n"
"v_mfma_f32_16x16x32_bf16 v[172:175], v[236:239], v[64:67], 0\n"
"ds_read_b64_tr_b16 v[228:229], v137 offset:32768\n"
"s_cmp_lg_u32 s32, 7\n"
"s_cbranch_scc1 .Lpi13\n"
"v_and_b32 v248, 0xf0, v253\n"
"s_lshl_b32 s95, s33, 11\n"
"v_lshlrev_b32 v248, 3, v248\n"
"s_add_u32 s95, s95, 0x886000\n"
"buffer_load_dwordx2 v[154:155], v248, s[12:15], s95 offen sc1\n"
".Lpi13:\n"
"v_mfma_f32_16x16x32_bf16 v[204:207], v[236:239], v[68:71], 0\n"
"ds_read_b64_tr_b16 v[230:231], v137 offset:33792\n"
"s_waitcnt vmcnt(10)\n"
"s_barrier\n"
"s_waitcnt lgkmcnt(2)\n"
"s_waitcnt vmcnt(9)\n"
"v_mfma_f32_16x16x32_bf16 v[160:163], v[224:227], v[72:75], v[160:163]\n"
"ds_read_b64_tr_b16 v[232:233], v138 offset:32768\n"
"s_add_u32 m0, s88, 0x18000\n"
"s_mul_i32 s96, s0, 491520\n"
"s_add_u32 s96, s96, s87\n"
"buffer_load_dwordx4 v250, s[20:23], s96 offen lds\n"
"s_waitcnt vmcnt(9)\n"
"v_mfma_f32_16x16x32_bf16 v[192:195], v[224:227], v[76:79], v[192:195]\n"
"ds_read_b64_tr_b16 v[234:235], v138 offset:33792\n"
"v_mbcnt_lo_u32_b32 v6, -1, 0\n"
"s_waitcnt lgkmcnt(2)\n"
"v_mfma_f32_16x16x32_bf16 v[164:167], v[228:231], v[72:75], v[164:167]\n"
"ds_read_b64_tr_b16 v[236:237], v139 offset:32768\n"
"s_add_u32 s97, s96, 0x4c00\n"
"buffer_load_dwordx4 v250, s[20:23], s97 offen offset:1024 lds\n"
"v_mbcnt_hi_u32_b32 v6, -1, v6\n"
"v_mfma_f32_16x16x32_bf16 v[196:199], v[228:231], v[76:79], v[196:199]\n"
"ds_read_b64_tr_b16 v[238:239], v139 offset:33792\n"
"s_mov_b32 s40, 0x3f80\n"
"s_waitcnt lgkmcnt(2)\n"
"v_mfma_f32_16x16x32_bf16 v[168:171], v[232:235], v[72:75], v[168:171]\n"
"v_add_u32 v136, 0x10000, v136\n"
"v_add_u32 v137, 0x10000, v137\n"
"v_add_u32 v138, 0x10000, v138\n"
"v_add_u32 v139, 0x10000, v139\n"
"ds_read_b64_tr_b16 v[224:225], v136 offset:16384\n"
"s_add_u32 s98, s89, 0xc000\n"
"buffer_load_dwordx4 v[96:99], v253, s[12:15], s98 offen sc1\n"
"s_mov_b32 s41, 0x3f800000\n"
"s_and_b32 s35, s32, 3\n"
"v_mfma_f32_16x16x32_bf16 v[200:203], v[232:235], v[76:79], v[200:203]\n"
"ds_read_b64_tr_b16 v[226:227], v136 offset:17408\n"
"s_mul_i32 s35, s35, 9216\n"
"s_waitcnt lgkmcnt(2)\n"
"v_mfma_f32_16x16x32_bf16 v[172:175], v[236:239], v[72:75], v[172:175]\n"
"ds_read_b64_tr_b16 v[228:229], v137 offset:16384\n"
"s_add_u32 s98, s89, 0x1d000\n"
"buffer_load_dwordx4 v[100:103], v253, s[12:15], s98 offen sc1\n"
"s_lshr_b32 s36, s32, 2\n"
"v_mfma_f32_16x16x32_bf16 v[204:207], v[236:239], v[76:79], v[204:207]\n"
"ds_read_b64_tr_b16 v[230:231], v137 offset:17408\n"
"s_mul_i32 s37, s36, 4608\n"
"s_add_u32 s35, s35, s37\n"
"s_waitcnt vmcnt(10)\n"
"s_cmp_lg_u32 s32, 7\n"
"s_cbranch_scc1 .Lok14\n"
"s_waitcnt vmcnt(4)\n"
"v_cmp_ne_u64 vcc, s[56:57], v[154:155]\n"
"s_nop 1\n"
"s_cmp_eq_u64 vcc, 0\n"
"s_cbranch_scc1 .Lok14\n"
"s_mov_b32 s94, 0\n"
"s_lshl_b32 s95, s33, 11\n"
"s_add_u32 s95, s95, 0x886000\n"
".Lagain15:\n"
"s_sleep 1\n"
"buffer_load_dwordx2 v[154:155], v248, s[12:15], s95 offen sc1\n"
"s_add_u32 s94, s94, 1\n"
"s_waitcnt vmcnt(0)\n"
"v_cmp_ne_u64 vcc, s[56:57], v[154:155]\n"
"s_nop 1\n"
"s_cmp_eq_u64 vcc, 0\n"
"s_cbranch_scc1 .Lok14\n"
"s_cmp_lt_u32 s94, 0x8000\n"
"s_cbranch_scc1 .Lagain15\n"
".Lok14:\n"
"s_barrier\n"
"s_waitcnt lgkmcnt(2)\n"
"s_waitcnt vmcnt(9)\n"
"v_mfma_f32_16x16x32_bf16 v[160:163], v[224:227], v[80:83], v[160:163]\n"
"ds_read_b64_tr_b16 v[232:233], v138 offset:16384\n"
"s_add_u32 m0, s88, 0x1c000\n"
"s_mul_i32 s96, s1, 491520\n"
"s_add_u32 s96, s96, s87\n"
"buffer_load_dwordx4 v250, s[20:23], s96 offen lds\n"
"s_waitcnt vmcnt(9)\n"
"v_mfma_f32_16x16x32_bf16 v[192:195], v[224:227], v[84:87], v[192:195]\n"
"ds_read_b64_tr_b16 v[234:235], v138 offset:17408\n"
"s_add_u32 s35, s35, 0x0\n"
"s_waitcnt lgkmcnt(2)\n"
"v_mfma_f32_16x16x32_bf16 v[164:167], v[228:231], v[80:83], v[164:167]\n"
"ds_read_b64_tr_b16 v[236:237], v139 offset:16384\n"
"s_add_u32 s97, s96, 0x4c00\n"
"buffer_load_dwordx4 v250, s[20:23], s97 offen offset:1024 lds\n"
"v_and_b32 v0, 15, v6\n"
"v_mfma_f32_16x16x32_bf16 v[196:199], v[228:231], v[84:87], v[196:199]\n"
"ds_read_b64_tr_b16 v[238:239], v139 offset:17408\n"
"v_mul_u32_u24 v0, 144, v0\n"
"s_waitcnt lgkmcnt(2)\n"
"v_mfma_f32_16x16x32_bf16 v[168:171], v[232:235], v[80:83], v[168:171]\n"
"ds_read_b64_tr_b16 v[224:225], v136 offset:0\n"
"s_lshl_b32 s98, s0, 12\n"
"s_add_u32 s98, s98, s89\n"
"s_add_u32 s98, s98, 0xd000\n"
"buffer_load_dwordx4 v[104:107], v253, s[12:15], s98 offen sc1\n"
"v_lshrrev_b32 v1, 4, v6\n"
"v_lshl_add_u32 v0, v1, 3, v0\n"
"v_mfma_f32_16x16x32_bf16 v[200:203], v[232:235], v[84:87], v[200:203]\n"
"ds_read_b64_tr_b16 v[226:227], v136 offset:1024\n"
"v_add_u32 v149, s35, v0\n"
"s_waitcnt lgkmcnt(2)\n"
"v_mfma_f32_16x16x32_bf16 v[172:175], v[236:239], v[80:83], v[172:175]\n"
"ds_read_b64_tr_b16 v[228:229], v137 offset:0\n"
"s_add_u32 s98, s98, 0x11000\n"
"buffer_load_dwordx4 v[108:111], v253, s[12:15], s98 offen sc1\n"
"s_mul_i32 s35, s32, 8\n"
"v_mfma_f32_16x16x32_bf16 v[204:207], v[236:239], v[84:87], v[204:207]\n"
"ds_read_b64_tr_b16 v[230:231], v137 offset:1024\n"
"v_lshrrev_b32 v1, 3, v6\n"
"v_add_u32 v1, s35, v1\n"
"s_waitcnt vmcnt(10)\n"
"s_barrier\n"
"s_waitcnt lgkmcnt(2)\n"
"s_waitcnt vmcnt(9)\n"
"v_mfma_f32_16x16x32_bf16 v[160:163], v[224:227], v[88:91], v[160:163]\n"
"ds_read_b64_tr_b16 v[232:233], v138 offset:0\n"
"s_add_u32 m0, s88, 0x20000\n"
"s_mul_i32 s96, s58, 491520\n"
"s_add_u32 s96, s96, s87\n"
"buffer_load_dwordx4 v250, s[20:23], s96 offen lds\n"
"s_waitcnt vmcnt(9)\n"
"v_mfma_f32_16x16x32_bf16 v[192:195], v[224:227], v[92:95], v[192:195]\n"
"ds_read_b64_tr_b16 v[234:235], v138 offset:1024\n"
"v_mul_u32_u24 v0, 144, v1\n"
"v_and_b32 v2, 7, v6\n"
"s_waitcnt lgkmcnt(2)\n"
"v_mfma_f32_16x16x32_bf16 v[164:167], v[228:231], v[88:91], v[164:167]\n"
"ds_read_b64_tr_b16 v[236:237], v139 offset:0\n"
"s_add_u32 s97, s96, 0x4c00\n"
"buffer_load_dwordx4 v250, s[20:23], s97 offen offset:1024 lds\n"
"v_lshl_add_u32 v0, v2, 4, v0\n"
"v_mfma_f32_16x16x32_bf16 v[196:199], v[228:231], v[92:95], v[196:199]\n"
"ds_read_b64_tr_b16 v[238:239], v139 offset:1024\n"
"v_add_u32 v150, 0x0, v0\n"
"s_mul_i32 s36, s36, 2\n"
"s_waitcnt lgkmcnt(2)\n"
"v_mfma_f32_16x16x32_bf16 v[168:171], v[232:235], v[88:91], v[168:171]\n"
"v_add_u32 v136, 0xffff0000, v136\n"
"v_add_u32 v137, 0xffff0000, v137\n"
"v_add_u32 v138, 0xffff0000, v138\n"
"v_add_u32 v139, 0xffff0000, v139\n"
"ds_read_b64_tr_b16 v[224:225], v136 offset:49152\n"
"s_lshl_b32 s98, s1, 12\n"
"s_add_u32 s98, s98, s89\n"
"s_add_u32 s98, s98, 0xd000\n"
"buffer_load_dwordx4 v[112:115], v253, s[12:15], s98 offen sc1\n"
"s_sub_u32 s36, s90, s36\n"
"v_mfma_f32_16x16x32_bf16 v[200:203], v[232:235], v[92:95], v[200:203]\n"
"ds_read_b64_tr_b16 v[226:227], v136 offset:50176\n"
"v_add_u32 v3, 0, v1\n"
"v_lshrrev_b32 v4, 4, v3\n"
"s_waitcnt lgkmcnt(2)\n"
"v_mfma_f32_16x16x32_bf16 v[172:175], v[236:239], v[88:91], v[172:175]\n"
"ds_read_b64_tr_b16 v[228:229], v137 offset:49152\n"
"s_add_u32 s98, s98, 0x11000\n"
"buffer_load_dwordx4 v[116:119], v253, s[12:15], s98 offen sc1\n"
"v_add_u32 v4, s36, v4\n"
"v_mfma_f32_16x16x32_bf16 v[204:207], v[236:239], v[92:95], v[204:207]\n"
"ds_read_b64_tr_b16 v[230:231], v137 offset:50176\n"
"v_and_b32 v147, 63, v147\n"
"ds_bpermute_b32 v144, v147, v148 offset:0\n"
"ds_bpermute_b32 v145, v147, v148 offset:64\n"
"s_waitcnt vmcnt(10)\n"
"s_barrier\n"
"s_xor_b32 s94, s32, 7\n"
"s_or_b32 s94, s94, s2\n"
"s_cmp_lg_u32 s94, 0\n"
"s_cbranch_scc1 .Leend16\n"
"v_lshlrev_b32 v8, 3, v253\n"
"s_mov_b32 s94, 0\n"
".Leagain17:\n"
"s_mov_b32 s95, 0x882000\n"
"buffer_load_dwordx2 v[10:11], v8, s[12:15], s95 offen sc1\n"
"s_mov_b32 s95, 0x884000\n"
"buffer_load_dwordx2 v[12:13], v8, s[12:15], s95 offen sc1\n"
"s_mov_b32 s95, 0x88a000\n"
"buffer_load_dwordx2 v[14:15], v8, s[12:15], s95 offen sc1\n"
"s_mov_b32 s95, 0x88c000\n"
"buffer_load_dwordx2 v[16:17], v8, s[12:15], s95 offen sc1\n"
"s_waitcnt vmcnt(0)\n"
"v_cmp_ne_u64 s[60:61], s[56:57], v[10:11]\n"
"v_cmp_ne_u64 s[62:63], s[56:57], v[12:13]\n"
"v_cmp_ne_u64 s[64:65], s[56:57], v[14:15]\n"
"v_cmp_ne_u64 s[66:67], s[56:57], v[16:17]\n"
"s_or_b64 s[60:61], s[60:61], s[62:63]\n"
"s_nop 0\n"
"s_or_b64 s[64:65], s[64:65], s[66:67]\n"
"s_or_b64 s[60:61], s[60:61], s[64:65]\n"
"s_add_u32 s94, s94, 1\n"
"s_cmp_eq_u64 s[60:61], 0\n"
"s_cbranch_scc1 .Leok18\n"
"s_sleep 2\n"
"s_cmp_lt_u32 s94, 0x8000\n"
"s_cbranch_scc1 .Leagain17\n"
".Leok18:\n"
"v_mov_b32 v10, s56\n"
"v_mov_b32 v11, s57\n"
"s_mov_b32 s95, 0x892000\n"
"s_mov_b64 exec, 1\n"
"buffer_store_dwordx2 v[10:11], off, s[12:15], s95 sc1\n"
"s_mov_b64 exec, -1\n"
".Leend16:\n"
"s_waitcnt lgkmcnt(4)\n"
"s_waitcnt vmcnt(9)\n"
"v_mfma_f32_16x16x32_bf16 v[160:163], v[224:227], v[96:99], v[160:163]\n"
"ds_read_b64_tr_b16 v[232:233], v138 offset:49152\n"
"s_add_u32 m0, s88, 0x24000\n"
"s_mul_i32 s96, s59, 491520\n"
"s_add_u32 s96, s96, s87\n"
"buffer_load_dwordx4 v250, s[20:23], s96 offen lds\n"
"s_waitcnt vmcnt(9)\n"
"v_mfma_f32_16x16x32_bf16 v[192:195], v[224:227], v[100:103], v[192:195]\n"
"ds_read_b64_tr_b16 v[234:235], v138 offset:50176\n"
"v_and_b32 v3, 15, v3\n"
"v_lshlrev_b32 v3, 1, v3\n"
"s_waitcnt lgkmcnt(4)\n"
"v_mfma_f32_16x16x32_bf16 v[164:167], v[228:231], v[96:99], v[164:167]\n"
"ds_read_b64_tr_b16 v[236:237], v139 offset:49152\n"
"s_add_u32 s97, s96, 0x4c00\n"
"buffer_load_dwordx4 v250, s[20:23], s97 offen offset:1024 lds\n"
"v_lshrrev_b32 v5, 2, v4\n"
"v_mfma_f32_16x16x32_bf16 v[196:199], v[228:231], v[100:103], v[196:199]\n"
"ds_read_b64_tr_b16 v[238:239], v139 offset:50176\n"
"v_lshl_add_u32 v3, v5, 6, v3\n"
"v_and_b32 v5, 1, v4\n"
"s_waitcnt lgkmcnt(2)\n"
"v_mfma_f32_16x16x32_bf16 v[168:171], v[232:235], v[96:99], v[168:171]\n"
"v_add_u32 v136, 0x10000, v136\n"
"v_add_u32 v137, 0x10000, v137\n"
"v_add_u32 v138, 0x10000, v138\n"
"v_add_u32 v139, 0x10000, v139\n"
"ds_read_b64_tr_b16 v[224:225], v136 offset:32768\n"
"s_lshl_b32 s98, s58, 12\n"
"s_add_u32 s98, s98, s89\n"
"s_add_u32 s98, s98, 0xd000\n"
"buffer_load_dwordx4 v[120:123], v253, s[12:15], s98 offen sc1\n"
"v_lshl_add_u32 v3, v5, 5, v3\n"
"v_mfma_f32_16x16x32_bf16 v[200:203], v[232:235], v[100:103], v[200:203]\n"
"ds_read_b64_tr_b16 v[226:227], v136 offset:33792\n"
"v_bfe_u32 v5, v4, 1, 1\n"
"v_add_u32 v3, v3, v5\n"
"s_waitcnt lgkmcnt(2)\n"
"v_mfma_f32_16x16x32_bf16 v[172:175], v[236:239], v[96:99], v[172:175]\n"
"ds_read_b64_tr_b16 v[228:229], v137 offset:32768\n"
"s_add_u32 s98, s98, 0x11000\n"
"buffer_load_dwordx4 v[124:127], v253, s[12:15], s98 offen sc1\n"
"v_mul_u32_u24 v3, 1280, v3\n"
"v_mfma_f32_16x16x32_bf16 v[204:207], v[236:239], v[100:103], v[204:207]\n"
"ds_read_b64_tr_b16 v[230:231], v137 offset:33792\n"
"v_lshl_add_u32 v151, v2, 4, v3\n"
"v_sub_f32 v147, 1.0, v144\n"
"v_sub_f32 v148, 1.0, v145\n"
"s_waitcnt vmcnt(10)\n"
"s_barrier\n"
"s_waitcnt lgkmcnt(2)\n"
"s_waitcnt vmcnt(9)\n"
"v_mfma_f32_16x16x32_bf16 v[176:179], v[224:227], v[104:107], v[176:179]\n"
"ds_read_b64_tr_b16 v[232:233], v138 offset:32768\n"
"s_waitcnt vmcnt(8)\n"
"v_mfma_f32_16x16x32_bf16 v[208:211], v[224:227], v[108:111], v[208:211]\n"
"ds_read_b64_tr_b16 v[234:235], v138 offset:33792\n"
"s_lshl_b32 s98, s59, 12\n"
"s_add_u32 s98, s98, s89\n"
"s_add_u32 s98, s98, 0xd000\n"
"buffer_load_dwordx4 v[128:131], v253, s[12:15], s98 offen sc1\n"
"s_waitcnt lgkmcnt(2)\n"
"v_mfma_f32_16x16x32_bf16 v[180:183], v[228:231], v[104:107], v[180:183]\n"
"ds_read_b64_tr_b16 v[236:237], v139 offset:32768\n"
"v_mfma_f32_16x16x32_bf16 v[212:215], v[228:231], v[108:111], v[212:215]\n"
"ds_read_b64_tr_b16 v[238:239], v139 offset:33792\n"
"s_waitcnt lgkmcnt(2)\n"
"v_mfma_f32_16x16x32_bf16 v[184:187], v[232:235], v[104:107], v[184:187]\n"
"ds_read_b64_tr_b16 v[224:225], v136 offset:49152\n"
"v_mfma_f32_16x16x32_bf16 v[216:219], v[232:235], v[108:111], v[216:219]\n"
"ds_read_b64_tr_b16 v[226:227], v136 offset:50176\n"
"s_add_u32 s98, s98, 0x11000\n"
"buffer_load_dwordx4 v[132:135], v253, s[12:15], s98 offen sc1\n"
"s_waitcnt lgkmcnt(2)\n"
"v_mfma_f32_16x16x32_bf16 v[188:191], v[236:239], v[104:107], v[188:191]\n"
"ds_read_b64_tr_b16 v[228:229], v137 offset:49152\n"
"v_mfma_f32_16x16x32_bf16 v[220:223], v[236:239], v[108:111], v[220:223]\n"
"ds_read_b64_tr_b16 v[230:231], v137 offset:50176\n"
"s_waitcnt vmcnt(8)\n"
"s_barrier\n"
"s_waitcnt lgkmcnt(2)\n"
"s_waitcnt vmcnt(7)\n"
"v_mfma_f32_16x16x32_bf16 v[176:179], v[224:227], v[112:115], v[176:179]\n"
"ds_read_b64_tr_b16 v[232:233], v138 offset:49152\n"
"v_mul_f32 v160, v144, v160\n"
"s_waitcnt vmcnt(6)\n"
"v_mfma_f32_16x16x32_bf16 v[208:211], v[224:227], v[116:119], v[208:211]\n"
"ds_read_b64_tr_b16 v[234:235], v138 offset:50176\n"
"v_mul_f32 v161, v144, v161\n"
"v_mul_f32 v162, v144, v162\n"
"s_waitcnt lgkmcnt(2)\n"
"v_mfma_f32_16x16x32_bf16 v[180:183], v[228:231], v[112:115], v[180:183]\n"
"ds_read_b64_tr_b16 v[236:237], v139 offset:49152\n"
"v_mul_f32 v163, v144, v163\n"
"v_mul_f32 v164, v144, v164\n"
"v_mfma_f32_16x16x32_bf16 v[212:215], v[228:231], v[116:119], v[212:215]\n"
"ds_read_b64_tr_b16 v[238:239], v139 offset:50176\n"
"v_mul_f32 v165, v144, v165\n"
"v_mul_f32 v166, v144, v166\n"
"s_waitcnt lgkmcnt(2)\n"
"v_mfma_f32_16x16x32_bf16 v[184:187], v[232:235], v[112:115], v[184:187]\n"
"v_add_u32 v136, 0x10000, v136\n"
"v_add_u32 v137, 0x10000, v137\n"
"v_add_u32 v138, 0x10000, v138\n"
"v_add_u32 v139, 0x10000, v139\n"
"ds_read_b64_tr_b16 v[224:225], v136 offset:0\n"
"v_mul_f32 v167, v144, v167\n"
"v_mul_f32 v168, v144, v168\n"
"v_mfma_f32_16x16x32_bf16 v[216:219], v[232:235], v[116:119], v[216:219]\n"
"ds_read_b64_tr_b16 v[226:227], v136 offset:1024\n"
"v_mul_f32 v169, v144, v169\n"
"v_mul_f32 v170, v144, v170\n"
"s_waitcnt lgkmcnt(2)\n"
"v_mfma_f32_16x16x32_bf16 v[188:191], v[236:239], v[112:115], v[188:191]\n"
"ds_read_b64_tr_b16 v[228:229], v137 offset:0\n"
"v_mul_f32 v171, v144, v171\n"
"v_mul_f32 v172, v144, v172\n"
"v_mfma_f32_16x16x32_bf16 v[220:223], v[236:239], v[116:119], v[220:223]\n"
"ds_read_b64_tr_b16 v[230:231], v137 offset:1024\n"
"v_mul_f32 v173, v144, v173\n"
"v_mul_f32 v174, v144, v174\n"
"v_mul_f32 v175, v144, v175\n"
"s_waitcnt vmcnt(4)\n"
"s_barrier\n"
"s_waitcnt lgkmcnt(2)\n"
"s_waitcnt vmcnt(3)\n"
"v_mfma_f32_16x16x32_bf16 v[176:179], v[224:227], v[120:123], v[176:179]\n"
"ds_read_b64_tr_b16 v[232:233], v138 offset:0\n"
"v_mul_f32 v192, v145, v192\n"
"s_waitcnt vmcnt(2)\n"
"v_mfma_f32_16x16x32_bf16 v[208:211], v[224:227], v[124:127], v[208:211]\n"
"ds_read_b64_tr_b16 v[234:235], v138 offset:1024\n"
"v_mul_f32 v193, v145, v193\n"
"v_mul_f32 v194, v145, v194\n"
"s_waitcnt lgkmcnt(2)\n"
"v_mfma_f32_16x16x32_bf16 v[180:183], v[228:231], v[120:123], v[180:183]\n"
"ds_read_b64_tr_b16 v[236:237], v139 offset:0\n"
"v_mul_f32 v195, v145, v195\n"
"v_mul_f32 v196, v145, v196\n"
"v_mfma_f32_16x16x32_bf16 v[212:215], v[228:231], v[124:127], v[212:215]\n"
"ds_read_b64_tr_b16 v[238:239], v139 offset:1024\n"
"v_mul_f32 v197, v145, v197\n"
"v_mul_f32 v198, v145, v198\n"
"s_waitcnt lgkmcnt(2)\n"
"v_mfma_f32_16x16x32_bf16 v[184:187], v[232:235], v[120:123], v[184:187]\n"
"ds_read_b64_tr_b16 v[224:225], v136 offset:16384\n"
"v_mul_f32 v199, v145, v199\n"
"v_mul_f32 v200, v145, v200\n"
"v_mfma_f32_16x16x32_bf16 v[216:219], v[232:235], v[124:127], v[216:219]\n"
"ds_read_b64_tr_b16 v[226:227], v136 offset:17408\n"
"v_mul_f32 v201, v145, v201\n"
"v_mul_f32 v202, v145, v202\n"
"s_waitcnt lgkmcnt(2)\n"
"v_mfma_f32_16x16x32_bf16 v[188:191], v[236:239], v[120:123], v[188:191]\n"
"ds_read_b64_tr_b16 v[228:229], v137 offset:16384\n"
"v_mul_f32 v203, v145, v203\n"
"v_mul_f32 v204, v145, v204\n"
"v_mfma_f32_16x16x32_bf16 v[220:223], v[236:239], v[124:127], v[220:223]\n"
"ds_read_b64_tr_b16 v[230:231], v137 offset:17408\n"
"v_mul_f32 v205, v145, v205\n"
"v_mul_f32 v206, v145, v206\n"
"v_mul_f32 v207, v145, v207\n"
"s_waitcnt lgkmcnt(2)\n"
"s_waitcnt vmcnt(1)\n"
"v_mfma_f32_16x16x32_bf16 v[176:179], v[224:227], v[128:131], v[176:179]\n"
"ds_read_b64_tr_b16 v[232:233], v138 offset:16384\n"
"s_waitcnt vmcnt(0)\n"
"v_mfma_f32_16x16x32_bf16 v[208:211], v[224:227], v[132:135], v[208:211]\n"
"ds_read_b64_tr_b16 v[234:235], v138 offset:17408\n"
"s_waitcnt lgkmcnt(2)\n"
"v_mfma_f32_16x16x32_bf16 v[180:183], v[228:231], v[128:131], v[180:183]\n"
"ds_read_b64_tr_b16 v[236:237], v139 offset:16384\n"
"v_mfma_f32_16x16x32_bf16 v[212:215], v[228:231], v[132:135], v[212:215]\n"
"ds_read_b64_tr_b16 v[238:239], v139 offset:17408\n"
"s_waitcnt lgkmcnt(2)\n"
"v_mfma_f32_16x16x32_bf16 v[184:187], v[232:235], v[128:131], v[184:187]\n"
"v_fmac_f32 v160, v147, v176\n"
"v_fmac_f32 v161, v147, v177\n"
"v_fmac_f32 v162, v147, v178\n"
"v_fmac_f32 v163, v147, v179\n"
"v_cvt_pk_bf16_f32 v160, v160, v161\n"
"v_cvt_pk_bf16_f32 v161, v162, v163\n"
"ds_write_b64 v149, v[160:161] offset:0\n"
"v_mfma_f32_16x16x32_bf16 v[216:219], v[232:235], v[132:135], v[216:219]\n"
"v_fmac_f32 v192, v148, v208\n"
"v_fmac_f32 v193, v148, v209\n"
"v_fmac_f32 v194, v148, v210\n"
"v_fmac_f32 v195, v148, v211\n"
"v_cvt_pk_bf16_f32 v192, v192, v193\n"
"v_cvt_pk_bf16_f32 v193, v194, v195\n"
"ds_write_b64 v149, v[192:193] offset:2304\n"
"s_waitcnt lgkmcnt(2)\n"
"v_mfma_f32_16x16x32_bf16 v[188:191], v[236:239], v[128:131], v[188:191]\n"
"v_fmac_f32 v164, v147, v180\n"
"v_fmac_f32 v165, v147, v181\n"
"v_fmac_f32 v166, v147, v182\n"
"v_fmac_f32 v167, v147, v183\n"
"v_cvt_pk_bf16_f32 v164, v164, v165\n"
"v_cvt_pk_bf16_f32 v165, v166, v167\n"
"ds_write_b64 v149, v[164:165] offset:32\n"
"v_mfma_f32_16x16x32_bf16 v[220:223], v[236:239], v[132:135], v[220:223]\n"
"v_fmac_f32 v196, v148, v212\n"
"v_fmac_f32 v197, v148, v213\n"
"v_fmac_f32 v198, v148, v214\n"
"v_fmac_f32 v199, v148, v215\n"
"v_cvt_pk_bf16_f32 v196, v196, v197\n"
"v_cvt_pk_bf16_f32 v197, v198, v199\n"
"ds_write_b64 v149, v[196:197] offset:2336\n"
"v_fmac_f32 v168, v147, v184\n"
"v_fmac_f32 v169, v147, v185\n"
"v_fmac_f32 v170, v147, v186\n"
"v_fmac_f32 v171, v147, v187\n"
"v_cvt_pk_bf16_f32 v168, v168, v169\n"
"v_cvt_pk_bf16_f32 v169, v170, v171\n"
"ds_write_b64 v149, v[168:169] offset:64\n"
"v_fmac_f32 v200, v148, v216\n"
"v_fmac_f32 v201, v148, v217\n"
"v_fmac_f32 v202, v148, v218\n"
"v_fmac_f32 v203, v148, v219\n"
"v_cvt_pk_bf16_f32 v200, v200, v201\n"
"v_cvt_pk_bf16_f32 v201, v202, v203\n"
"ds_write_b64 v149, v[200:201] offset:2368\n"
"v_fmac_f32 v172, v147, v188\n"
"v_fmac_f32 v173, v147, v189\n"
"v_fmac_f32 v174, v147, v190\n"
"v_fmac_f32 v175, v147, v191\n"
"v_cvt_pk_bf16_f32 v172, v172, v173\n"
"v_cvt_pk_bf16_f32 v173, v174, v175\n"
"ds_write_b64 v149, v[172:173] offset:96\n"
"v_fmac_f32 v204, v148, v220\n"
"v_fmac_f32 v205, v148, v221\n"
"v_fmac_f32 v206, v148, v222\n"
"v_fmac_f32 v207, v148, v223\n"
"v_cvt_pk_bf16_f32 v204, v204, v205\n"
"v_cvt_pk_bf16_f32 v205, v206, v207\n"
"ds_write_b64 v149, v[204:205] offset:2400\n"
"s_waitcnt lgkmcnt(0)\n"
"s_barrier\n"
"ds_read_b128 v[0:3], v150 offset:0\n"
"ds_read_b128 v[4:7], v150 offset:9216\n"
"ds_read_b128 v[8:11], v150 offset:18432\n"
"ds_read_b128 v[12:15], v150 offset:27648\n"
"s_waitcnt lgkmcnt(3)\n"
"v_lshlrev_b32 v16, 16, v0\n"
"v_and_b32 v17, 0xffff0000, v0\n"
"v_lshlrev_b32 v18, 16, v1\n"
"v_and_b32 v19, 0xffff0000, v1\n"
"v_lshlrev_b32 v20, 16, v2\n"
"v_and_b32 v21, 0xffff0000, v2\n"
"v_lshlrev_b32 v22, 16, v3\n"
"v_and_b32 v23, 0xffff0000, v3\n"
"s_waitcnt lgkmcnt(2)\n"
"v_dot2c_f32_bf16 v16, s40, v4\n"
"v_dot2c_f32_bf16 v17, s41, v4\n"
"v_dot2c_f32_bf16 v18, s40, v5\n"
"v_dot2c_f32_bf16 v19, s41, v5\n"
"v_dot2c_f32_bf16 v20, s40, v6\n"
"v_dot2c_f32_bf16 v21, s41, v6\n"
"v_dot2c_f32_bf16 v22, s40, v7\n"
"v_dot2c_f32_bf16 v23, s41, v7\n"
"s_waitcnt lgkmcnt(1)\n"
"v_dot2c_f32_bf16 v16, s40, v8\n"
"v_dot2c_f32_bf16 v17, s41, v8\n"
"v_dot2c_f32_bf16 v18, s40, v9\n"
"v_dot2c_f32_bf16 v19, s41, v9\n"
"v_dot2c_f32_bf16 v20, s40, v10\n"
"v_dot2c_f32_bf16 v21, s41, v10\n"
"v_dot2c_f32_bf16 v22, s40, v11\n"
"v_dot2c_f32_bf16 v23, s41, v11\n"
"s_waitcnt lgkmcnt(0)\n"
"v_dot2c_f32_bf16 v16, s40, v12\n"
"v_dot2c_f32_bf16 v17, s41, v12\n"
"v_dot2c_f32_bf16 v18, s40, v13\n"
"v_dot2c_f32_bf16 v19, s41, v13\n"
"v_dot2c_f32_bf16 v20, s40, v14\n"
"v_dot2c_f32_bf16 v21, s41, v14\n"
"v_dot2c_f32_bf16 v22, s40, v15\n"
"v_dot2c_f32_bf16 v23, s41, v15\n"
"s_nop 2\n"
"v_cvt_pk_bf16_f32 v0, v16, v17\n"
"v_cvt_pk_bf16_f32 v1, v18, v19\n"
"v_cvt_pk_bf16_f32 v2, v20, v21\n"
"v_cvt_pk_bf16_f32 v3, v22, v23\n"
"buffer_store_dwordx4 v[0:3], v151, s[24:27], s91 offen sc1\n"
"s_endpgm\n"
".Lqkv:\n"
"v_lshlrev_b32 v254, 4, v0\n"
"v_lshrrev_b32 v1, 3, v0\n"
"v_mul_u32_u24 v2, 2560, v1\n"
"v_lshrrev_b32 v4, 1, v1\n"
"v_and_b32 v3, 7, v0\n"
"v_xor_b32 v4, v4, v3\n"
"v_lshl_add_u32 v247, v4, 4, v2\n"
"v_xor_b32 v2, 64, v247\n"
"v_add_u32 v248, 19456, v2\n"
"v_mul_u32_u24 v5, 205, v0\n"
"v_lshrrev_b32 v5, 11, v5\n"
"v_mul_u32_u24 v2, 10, v5\n"
"v_sub_u32 v2, v0, v2\n"
"v_and_b32 v3, 1, v5\n"
"v_mul_u32_u24 v3, 3840, v3\n"
"v_lshl_add_u32 v2, v2, 4, v3\n"
"v_lshrrev_b32 v5, 1, v5\n"
"v_mul_u32_u24 v5, 1280, v5\n"
"v_add_u32 v5, v5, v2\n"
"v_mov_b32 v2, 0x40000000\n"
"v_cmp_gt_u32 vcc, 60, v0\n"
"s_nop 1\n"
"v_cndmask_b32 v246, v2, v5, vcc\n"
"s_sub_u32 s38, s34, 16\n"
"s_lshr_b32 s35, s33, 2\n"
"s_lshl_b32 s35, s35, 3\n"
"s_and_b32 s36, s38, 7\n"
"s_add_u32 s39, s35, s36\n"
"s_and_b32 s35, s33, 3\n"
"s_lshl_b32 s35, s35, 1\n"
"s_lshr_b32 s37, s38, 3\n"
"s_add_u32 s40, s35, s37\n"
"s_lshr_b32 s35, s32, 2\n"
"s_lshl_b32 s35, s35, 6\n"
"s_bfe_u32 s36, s32, 0x10001\n"
"s_add_u32 s35, s35, s36\n"
"s_and_b32 s37, s32, 1\n"
"s_lshl_b32 s38, s37, 5\n"
"s_add_u32 s35, s35, s38\n"
"s_lshl_b32 s38, s39, 7\n"
"s_add_u32 s35, s35, s38\n"
"s_mul_i32 s45, s35, 1280\n"
"s_lshl_b32 s35, s37, 3\n"
"s_add_u32 s35, s35, s38\n"
"s_sub_u32 s35, s35, 16\n"
"s_lshl_b32 s3, s37, 10\n"
"s_lshl_b32 s38, s37, 2\n"
"v_xor_b32 v4, s38, v4\n"
"v_add_u32 v1, s35, v1\n"
"v_max_i32 v1, 0, v1\n"
"v_mul_u32_u24 v1, 1280, v1\n"
"v_lshl_add_u32 v249, v4, 4, v1\n"
"s_lshr_b32 s35, s32, 2\n"
"s_lshl_b32 s35, s35, 2\n"
"s_add_u32 s35, s35, s36\n"
"s_lshl_b32 s37, s37, 1\n"
"s_add_u32 s35, s35, s37\n"
"s_mul_i32 s46, s35, 15360\n"
"s_mul_i32 s35, s40, 160\n"
"s_add_u32 s46, s46, s35\n"
"s_mul_i32 s48, s32, 2176\n"
"s_lshl_b32 s50, s32, 11\n"
"v_mov_b32 v127, v0\n"
"s_waitcnt lgkmcnt(0)\n"
"s_mov_b32 s4, s64\n"
"s_and_b32 s5, s65, 0xffff\n"
"s_mov_b32 s6, 0x280000\n"
"s_mov_b32 s7, 0x20000\n"
"s_mov_b32 s8, s66\n"
"s_and_b32 s9, s67, 0xffff\n"
"s_mov_b32 s10, 0x258000\n"
"s_mov_b32 s11, 0x20000\n"
"s_mov_b32 s28, s76\n"
"s_and_b32 s29, s77, 0xffff\n"
"s_mov_b32 s30, 0x500\n"
"s_mov_b32 s31, 0x20000\n"
"s_mul_i32 s35, s32, 160\n"
"v_lshlrev_b32 v1, 2, v127\n"
"buffer_load_dword v119, v1, s[28:31], s35 offen\n"
"s_add_u32 m0, s48, 0x0\n"
"s_add_u32 s43, s46, 0x0\n"
"buffer_load_dwordx4 v246, s[8:11], s43 offen lds\n"
"s_add_u32 s44, s46, 0x19c0\n"
"buffer_load_dwordx4 v246, s[8:11], s44 offen offset:1088 lds\n"
"s_cmp_ge_u32 s32, 2\n"
"s_cbranch_scc1 .Lhx9_3001\n"
"s_add_u32 m0, s3, 0x1d800\n"
"s_mov_b32 s41, 0\n"
"buffer_load_dwordx4 v249, s[4:7], s41 offen lds\n"
".Lhx9_3001:\n"
"s_add_u32 m0, s50, 0x19800\n"
"s_add_u32 s41, s45, 0\n"
"buffer_load_dwordx4 v247, s[4:7], s41 offen lds\n"
"buffer_load_dwordx4 v248, s[4:7], s41 offen offset:1024 lds\n"
"s_add_u32 m0, s48, 0x4400\n"
"s_add_u32 s43, s46, 0x1e000\n"
"buffer_load_dwordx4 v246, s[8:11], s43 offen lds\n"
"s_add_u32 s44, s46, 0x1f9c0\n"
"buffer_load_dwordx4 v246, s[8:11], s44 offen offset:1088 lds\n"
"s_barrier\n"
"s_add_u32 m0, s48, 0x8800\n"
"s_add_u32 s43, s46, 0x3c000\n"
"buffer_load_dwordx4 v246, s[8:11], s43 offen lds\n"
"s_add_u32 s44, s46, 0x3d9c0\n"
"buffer_load_dwordx4 v246, s[8:11], s44 offen offset:1088 lds\n"
"s_cmp_ge_u32 s32, 2\n"
"s_cbranch_scc1 .Lhx9_3002\n"
"s_add_u32 m0, s3, 0x22000\n"
"s_mov_b32 s41, 128\n"
"buffer_load_dwordx4 v249, s[4:7], s41 offen lds\n"
".Lhx9_3002:\n"
"s_add_u32 m0, s50, 0x1e000\n"
"s_add_u32 s41, s45, 128\n"
"buffer_load_dwordx4 v247, s[4:7], s41 offen lds\n"
"buffer_load_dwordx4 v248, s[4:7], s41 offen offset:1024 lds\n"
"s_add_u32 m0, s48, 0xcc00\n"
"s_add_u32 s43, s46, 0x5a000\n"
"buffer_load_dwordx4 v246, s[8:11], s43 offen lds\n"
"s_add_u32 s44, s46, 0x5b9c0\n"
"buffer_load_dwordx4 v246, s[8:11], s44 offen offset:1088 lds\n"
"s_mov_b32 s60, s64\n"
"s_mov_b32 s61, s65\n"
"s_mov_b32 s62, 0x280000\n"
"s_mov_b32 s82, 40\n"
"s_cmp_ge_u32 s32, 2\n"
"s_cselect_b32 s60, s66, s60\n"
"s_cselect_b32 s61, s67, s61\n"
"s_cselect_b32 s62, 0x258000, s62\n"
"s_cselect_b32 s82, 38, s82\n"
"s_cmp_ge_u32 s32, 4\n"
"s_cselect_b32 s60, s70, s60\n"
"s_cselect_b32 s61, s71, s61\n"
"s_cselect_b32 s62, 0x1e0000, s62\n"
"s_cselect_b32 s82, 60, s82\n"
"s_cmp_ge_u32 s32, 5\n"
"s_cselect_b32 s60, s72, s60\n"
"s_cselect_b32 s61, s73, s61\n"
"s_cselect_b32 s62, 0x1e0000, s62\n"
"s_cselect_b32 s82, 60, s82\n"
"s_cmp_ge_u32 s32, 6\n"
"s_cselect_b32 s60, s74, s60\n"
"s_cselect_b32 s61, s75, s61\n"
"s_cselect_b32 s62, 0x1e0000, s62\n"
"s_cselect_b32 s82, 60, s82\n"
"s_cmp_ge_u32 s32, 7\n"
"s_cselect_b32 s60, s68, s60\n"
"s_cselect_b32 s61, s69, s61\n"
"s_cselect_b32 s62, 0xc8000, s62\n"
"s_cselect_b32 s82, 25, s82\n"
"s_and_b32 s61, s61, 0xffff\n"
"s_mov_b32 s63, 0x20000\n"
"s_lshl_b32 s83, s2, 1\n"
"s_and_b32 s98, s32, 1\n"
"s_add_u32 s83, s83, s98\n"
"s_cmp_lt_u32 s32, 4\n"
"s_cselect_b32 s83, s83, s2\n"
"s_mul_i32 s83, s83, s82\n"
"s_lshl_b32 s83, s83, 7\n"
"v_lshlrev_b32 v124, 7, v127\n"
"v_mov_b32 v125, 0x40000000\n"
"v_cmp_gt_u32 vcc, s82, v127\n"
"s_nop 1\n"
"v_cndmask_b32 v124, v125, v124, vcc\n"
"buffer_load_dword v125, v124, s[60:63], s83 offen\n"
"s_lshr_b32 s35, s32, 2\n"
"s_and_b32 s36, s32, 3\n"
"s_lshl_b32 s37, s35, 13\n"
"s_add_u32 s37, s37, 0x19800\n"
"v_and_b32 v1, 15, v127\n"
"v_lshrrev_b32 v2, 1, v1\n"
"v_lshrrev_b32 v3, 4, v127\n"
"v_xor_b32 v2, v2, v3\n"
"v_lshlrev_b32 v1, 7, v1\n"
"v_lshl_add_u32 v1, v2, 4, v1\n"
"v_add_u32 v242, s37, v1\n"
"v_xor_b32 v243, 64, v242\n"
"v_add_u32 v244, 0x1d800, v1\n"
"v_xor_b32 v245, 64, v244\n"
"v_bfe_u32 v1, v127, 3, 2\n"
"v_lshrrev_b32 v2, 5, v127\n"
"v_lshl_add_u32 v1, v2, 3, v1\n"
"v_mul_u32_u24 v1, 1088, v1\n"
"v_bfe_u32 v2, v127, 2, 1\n"
"v_mul_u32_u24 v2, 160, v2\n"
"v_add_u32 v1, v1, v2\n"
"v_and_b32 v2, 3, v127\n"
"s_mul_i32 s37, s36, 40\n"
"v_lshl_add_u32 v3, v2, 3, v1\n"
"v_add_u32 v240, s37, v3\n"
"s_min_u32 s37, s36, 2\n"
"s_mul_i32 s37, s37, 320\n"
"s_add_u32 s37, s37, 32\n"
"v_mul_u32_u24 v2, 40, v2\n"
"v_add3_u32 v241, v1, v2, s37\n"
"v_lshrrev_b32 v1, 2, v127\n"
"v_lshlrev_b32 v1, 3, v1\n"
"v_add_u32 v1, 0x15400, v1\n"
"s_cmp_eq_u32 s36, 3\n"
"s_cselect_b64 vcc, -1, 0\n"
"s_cselect_b32 s47, 0xffffbc80, 0\n"
"s_cselect_b32 s37, 1, 0\n"
"v_cndmask_b32 v241, v241, v1, vcc\n"
"s_mov_b32 s95, 0xcc00\n"
"s_mov_b32 s99, 0xffff3400\n"
"s_cmp_eq_u32 s36, 3\n"
"s_cselect_b32 s95, 0x8880, s95\n"
"s_cselect_b32 s99, 0x4480, s99\n"
"s_mul_i32 s37, s32, 40\n"
"v_add_u32 v1, s37, v127\n"
"v_bfe_u32 v2, v1, 1, 1\n"
"v_mul_u32_u24 v2, 4352, v2\n"
"v_lshrrev_b32 v3, 4, v1\n"
"v_lshl_add_u32 v2, v3, 7, v2\n"
"v_bfe_u32 v3, v1, 2, 2\n"
"v_lshl_add_u32 v2, v3, 5, v2\n"
"v_and_b32 v3, 1, v1\n"
"v_lshl_add_u32 v2, v3, 4, v2\n"
"v_add_u32 v2, 0x15400, v2\n"
"v_mov_b32 v3, 0x17400\n"
"v_cmp_gt_u32 vcc, 40, v127\n"
"s_nop 1\n"
"v_cndmask_b32 v2, v3, v2, vcc\n"
"s_waitcnt vmcnt(13)\n"
"ds_write_b16 v2, v119\n"
"ds_write_b16_d16_hi v2, v119 offset:8\n"
"s_waitcnt vmcnt(7)\n"
"s_waitcnt lgkmcnt(0)\n"
"s_barrier\n"
"ds_read_b128 v[72:75], v242 offset:0\n"
"ds_read_b128 v[76:79], v242 offset:2048\n"
"ds_read_b128 v[80:83], v242 offset:4096\n"
"ds_read_b128 v[84:87], v242 offset:6144\n"
"ds_read_b128 v[88:91], v244 offset:0\n"
"ds_read_b64_tr_b16 v[208:209], v240 offset:0\n"
"ds_read_b64_tr_b16 v[210:211], v240 offset:4352\n"
"ds_read_b64_tr_b16 v[212:213], v240 offset:320\n"
"ds_read_b64_tr_b16 v[214:215], v240 offset:4672\n"
"s_cmp_ge_u32 s32, 4\n"
"s_cbranch_scc1 .Lqloop1\n"
"s_waitcnt lgkmcnt(2)\n"
"v_mfma_f32_16x16x32_bf16 v[0:3], v[208:211], v[72:75], 0\n"
"ds_read_b64_tr_b16 v[216:217], v240 offset:640\n"
"v_mfma_f32_16x16x32_bf16 v[4:7], v[208:211], v[76:79], 0\n"
"ds_read_b64_tr_b16 v[218:219], v240 offset:4992\n"
"s_cmp_ge_u32 s32, 2\n"
"s_cbranch_scc1 .Lhx0_3003\n"
"s_add_u32 m0, s3, 0x26800\n"
"s_mov_b32 s41, 256\n"
"buffer_load_dwordx4 v249, s[4:7], s41 offen lds\n"
".Lhx0_3003:\n"
"v_mfma_f32_16x16x32_bf16 v[8:11], v[208:211], v[80:83], 0\n"
"ds_read_b128 v[92:95], v243 offset:0\n"
"v_mfma_f32_16x16x32_bf16 v[12:15], v[208:211], v[84:87], 0\n"
"ds_read_b128 v[96:99], v243 offset:2048\n"
"s_waitcnt lgkmcnt(4)\n"
"v_mfma_f32_16x16x32_bf16 v[16:19], v[212:215], v[72:75], 0\n"
"ds_read_b64_tr_b16 v[220:221], v241 offset:0\n"
"s_add_u32 m0, s50, 0x22800\n"
"s_add_u32 s41, s45, 256\n"
"buffer_load_dwordx4 v247, s[4:7], s41 offen lds\n"
"v_mfma_f32_16x16x32_bf16 v[20:23], v[212:215], v[76:79], 0\n"
"ds_read_b64_tr_b16 v[222:223], v241 offset:4352\n"
"v_mfma_f32_16x16x32_bf16 v[24:27], v[212:215], v[80:83], 0\n"
"ds_read_b128 v[100:103], v243 offset:4096\n"
"v_mfma_f32_16x16x32_bf16 v[28:31], v[212:215], v[84:87], 0\n"
"ds_read_b128 v[104:107], v243 offset:6144\n"
"v_mfma_f32_16x16x32_bf16 v[64:67], v[212:215], v[88:91], 0\n"
"ds_read_b128 v[108:111], v245 offset:0\n"
"buffer_load_dwordx4 v248, s[4:7], s41 offen offset:1024 lds\n"
"s_waitcnt lgkmcnt(7)\n"
"v_mfma_f32_16x16x32_bf16 v[32:35], v[72:75], v[216:219], 0\n"
"ds_read_b64_tr_b16 v[208:209], v240 offset:17408\n"
"v_mfma_f32_16x16x32_bf16 v[36:39], v[76:79], v[216:219], 0\n"
"ds_read_b64_tr_b16 v[210:211], v240 offset:21760\n"
"v_mfma_f32_16x16x32_bf16 v[40:43], v[80:83], v[216:219], 0\n"
"s_add_u32 m0, s48, 0x11000\n"
"s_add_u32 s43, s46, 0x78000\n"
"buffer_load_dwordx4 v246, s[8:11], s43 offen lds\n"
"v_mfma_f32_16x16x32_bf16 v[44:47], v[84:87], v[216:219], 0\n"
"s_waitcnt lgkmcnt(5)\n"
"v_mfma_f32_16x16x32_bf16 v[48:51], v[220:223], v[72:75], 0\n"
"ds_read_b64_tr_b16 v[212:213], v240 offset:17728\n"
"v_mfma_f32_16x16x32_bf16 v[52:55], v[220:223], v[76:79], 0\n"
"ds_read_b64_tr_b16 v[214:215], v240 offset:22080\n"
"v_mfma_f32_16x16x32_bf16 v[56:59], v[220:223], v[80:83], 0\n"
"s_add_u32 s44, s46, 0x799c0\n"
"buffer_load_dwordx4 v246, s[8:11], s44 offen offset:1088 lds\n"
"v_mfma_f32_16x16x32_bf16 v[60:63], v[220:223], v[84:87], 0\n"
"v_mfma_f32_16x16x32_bf16 v[68:71], v[220:223], v[88:91], 0\n"
"s_waitcnt vmcnt(7)\n"
"s_waitcnt lgkmcnt(4)\n"
"s_barrier\n"
"s_waitcnt lgkmcnt(2)\n"
"v_mfma_f32_16x16x32_bf16 v[0:3], v[208:211], v[92:95], v[0:3]\n"
"ds_read_b64_tr_b16 v[216:217], v240 offset:18048\n"
"s_lshl_b32 s52, s39, 3\n"
"v_mfma_f32_16x16x32_bf16 v[4:7], v[208:211], v[96:99], v[4:7]\n"
"ds_read_b64_tr_b16 v[218:219], v240 offset:22400\n"
"s_add_u32 s49, s52, s40\n"
"s_lshl_b32 s52, s49, 7\n"
"v_mfma_f32_16x16x32_bf16 v[8:11], v[208:211], v[100:103], v[8:11]\n"
"ds_read_b128 v[72:75], v242 offset:18432\n"
"s_add_u32 m0, s48, 0x0\n"
"s_add_u32 s43, s46, 0x96000\n"
"buffer_load_dwordx4 v246, s[8:11], s43 offen lds\n"
"s_mov_b32 s12, s80\n"
"s_and_b32 s13, s81, 0xffff\n"
"v_mfma_f32_16x16x32_bf16 v[12:15], v[208:211], v[104:107], v[12:15]\n"
"ds_read_b128 v[76:79], v242 offset:20480\n"
"s_mov_b32 s14, 0xa00000\n"
"s_mov_b32 s15, 0x20000\n"
"s_waitcnt lgkmcnt(4)\n"
"v_mfma_f32_16x16x32_bf16 v[16:19], v[212:215], v[92:95], v[16:19]\n"
"v_add_u32 v241, s47, v241\n"
"ds_read_b64_tr_b16 v[220:221], v241 offset:17408\n"
"s_mov_b32 s16, s68\n"
"s_and_b32 s17, s69, 0xffff\n"
"v_mfma_f32_16x16x32_bf16 v[20:23], v[212:215], v[96:99], v[20:23]\n"
"ds_read_b64_tr_b16 v[222:223], v241 offset:21760\n"
"s_mov_b32 s18, 0xc8000\n"
"s_mov_b32 s19, 0x20000\n"
"v_mfma_f32_16x16x32_bf16 v[24:27], v[212:215], v[100:103], v[24:27]\n"
"ds_read_b128 v[80:83], v242 offset:22528\n"
"s_mov_b32 s20, s74\n"
"s_and_b32 s21, s75, 0xffff\n"
"v_mfma_f32_16x16x32_bf16 v[28:31], v[212:215], v[104:107], v[28:31]\n"
"ds_read_b128 v[84:87], v242 offset:24576\n"
"s_mov_b32 s22, 0x1e0000\n"
"s_mov_b32 s23, 0x20000\n"
"v_mfma_f32_16x16x32_bf16 v[64:67], v[212:215], v[108:111], v[64:67]\n"
"ds_read_b128 v[88:91], v244 offset:18432\n"
"s_mov_b32 s24, s78\n"
"s_and_b32 s25, s79, 0xffff\n"
"s_waitcnt lgkmcnt(7)\n"
"v_mfma_f32_16x16x32_bf16 v[32:35], v[92:95], v[216:219], v[32:35]\n"
"ds_read_b64_tr_b16 v[208:209], v240 offset:34816\n"
"s_mov_b32 s26, 0x280000\n"
"s_mov_b32 s27, 0x20000\n"
"s_sub_u32 s35, s34, 16\n"
"v_mfma_f32_16x16x32_bf16 v[36:39], v[96:99], v[216:219], v[36:39]\n"
"ds_read_b64_tr_b16 v[210:211], v240 offset:39168\n"
"s_lshr_b32 s91, s35, 1\n"
"s_and_b32 s35, s35, 1\n"
"v_mfma_f32_16x16x32_bf16 v[40:43], v[100:103], v[216:219], v[40:43]\n"
"s_add_u32 s44, s46, 0x979c0\n"
"buffer_load_dwordx4 v246, s[8:11], s44 offen offset:1088 lds\n"
"s_mul_i32 s35, s35, 6\n"
"s_lshl_b32 s90, s33, 4\n"
"v_mfma_f32_16x16x32_bf16 v[44:47], v[104:107], v[216:219], v[44:47]\n"
"s_add_u32 s90, s90, s35\n"
"s_add_u32 s90, s90, 4\n"
"s_waitcnt lgkmcnt(5)\n"
"v_mfma_f32_16x16x32_bf16 v[48:51], v[220:223], v[92:95], v[48:51]\n"
"ds_read_b64_tr_b16 v[212:213], v240 offset:35136\n"
"s_and_b32 s35, s32, 3\n"
"s_lshr_b32 s36, s32, 2\n"
"v_mfma_f32_16x16x32_bf16 v[52:55], v[220:223], v[96:99], v[52:55]\n"
"ds_read_b64_tr_b16 v[214:215], v240 offset:39488\n"
"s_lshl_b32 s91, s91, 7\n"
"s_mul_i32 s38, s36, 25600\n"
"v_mfma_f32_16x16x32_bf16 v[56:59], v[220:223], v[100:103], v[56:59]\n"
"s_mul_i32 s85, s35, 51200\n"
"s_add_u32 s85, s85, s38\n"
"s_add_u32 s85, s85, s91\n"
"v_mfma_f32_16x16x32_bf16 v[60:63], v[220:223], v[104:107], v[60:63]\n"
"s_mul_i32 s38, s36, 10240\n"
"s_add_u32 s38, s38, s91\n"
"v_mfma_f32_16x16x32_bf16 v[68:71], v[220:223], v[108:111], v[68:71]\n"
"s_mul_i32 s87, s35, 122880\n"
"s_add_u32 s87, s87, s38\n"
"s_waitcnt vmcnt(7)\n"
"s_barrier\n"
"s_waitcnt lgkmcnt(2)\n"
"v_mfma_f32_16x16x32_bf16 v[0:3], v[208:211], v[72:75], v[0:3]\n"
"ds_read_b64_tr_b16 v[216:217], v240 offset:35456\n"
"s_bitcmp1_b32 s35, 0\n"
"s_cselect_b32 s38, 40960, 0\n"
"v_mfma_f32_16x16x32_bf16 v[4:7], v[208:211], v[76:79], v[4:7]\n"
"ds_read_b64_tr_b16 v[218:219], v240 offset:39808\n"
"s_cmp_ge_u32 s32, 2\n"
"s_cbranch_scc1 .Lhx0_3004\n"
"s_add_u32 m0, s3, 0x1d800\n"
"s_mov_b32 s41, 384\n"
"buffer_load_dwordx4 v249, s[4:7], s41 offen lds\n"
".Lhx0_3004:\n"
"s_add_u32 s84, s87, s38\n"
"v_mfma_f32_16x16x32_bf16 v[8:11], v[208:211], v[80:83], v[8:11]\n"
"ds_read_b128 v[92:95], v243 offset:18432\n"
"s_bitcmp1_b32 s35, 0\n"
"s_cselect_b32 s38, 0, 81920\n"
"v_mfma_f32_16x16x32_bf16 v[12:15], v[208:211], v[84:87], v[12:15]\n"
"ds_read_b128 v[96:99], v243 offset:20480\n"
"s_add_u32 s87, s87, s38\n"
"s_lshl_b32 s86, s35, 1\n"
"s_waitcnt lgkmcnt(4)\n"
"v_mfma_f32_16x16x32_bf16 v[16:19], v[212:215], v[72:75], v[16:19]\n"
"v_add_u32 v241, s47, v241\n"
"ds_read_b64_tr_b16 v[220:221], v241 offset:34816\n"
"s_add_u32 m0, s50, 0x19800\n"
"s_add_u32 s41, s45, 384\n"
"buffer_load_dwordx4 v247, s[4:7], s41 offen lds\n"
"s_add_u32 s86, s86, s36\n"
"s_mul_i32 s86, s86, 102400\n"
"v_mfma_f32_16x16x32_bf16 v[20:23], v[212:215], v[76:79], v[20:23]\n"
"ds_read_b64_tr_b16 v[222:223], v241 offset:39168\n"
"s_add_u32 s86, s86, s91\n"
"s_add_u32 s86, s86, 20480\n"
"v_mfma_f32_16x16x32_bf16 v[24:27], v[212:215], v[80:83], v[24:27]\n"
"ds_read_b128 v[100:103], v243 offset:22528\n"
"s_lshl_b32 s88, s35, 12\n"
"s_lshl_b32 s37, s36, 11\n"
"v_mfma_f32_16x16x32_bf16 v[28:31], v[212:215], v[84:87], v[28:31]\n"
"ds_read_b128 v[104:107], v243 offset:24576\n"
"s_add_u32 s88, s88, s37\n"
"s_mul_i32 s37, s36, 3\n"
"v_mfma_f32_16x16x32_bf16 v[64:67], v[212:215], v[88:91], v[64:67]\n"
"ds_read_b128 v[108:111], v245 offset:18432\n"
"buffer_load_dwordx4 v248, s[4:7], s41 offen offset:1024 lds\n"
"s_add_u32 s90, s90, s37\n"
"s_mul_i32 s89, s90, 69632\n"
"s_waitcnt lgkmcnt(7)\n"
"v_mfma_f32_16x16x32_bf16 v[32:35], v[72:75], v[216:219], v[32:35]\n"
"v_add_u32 v240, 0xcc00, v240\n"
"ds_read_b64_tr_b16 v[208:209], v240 offset:0\n"
"s_lshl_b32 s37, s35, 10\n"
"s_add_u32 s89, s89, s37\n"
"v_lshrrev_b32 v120, 5, v127\n"
"v_mfma_f32_16x16x32_bf16 v[36:39], v[76:79], v[216:219], v[36:39]\n"
"ds_read_b64_tr_b16 v[210:211], v240 offset:4352\n"
"v_mul_u32_u24 v120, 4, v120\n"
"v_bfe_u32 v121, v127, 3, 2\n"
"v_mfma_f32_16x16x32_bf16 v[40:43], v[80:83], v[216:219], v[40:43]\n"
"s_add_u32 m0, s48, 0x4400\n"
"s_add_u32 s43, s46, 0xb4000\n"
"buffer_load_dwordx4 v246, s[8:11], s43 offen lds\n"
"v_add_u32 v120, v120, v121\n"
"v_mul_u32_u24 v120, 1280, v120\n"
"v_mfma_f32_16x16x32_bf16 v[44:47], v[84:87], v[216:219], v[44:47]\n"
"v_bfe_u32 v121, v127, 1, 2\n"
"v_lshrrev_b32 v122, 4, v127\n"
"s_waitcnt lgkmcnt(5)\n"
"v_mfma_f32_16x16x32_bf16 v[48:51], v[220:223], v[72:75], v[48:51]\n"
"ds_read_b64_tr_b16 v[212:213], v240 offset:320\n"
"v_xor_b32 v121, v121, v122\n"
"v_lshl_add_u32 v120, v121, 5, v120\n"
"v_mfma_f32_16x16x32_bf16 v[52:55], v[220:223], v[76:79], v[52:55]\n"
"ds_read_b64_tr_b16 v[214:215], v240 offset:4672\n"
"v_and_b32 v121, 1, v127\n"
"v_lshl_add_u32 v253, v121, 4, v120\n"
"v_mfma_f32_16x16x32_bf16 v[56:59], v[220:223], v[80:83], v[56:59]\n"
"s_add_u32 s44, s46, 0xb59c0\n"
"buffer_load_dwordx4 v246, s[8:11], s44 offen offset:1088 lds\n"
"v_lshrrev_b32 v120, 5, v127\n"
"v_mul_u32_u24 v120, 8, v120\n"
"v_mfma_f32_16x16x32_bf16 v[60:63], v[220:223], v[84:87], v[60:63]\n"
"v_bfe_u32 v121, v127, 3, 2\n"
"v_add_u32 v120, v120, v121\n"
"v_mfma_f32_16x16x32_bf16 v[68:71], v[220:223], v[88:91], v[68:71]\n"
"v_mul_u32_u24 v120, 1280, v120\n"
"v_bfe_u32 v121, v127, 1, 2\n"
"v_lshrrev_b32 v122, 4, v127\n"
"s_waitcnt vmcnt(6)\n"
"s_waitcnt lgkmcnt(4)\n"
"s_barrier\n"
"s_waitcnt lgkmcnt(2)\n"
"v_mfma_f32_16x16x32_bf16 v[0:3], v[208:211], v[92:95], v[0:3]\n"
"ds_read_b64_tr_b16 v[216:217], v240 offset:640\n"
"v_xor_b32 v121, v121, v122\n"
"v_mfma_f32_16x16x32_bf16 v[4:7], v[208:211], v[96:99], v[4:7]\n"
"ds_read_b64_tr_b16 v[218:219], v240 offset:4992\n"
"s_add_u32 m0, s48, 0x8800\n"
"s_add_u32 s43, s46, 0xd2000\n"
"buffer_load_dwordx4 v246, s[8:11], s43 offen lds\n"
"v_lshl_add_u32 v120, v121, 5, v120\n"
"v_and_b32 v121, 1, v127\n"
"v_mfma_f32_16x16x32_bf16 v[8:11], v[208:211], v[100:103], v[8:11]\n"
"ds_read_b128 v[72:75], v242 offset:36864\n"
"v_lshl_add_u32 v252, v121, 4, v120\n"
"v_lshrrev_b32 v120, 5, v127\n"
"v_mfma_f32_16x16x32_bf16 v[12:15], v[208:211], v[104:107], v[12:15]\n"
"ds_read_b128 v[76:79], v242 offset:38912\n"
"v_mul_u32_u24 v120, 40, v120\n"
"v_bfe_u32 v121, v127, 3, 2\n"
"s_waitcnt lgkmcnt(4)\n"
"v_mfma_f32_16x16x32_bf16 v[16:19], v[212:215], v[92:95], v[16:19]\n"
"v_add_u32 v241, s95, v241\n"
"ds_read_b64_tr_b16 v[220:221], v241 offset:0\n"
"v_add_u32 v120, v120, v121\n"
"v_mul_u32_u24 v120, 1280, v120\n"
"v_bfe_u32 v121, v127, 1, 2\n"
"v_mfma_f32_16x16x32_bf16 v[20:23], v[212:215], v[96:99], v[20:23]\n"
"ds_read_b64_tr_b16 v[222:223], v241 offset:4352\n"
"v_lshrrev_b32 v122, 4, v127\n"
"v_xor_b32 v121, v121, v122\n"
"v_mfma_f32_16x16x32_bf16 v[24:27], v[212:215], v[100:103], v[24:27]\n"
"ds_read_b128 v[80:83], v242 offset:40960\n"
"v_lshl_add_u32 v120, v121, 5, v120\n"
"v_and_b32 v121, 1, v127\n"
"v_mfma_f32_16x16x32_bf16 v[28:31], v[212:215], v[104:107], v[28:31]\n"
"ds_read_b128 v[84:87], v242 offset:43008\n"
"s_add_u32 s44, s46, 0xd39c0\n"
"buffer_load_dwordx4 v246, s[8:11], s44 offen offset:1088 lds\n"
"v_lshl_add_u32 v251, v121, 4, v120\n"
"s_lshr_b32 s54, s32, 2\n"
"v_mfma_f32_16x16x32_bf16 v[64:67], v[212:215], v[108:111], v[64:67]\n"
"ds_read_b128 v[88:91], v244 offset:36864\n"
"s_and_b32 s55, s32, 3\n"
"v_mov_b32 v224, 0x3f803f80\n"
"s_waitcnt lgkmcnt(7)\n"
"v_mfma_f32_16x16x32_bf16 v[32:35], v[92:95], v[216:219], v[32:35]\n"
"ds_read_b64_tr_b16 v[208:209], v240 offset:17408\n"
"v_mov_b32 v225, 0x3f803f80\n"
"v_mov_b32 v226, 0x3f803f80\n"
"v_mfma_f32_16x16x32_bf16 v[36:39], v[96:99], v[216:219], v[36:39]\n"
"ds_read_b64_tr_b16 v[210:211], v240 offset:21760\n"
"v_mov_b32 v227, 0x3f803f80\n"
"v_lshrrev_b32 v120, 1, v254\n"
"v_mfma_f32_16x16x32_bf16 v[40:43], v[100:103], v[216:219], v[40:43]\n"
"v_and_b32 v121, 15, v127\n"
"v_lshrrev_b32 v122, 4, v127\n"
"v_lshlrev_b32 v123, 5, v121\n"
"v_mfma_f32_16x16x32_bf16 v[44:47], v[104:107], v[216:219], v[44:47]\n"
"v_lshl_add_u32 v123, v122, 3, v123\n"
"s_lshl_b32 s35, s54, 11\n"
"s_waitcnt lgkmcnt(5)\n"
"v_mfma_f32_16x16x32_bf16 v[48:51], v[220:223], v[92:95], v[48:51]\n"
"ds_read_b64_tr_b16 v[212:213], v240 offset:17728\n"
"s_mov_b32 s94, 0x892000\n"
"buffer_load_dwordx2 v[112:113], off, s[12:15], s94 sc1\n"
"s_mov_b32 s36, 0x22600\n"
"s_mov_b32 s37, 0x27800\n"
"v_mfma_f32_16x16x32_bf16 v[52:55], v[220:223], v[96:99], v[52:55]\n"
"ds_read_b64_tr_b16 v[214:215], v240 offset:22080\n"
"s_cmp_eq_u32 s55, 1\n"
"s_cselect_b32 s36, 0x21600, s36\n"
"s_cselect_b32 s37, 0x21400, s37\n"
"v_mfma_f32_16x16x32_bf16 v[56:59], v[220:223], v[100:103], v[56:59]\n"
"s_cmp_eq_u32 s55, 2\n"
"s_cselect_b32 s36, 0x23800, s36\n"
"s_cselect_b32 s37, 0x23600, s37\n"
"s_cselect_b64 vcc, -1, 0\n"
"v_mfma_f32_16x16x32_bf16 v[60:63], v[220:223], v[104:107], v[60:63]\n"
"v_mfma_f32_16x16x32_bf16 v[68:71], v[220:223], v[108:111], v[68:71]\n"
"v_cndmask_b32 v124, v120, v123, vcc\n"
"s_add_u32 s36, s36, s35\n"
"s_waitcnt vmcnt(7)\n"
"s_barrier\n"
"s_waitcnt lgkmcnt(2)\n"
"v_mfma_f32_16x16x32_bf16 v[0:3], v[208:211], v[72:75], v[0:3]\n"
"ds_read_b64_tr_b16 v[216:217], v240 offset:18048\n"
"s_cmp_eq_u32 s55, 3\n"
"s_cselect_b32 s36, 0x27800, s36\n"
"v_mfma_f32_16x16x32_bf16 v[4:7], v[208:211], v[76:79], v[4:7]\n"
"ds_read_b64_tr_b16 v[218:219], v240 offset:22400\n"
"s_cmp_ge_u32 s32, 2\n"
"s_cbranch_scc1 .Lhx0_3005\n"
"s_add_u32 m0, s3, 0x22000\n"
"s_mov_b32 s41, 512\n"
"buffer_load_dwordx4 v249, s[4:7], s41 offen lds\n"
".Lhx0_3005:\n"
"v_add_u32 v156, s36, v124\n"
"v_mfma_f32_16x16x32_bf16 v[8:11], v[208:211], v[80:83], v[8:11]\n"
"ds_read_b128 v[92:95], v243 offset:36864\n"
"v_add_u32 v157, s37, v124\n"
"s_mul_i32 s36, s55, 3072\n"
"v_mfma_f32_16x16x32_bf16 v[12:15], v[208:211], v[84:87], v[12:15]\n"
"ds_read_b128 v[96:99], v243 offset:38912\n"
"s_add_u32 s36, s36, 0x24800\n"
"v_add_u32 v158, s36, v254\n"
"s_waitcnt lgkmcnt(4)\n"
"v_mfma_f32_16x16x32_bf16 v[16:19], v[212:215], v[72:75], v[16:19]\n"
"v_add_u32 v241, s47, v241\n"
"ds_read_b64_tr_b16 v[220:221], v241 offset:17408\n"
"s_add_u32 m0, s50, 0x1e000\n"
"s_add_u32 s41, s45, 512\n"
"buffer_load_dwordx4 v247, s[4:7], s41 offen lds\n"
"s_add_u32 s37, s36, 1024\n"
"s_cmp_eq_u32 s54, 0\n"
"s_cselect_b32 s37, s37, 0x27800\n"
"v_mfma_f32_16x16x32_bf16 v[20:23], v[212:215], v[76:79], v[20:23]\n"
"ds_read_b64_tr_b16 v[222:223], v241 offset:21760\n"
"v_add_u32 v159, s37, v254\n"
"s_lshl_b32 s37, s54, 10\n"
"v_mfma_f32_16x16x32_bf16 v[24:27], v[212:215], v[80:83], v[24:27]\n"
"ds_read_b128 v[100:103], v243 offset:40960\n"
"v_add_u32 v160, s37, v158\n"
"v_add_u32 v161, s37, v160\n"
"v_mfma_f32_16x16x32_bf16 v[28:31], v[212:215], v[84:87], v[28:31]\n"
"ds_read_b128 v[104:107], v243 offset:43008\n"
"s_add_u32 s36, s35, 0x21600\n"
"v_mfma_f32_16x16x32_bf16 v[64:67], v[212:215], v[88:91], v[64:67]\n"
"ds_read_b128 v[108:111], v245 offset:36864\n"
"buffer_load_dwordx4 v248, s[4:7], s41 offen offset:1024 lds\n"
"v_add_u32 v162, s36, v120\n"
"s_add_u32 s36, s37, 0x21400\n"
"s_waitcnt lgkmcnt(7)\n"
"v_mfma_f32_16x16x32_bf16 v[32:35], v[72:75], v[216:219], v[32:35]\n"
"v_add_u32 v240, 0xffff3400, v240\n"
"ds_read_b64_tr_b16 v[208:209], v240 offset:0\n"
"v_add_u32 v163, s36, v120\n"
"v_add_u32 v164, s37, v163\n"
"v_lshrrev_b32 v124, 2, v127\n"
"v_mfma_f32_16x16x32_bf16 v[36:39], v[76:79], v[216:219], v[36:39]\n"
"ds_read_b64_tr_b16 v[210:211], v240 offset:4352\n"
"v_lshlrev_b32 v124, 5, v124\n"
"s_lshl_b32 s38, s55, 3\n"
"v_mfma_f32_16x16x32_bf16 v[40:43], v[80:83], v[216:219], v[40:43]\n"
"s_add_u32 m0, s48, 0xcc00\n"
"s_add_u32 s43, s46, 0xf0000\n"
"buffer_load_dwordx4 v246, s[8:11], s43 offen lds\n"
"s_add_u32 s36, s35, s38\n"
"s_add_u32 s36, s36, 0x23800\n"
"v_mfma_f32_16x16x32_bf16 v[44:47], v[84:87], v[216:219], v[44:47]\n"
"v_add_u32 v165, s36, v124\n"
"s_add_u32 s36, s37, s38\n"
"s_waitcnt lgkmcnt(5)\n"
"v_mfma_f32_16x16x32_bf16 v[48:51], v[220:223], v[72:75], v[48:51]\n"
"ds_read_b64_tr_b16 v[212:213], v240 offset:320\n"
"s_add_u32 s36, s36, 0x23600\n"
"v_add_u32 v166, s36, v124\n"
"v_mfma_f32_16x16x32_bf16 v[52:55], v[220:223], v[76:79], v[52:55]\n"
"ds_read_b64_tr_b16 v[214:215], v240 offset:4672\n"
"v_add_u32 v167, s37, v166\n"
"v_and_b32 v123, 1, v122\n"
"v_mfma_f32_16x16x32_bf16 v[56:59], v[220:223], v[80:83], v[56:59]\n"
"s_add_u32 s44, s46, 0xf19c0\n"
"buffer_load_dwordx4 v246, s[8:11], s44 offen offset:1088 lds\n"
"v_mul_u32_u24 v123, 0x11000, v123\n"
"v_lshrrev_b32 v124, 1, v122\n"
"v_mfma_f32_16x16x32_bf16 v[60:63], v[220:223], v[84:87], v[60:63]\n"
"v_lshl_add_u32 v124, v124, 4, v121\n"
"v_lshl_add_u32 v168, v124, 4, v123\n"
"v_mfma_f32_16x16x32_bf16 v[68:71], v[220:223], v[88:91], v[68:71]\n"
"v_mul_u32_u24 v123, 0x11000, v122\n"
"v_lshl_add_u32 v169, v121, 4, v123\n"
"s_lshl_b32 s35, s39, 3\n"
"s_waitcnt vmcnt(7)\n"
"s_waitcnt lgkmcnt(4)\n"
"s_barrier\n"
"s_waitcnt lgkmcnt(2)\n"
"v_mfma_f32_16x16x32_bf16 v[0:3], v[208:211], v[92:95], v[0:3]\n"
"ds_read_b64_tr_b16 v[216:217], v240 offset:640\n"
"s_lshl_b32 s36, s54, 2\n"
"v_mfma_f32_16x16x32_bf16 v[4:7], v[208:211], v[96:99], v[4:7]\n"
"ds_read_b64_tr_b16 v[218:219], v240 offset:4992\n"
"s_add_u32 s35, s35, s36\n"
"s_mul_i32 s35, s35, 69632\n"
"v_mfma_f32_16x16x32_bf16 v[8:11], v[208:211], v[100:103], v[8:11]\n"
"ds_read_b128 v[72:75], v242 offset:0\n"
"s_add_u32 m0, s48, 0x11000\n"
"s_add_u32 s43, s46, 0x10e000\n"
"buffer_load_dwordx4 v246, s[8:11], s43 offen lds\n"
"s_lshl_b32 s36, s40, 2\n"
"s_add_u32 s36, s36, s55\n"
"v_mfma_f32_16x16x32_bf16 v[12:15], v[208:211], v[104:107], v[12:15]\n"
"ds_read_b128 v[76:79], v242 offset:2048\n"
"s_lshl_b32 s37, s36, 9\n"
"s_add_u32 s51, s35, s37\n"
"s_waitcnt lgkmcnt(4)\n"
"v_mfma_f32_16x16x32_bf16 v[16:19], v[212:215], v[92:95], v[16:19]\n"
"v_add_u32 v241, s99, v241\n"
"ds_read_b64_tr_b16 v[220:221], v241 offset:0\n"
"s_add_u32 s51, s51, 0x8000\n"
"s_lshr_b32 s37, s36, 1\n"
"v_mfma_f32_16x16x32_bf16 v[20:23], v[212:215], v[96:99], v[20:23]\n"
"ds_read_b64_tr_b16 v[222:223], v241 offset:4352\n"
"s_lshl_b32 s37, s37, 8\n"
"s_and_b32 s36, s36, 1\n"
"v_mfma_f32_16x16x32_bf16 v[24:27], v[212:215], v[100:103], v[24:27]\n"
"ds_read_b128 v[80:83], v242 offset:4096\n"
"s_lshl_b32 s36, s36, 3\n"
"s_add_u32 s37, s37, s36\n"
"v_mfma_f32_16x16x32_bf16 v[28:31], v[212:215], v[104:107], v[28:31]\n"
"ds_read_b128 v[84:87], v242 offset:6144\n"
"s_add_u32 s42, s35, s37\n"
"s_add_u32 s42, s42, 0xc000\n"
"v_mfma_f32_16x16x32_bf16 v[64:67], v[212:215], v[108:111], v[64:67]\n"
"ds_read_b128 v[88:91], v244 offset:0\n"
"v_lshlrev_b32 v122, 2, v122\n"
"v_sub_u32 v123, v121, v122\n"
"s_waitcnt lgkmcnt(7)\n"
"v_mfma_f32_16x16x32_bf16 v[32:35], v[92:95], v[216:219], v[32:35]\n"
"ds_read_b64_tr_b16 v[208:209], v240 offset:17408\n"
"v_add_u32 v124, 9, v123\n"
"v_lshlrev_b32 v125, 1, v121\n"
"v_sub_u32 v125, v125, v122\n"
"v_mfma_f32_16x16x32_bf16 v[36:39], v[96:99], v[216:219], v[36:39]\n"
"ds_read_b64_tr_b16 v[210:211], v240 offset:21760\n"
"v_add_u32 v125, 2, v125\n"
"s_cmp_eq_u32 s54, 0\n"
"s_cselect_b64 vcc, -1, 0\n"
"s_cselect_b32 s35, 0xff800000, 0\n"
"v_mfma_f32_16x16x32_bf16 v[40:43], v[100:103], v[216:219], v[40:43]\n"
"s_add_u32 s44, s46, 0x10f9c0\n"
"buffer_load_dwordx4 v246, s[8:11], s44 offen offset:1088 lds\n"
"v_cndmask_b32 v125, v124, v125, vcc\n"
"v_mfma_f32_16x16x32_bf16 v[44:47], v[104:107], v[216:219], v[44:47]\n"
"s_or_b32 s38, s54, s39\n"
"s_cmp_eq_u32 s38, 0\n"
"s_cselect_b32 s38, 0xcf8547b1, 0\n"
"s_waitcnt lgkmcnt(5)\n"
"v_mfma_f32_16x16x32_bf16 v[48:51], v[220:223], v[92:95], v[48:51]\n"
"ds_read_b64_tr_b16 v[212:213], v240 offset:17728\n"
"s_xor_b32 s37, s54, 1\n"
"v_mfma_f32_16x16x32_bf16 v[52:55], v[220:223], v[96:99], v[52:55]\n"
"ds_read_b64_tr_b16 v[214:215], v240 offset:22080\n"
"v_mov_b32 v120, 0xff800000\n"
"v_mov_b32 v121, s38\n"
"v_mfma_f32_16x16x32_bf16 v[56:59], v[220:223], v[100:103], v[56:59]\n"
"v_subrev_u32 v126, 0, v123\n"
"v_cmp_le_u32 vcc, 8, v126\n"
"v_mfma_f32_16x16x32_bf16 v[60:63], v[220:223], v[104:107], v[60:63]\n"
"s_nop 0\n"
"v_cndmask_b32 v228, 0, v120, vcc\n"
"v_cmp_lt_i32 vcc, 0, v124\n"
"v_mfma_f32_16x16x32_bf16 v[68:71], v[220:223], v[108:111], v[68:71]\n"
"s_nop 0\n"
"v_cndmask_b32 v232, 0, v120, vcc\n"
"v_cmp_lt_i32 vcc, 0, v125\n"
"s_waitcnt vmcnt(7)\n"
"s_barrier\n"
"s_waitcnt vmcnt(6)\n"
"v_readfirstlane_b32 s58, v112\n"
"v_readfirstlane_b32 s59, v113\n"
"s_nop 0\n"
"s_mul_i32 s56, s58, 0x7F4A7C15\n"
"s_mul_hi_u32 s57, s58, 0x7F4A7C15\n"
"s_mul_i32 s58, s58, 0x9E3779B9\n"
"s_add_u32 s57, s57, s58\n"
"s_mul_i32 s59, s59, 0x7F4A7C15\n"
"s_add_u32 s57, s57, s59\n"
"s_add_u32 s56, s56, 0xD9B4E019\n"
"s_addc_u32 s57, s57, 0x632BE59B\n"
"s_waitcnt lgkmcnt(2)\n"
"v_mfma_f32_16x16x32_bf16 v[0:3], v[208:211], v[72:75], v[0:3]\n"
"ds_read_b64_tr_b16 v[216:217], v240 offset:18048\n"
"v_cndmask_b32 v236, v121, v120, vcc\n"
"v_mfma_f32_16x16x32_bf16 v[4:7], v[208:211], v[76:79], v[4:7]\n"
"ds_read_b64_tr_b16 v[218:219], v240 offset:22400\n"
"s_cmp_ge_u32 s32, 2\n"
"s_cbranch_scc1 .Lhx0_3006\n"
"s_add_u32 m0, s3, 0x26800\n"
"s_mov_b32 s41, 640\n"
"buffer_load_dwordx4 v249, s[4:7], s41 offen lds\n"
".Lhx0_3006:\n"
"v_add_f32 v152, s35, v236\n"
"v_subrev_u32 v126, 1, v123\n"
"v_mfma_f32_16x16x32_bf16 v[8:11], v[208:211], v[80:83], v[8:11]\n"
"ds_read_b128 v[92:95], v243 offset:0\n"
"v_cmp_le_u32 vcc, 8, v126\n"
"s_nop 1\n"
"v_cndmask_b32 v229, 0, v120, vcc\n"
"v_mfma_f32_16x16x32_bf16 v[12:15], v[208:211], v[84:87], v[12:15]\n"
"ds_read_b128 v[96:99], v243 offset:2048\n"
"v_cmp_lt_i32 vcc, 1, v124\n"
"s_nop 1\n"
"v_cndmask_b32 v233, 0, v120, vcc\n"
"s_waitcnt lgkmcnt(4)\n"
"v_mfma_f32_16x16x32_bf16 v[16:19], v[212:215], v[72:75], v[16:19]\n"
"v_add_u32 v241, s47, v241\n"
"ds_read_b64_tr_b16 v[220:221], v241 offset:17408\n"
"s_add_u32 m0, s50, 0x22800\n"
"s_add_u32 s41, s45, 640\n"
"buffer_load_dwordx4 v247, s[4:7], s41 offen lds\n"
"v_add_u32 v126, s37, v125\n"
"v_cmp_lt_i32 vcc, 1, v126\n"
"s_nop 1\n"
"v_cndmask_b32 v153, v121, v120, vcc\n"
"v_mfma_f32_16x16x32_bf16 v[20:23], v[212:215], v[76:79], v[20:23]\n"
"ds_read_b64_tr_b16 v[222:223], v241 offset:21760\n"
"v_add_f32 v237, s35, v153\n"
"v_subrev_u32 v126, 2, v123\n"
"v_mfma_f32_16x16x32_bf16 v[24:27], v[212:215], v[80:83], v[24:27]\n"
"ds_read_b128 v[100:103], v243 offset:4096\n"
"v_cmp_le_u32 vcc, 8, v126\n"
"s_nop 1\n"
"v_cndmask_b32 v230, 0, v120, vcc\n"
"v_mfma_f32_16x16x32_bf16 v[28:31], v[212:215], v[84:87], v[28:31]\n"
"ds_read_b128 v[104:107], v243 offset:6144\n"
"v_cmp_lt_i32 vcc, 2, v124\n"
"s_nop 1\n"
"v_cndmask_b32 v234, 0, v120, vcc\n"
"v_mfma_f32_16x16x32_bf16 v[64:67], v[212:215], v[88:91], v[64:67]\n"
"ds_read_b128 v[108:111], v245 offset:0\n"
"buffer_load_dwordx4 v248, s[4:7], s41 offen offset:1024 lds\n"
"v_cmp_lt_i32 vcc, 2, v125\n"
"s_nop 1\n"
"v_cndmask_b32 v238, v121, v120, vcc\n"
"s_waitcnt lgkmcnt(7)\n"
"v_mfma_f32_16x16x32_bf16 v[32:35], v[72:75], v[216:219], v[32:35]\n"
"ds_read_b64_tr_b16 v[208:209], v240 offset:34816\n"
"v_add_f32 v154, s35, v238\n"
"v_subrev_u32 v126, 3, v123\n"
"v_mfma_f32_16x16x32_bf16 v[36:39], v[76:79], v[216:219], v[36:39]\n"
"ds_read_b64_tr_b16 v[210:211], v240 offset:39168\n"
"v_cmp_le_u32 vcc, 8, v126\n"
"s_nop 1\n"
"v_cndmask_b32 v231, 0, v120, vcc\n"
"v_mfma_f32_16x16x32_bf16 v[40:43], v[80:83], v[216:219], v[40:43]\n"
"s_add_u32 m0, s48, 0x0\n"
"s_add_u32 s43, s46, 0x12c000\n"
"buffer_load_dwordx4 v246, s[8:11], s43 offen lds\n"
"v_cmp_lt_i32 vcc, 3, v124\n"
"s_nop 1\n"
"v_cndmask_b32 v235, 0, v120, vcc\n"
"v_mfma_f32_16x16x32_bf16 v[44:47], v[84:87], v[216:219], v[44:47]\n"
"v_add_u32 v126, s37, v125\n"
"v_cmp_lt_i32 vcc, 3, v126\n"
"s_waitcnt lgkmcnt(5)\n"
"v_mfma_f32_16x16x32_bf16 v[48:51], v[220:223], v[72:75], v[48:51]\n"
"ds_read_b64_tr_b16 v[212:213], v240 offset:35136\n"
"v_cndmask_b32 v155, v121, v120, vcc\n"
"v_add_f32 v239, s35, v155\n"
"s_lshl_b32 s37, s39, 1\n"
"v_mfma_f32_16x16x32_bf16 v[52:55], v[220:223], v[76:79], v[52:55]\n"
"ds_read_b64_tr_b16 v[214:215], v240 offset:39488\n"
"s_add_u32 s37, s37, s54\n"
"s_lshl_b32 s37, s37, 8\n"
"v_mfma_f32_16x16x32_bf16 v[56:59], v[220:223], v[80:83], v[56:59]\n"
"s_add_u32 s44, s46, 0x12d9c0\n"
"buffer_load_dwordx4 v246, s[8:11], s44 offen offset:1088 lds\n"
"s_add_u32 s49, s37, 0x880000\n"
"s_xor_b32 s38, s55, 3\n"
"v_mfma_f32_16x16x32_bf16 v[60:63], v[220:223], v[84:87], v[60:63]\n"
"s_or_b32 s38, s38, s40\n"
"s_cmp_eq_u32 s38, 0\n"
"s_cselect_b32 s49, s49, 0\n"
"v_mfma_f32_16x16x32_bf16 v[68:71], v[220:223], v[88:91], v[68:71]\n"
"v_lshrrev_b32 v170, 2, v254\n"
"s_mov_b32 s53, 0x3ea52b5c\n"
"s_waitcnt vmcnt(6)\n"
"s_waitcnt lgkmcnt(4)\n"
"s_barrier\n"
"s_waitcnt lgkmcnt(2)\n"
"v_mfma_f32_16x16x32_bf16 v[0:3], v[208:211], v[92:95], v[0:3]\n"
"ds_read_b64_tr_b16 v[216:217], v240 offset:35456\n"
"v_mfma_f32_16x16x32_bf16 v[4:7], v[208:211], v[96:99], v[4:7]\n"
"ds_read_b64_tr_b16 v[218:219], v240 offset:39808\n"
"v_mfma_f32_16x16x32_bf16 v[8:11], v[208:211], v[100:103], v[8:11]\n"
"ds_read_b128 v[72:75], v242 offset:18432\n"
"s_add_u32 m0, s48, 0x4400\n"
"s_add_u32 s43, s46, 0x14a000\n"
"buffer_load_dwordx4 v246, s[8:11], s43 offen lds\n"
"v_mfma_f32_16x16x32_bf16 v[12:15], v[208:211], v[104:107], v[12:15]\n"
"ds_read_b128 v[76:79], v242 offset:20480\n"
"s_waitcnt lgkmcnt(4)\n"
"v_mfma_f32_16x16x32_bf16 v[16:19], v[212:215], v[92:95], v[16:19]\n"
"v_add_u32 v241, s47, v241\n"
"ds_read_b64_tr_b16 v[220:221], v241 offset:34816\n"
"v_mfma_f32_16x16x32_bf16 v[20:23], v[212:215], v[96:99], v[20:23]\n"
"ds_read_b64_tr_b16 v[222:223], v241 offset:39168\n"
"v_mfma_f32_16x16x32_bf16 v[24:27], v[212:215], v[100:103], v[24:27]\n"
"ds_read_b128 v[80:83], v242 offset:22528\n"
"v_mfma_f32_16x16x32_bf16 v[28:31], v[212:215], v[104:107], v[28:31]\n"
"ds_read_b128 v[84:87], v242 offset:24576\n"
"v_mfma_f32_16x16x32_bf16 v[64:67], v[212:215], v[108:111], v[64:67]\n"
"ds_read_b128 v[88:91], v244 offset:18432\n"
"s_waitcnt lgkmcnt(7)\n"
"v_mfma_f32_16x16x32_bf16 v[32:35], v[92:95], v[216:219], v[32:35]\n"
"v_add_u32 v240, 0xcc00, v240\n"
"ds_read_b64_tr_b16 v[208:209], v240 offset:0\n"
"v_mfma_f32_16x16x32_bf16 v[36:39], v[96:99], v[216:219], v[36:39]\n"
"ds_read_b64_tr_b16 v[210:211], v240 offset:4352\n"
"v_mfma_f32_16x16x32_bf16 v[40:43], v[100:103], v[216:219], v[40:43]\n"
"s_add_u32 s44, s46, 0x14b9c0\n"
"buffer_load_dwordx4 v246, s[8:11], s44 offen offset:1088 lds\n"
"v_mfma_f32_16x16x32_bf16 v[44:47], v[104:107], v[216:219], v[44:47]\n"
"s_waitcnt lgkmcnt(5)\n"
"v_mfma_f32_16x16x32_bf16 v[48:51], v[220:223], v[92:95], v[48:51]\n"
"ds_read_b64_tr_b16 v[212:213], v240 offset:320\n"
"v_mfma_f32_16x16x32_bf16 v[52:55], v[220:223], v[96:99], v[52:55]\n"
"ds_read_b64_tr_b16 v[214:215], v240 offset:4672\n"
"v_mfma_f32_16x16x32_bf16 v[56:59], v[220:223], v[100:103], v[56:59]\n"
"v_mfma_f32_16x16x32_bf16 v[60:63], v[220:223], v[104:107], v[60:63]\n"
"v_mfma_f32_16x16x32_bf16 v[68:71], v[220:223], v[108:111], v[68:71]\n"
"s_waitcnt vmcnt(6)\n"
"s_barrier\n"
"s_waitcnt lgkmcnt(2)\n"
"v_mfma_f32_16x16x32_bf16 v[0:3], v[208:211], v[72:75], v[0:3]\n"
"ds_read_b64_tr_b16 v[216:217], v240 offset:640\n"
"v_mfma_f32_16x16x32_bf16 v[4:7], v[208:211], v[76:79], v[4:7]\n"
"ds_read_b64_tr_b16 v[218:219], v240 offset:4992\n"
"s_cmp_ge_u32 s32, 2\n"
"s_cbranch_scc1 .Lhx0_3007\n"
"s_add_u32 m0, s3, 0x1d800\n"
"s_mov_b32 s41, 768\n"
"buffer_load_dwordx4 v249, s[4:7], s41 offen lds\n"
".Lhx0_3007:\n"
"v_mfma_f32_16x16x32_bf16 v[8:11], v[208:211], v[80:83], v[8:11]\n"
"ds_read_b128 v[92:95], v243 offset:18432\n"
"v_mfma_f32_16x16x32_bf16 v[12:15], v[208:211], v[84:87], v[12:15]\n"
"ds_read_b128 v[96:99], v243 offset:20480\n"
"s_waitcnt lgkmcnt(4)\n"
"v_mfma_f32_16x16x32_bf16 v[16:19], v[212:215], v[72:75], v[16:19]\n"
"v_add_u32 v241, s95, v241\n"
"ds_read_b64_tr_b16 v[220:221], v241 offset:0\n"
"s_add_u32 m0, s50, 0x19800\n"
"s_add_u32 s41, s45, 768\n"
"buffer_load_dwordx4 v247, s[4:7], s41 offen lds\n"
"v_mfma_f32_16x16x32_bf16 v[20:23], v[212:215], v[76:79], v[20:23]\n"
"ds_read_b64_tr_b16 v[222:223], v241 offset:4352\n"
"v_mfma_f32_16x16x32_bf16 v[24:27], v[212:215], v[80:83], v[24:27]\n"
"ds_read_b128 v[100:103], v243 offset:22528\n"
"v_mfma_f32_16x16x32_bf16 v[28:31], v[212:215], v[84:87], v[28:31]\n"
"ds_read_b128 v[104:107], v243 offset:24576\n"
"v_mfma_f32_16x16x32_bf16 v[64:67], v[212:215], v[88:91], v[64:67]\n"
"ds_read_b128 v[108:111], v245 offset:18432\n"
"buffer_load_dwordx4 v248, s[4:7], s41 offen offset:1024 lds\n"
"s_waitcnt lgkmcnt(7)\n"
"v_mfma_f32_16x16x32_bf16 v[32:35], v[72:75], v[216:219], v[32:35]\n"
"ds_read_b64_tr_b16 v[208:209], v240 offset:17408\n"
"v_mfma_f32_16x16x32_bf16 v[36:39], v[76:79], v[216:219], v[36:39]\n"
"ds_read_b64_tr_b16 v[210:211], v240 offset:21760\n"
"v_mfma_f32_16x16x32_bf16 v[40:43], v[80:83], v[216:219], v[40:43]\n"
"s_add_u32 m0, s48, 0x8800\n"
"s_add_u32 s43, s46, 0x168000\n"
"buffer_load_dwordx4 v246, s[8:11], s43 offen lds\n"
"v_mfma_f32_16x16x32_bf16 v[44:47], v[84:87], v[216:219], v[44:47]\n"
"s_waitcnt lgkmcnt(5)\n"
"v_mfma_f32_16x16x32_bf16 v[48:51], v[220:223], v[72:75], v[48:51]\n"
"ds_read_b64_tr_b16 v[212:213], v240 offset:17728\n"
"v_mfma_f32_16x16x32_bf16 v[52:55], v[220:223], v[76:79], v[52:55]\n"
"ds_read_b64_tr_b16 v[214:215], v240 offset:22080\n"
"v_mfma_f32_16x16x32_bf16 v[56:59], v[220:223], v[80:83], v[56:59]\n"
"s_add_u32 s44, s46, 0x1699c0\n"
"buffer_load_dwordx4 v246, s[8:11], s44 offen offset:1088 lds\n"
"v_mfma_f32_16x16x32_bf16 v[60:63], v[220:223], v[84:87], v[60:63]\n"
"v_mfma_f32_16x16x32_bf16 v[68:71], v[220:223], v[88:91], v[68:71]\n"
"s_waitcnt vmcnt(6)\n"
"s_waitcnt lgkmcnt(4)\n"
"s_barrier\n"
"s_waitcnt lgkmcnt(2)\n"
"v_mfma_f32_16x16x32_bf16 v[0:3], v[208:211], v[92:95], v[0:3]\n"
"ds_read_b64_tr_b16 v[216:217], v240 offset:18048\n"
"v_mfma_f32_16x16x32_bf16 v[4:7], v[208:211], v[96:99], v[4:7]\n"
"ds_read_b64_tr_b16 v[218:219], v240 offset:22400\n"
"v_mfma_f32_16x16x32_bf16 v[8:11], v[208:211], v[100:103], v[8:11]\n"
"ds_read_b128 v[72:75], v242 offset:36864\n"
"s_add_u32 m0, s48, 0xcc00\n"
"s_add_u32 s43, s46, 0x186000\n"
"buffer_load_dwordx4 v246, s[8:11], s43 offen lds\n"
"v_mfma_f32_16x16x32_bf16 v[12:15], v[208:211], v[104:107], v[12:15]\n"
"ds_read_b128 v[76:79], v242 offset:38912\n"
"s_waitcnt lgkmcnt(4)\n"
"v_mfma_f32_16x16x32_bf16 v[16:19], v[212:215], v[92:95], v[16:19]\n"
"v_add_u32 v241, s47, v241\n"
"ds_read_b64_tr_b16 v[220:221], v241 offset:17408\n"
"v_mfma_f32_16x16x32_bf16 v[20:23], v[212:215], v[96:99], v[20:23]\n"
"ds_read_b64_tr_b16 v[222:223], v241 offset:21760\n"
"v_mfma_f32_16x16x32_bf16 v[24:27], v[212:215], v[100:103], v[24:27]\n"
"ds_read_b128 v[80:83], v242 offset:40960\n"
"v_mfma_f32_16x16x32_bf16 v[28:31], v[212:215], v[104:107], v[28:31]\n"
"ds_read_b128 v[84:87], v242 offset:43008\n"
"v_mfma_f32_16x16x32_bf16 v[64:67], v[212:215], v[108:111], v[64:67]\n"
"ds_read_b128 v[88:91], v244 offset:36864\n"
"s_waitcnt lgkmcnt(7)\n"
"v_mfma_f32_16x16x32_bf16 v[32:35], v[92:95], v[216:219], v[32:35]\n"
"v_add_u32 v240, 0xffff3400, v240\n"
"ds_read_b64_tr_b16 v[208:209], v240 offset:0\n"
"v_mfma_f32_16x16x32_bf16 v[36:39], v[96:99], v[216:219], v[36:39]\n"
"ds_read_b64_tr_b16 v[210:211], v240 offset:4352\n"
"v_mfma_f32_16x16x32_bf16 v[40:43], v[100:103], v[216:219], v[40:43]\n"
"s_add_u32 s44, s46, 0x1879c0\n"
"buffer_load_dwordx4 v246, s[8:11], s44 offen offset:1088 lds\n"
"v_mfma_f32_16x16x32_bf16 v[44:47], v[104:107], v[216:219], v[44:47]\n"
"s_waitcnt lgkmcnt(5)\n"
"v_mfma_f32_16x16x32_bf16 v[48:51], v[220:223], v[92:95], v[48:51]\n"
"ds_read_b64_tr_b16 v[212:213], v240 offset:320\n"
"v_mfma_f32_16x16x32_bf16 v[52:55], v[220:223], v[96:99], v[52:55]\n"
"ds_read_b64_tr_b16 v[214:215], v240 offset:4672\n"
"v_mfma_f32_16x16x32_bf16 v[56:59], v[220:223], v[100:103], v[56:59]\n"
"v_mfma_f32_16x16x32_bf16 v[60:63], v[220:223], v[104:107], v[60:63]\n"
"v_mfma_f32_16x16x32_bf16 v[68:71], v[220:223], v[108:111], v[68:71]\n"
"s_waitcnt vmcnt(6)\n"
"s_barrier\n"
"s_waitcnt lgkmcnt(2)\n"
"v_mfma_f32_16x16x32_bf16 v[0:3], v[208:211], v[72:75], v[0:3]\n"
"ds_read_b64_tr_b16 v[216:217], v240 offset:640\n"
"v_mfma_f32_16x16x32_bf16 v[4:7], v[208:211], v[76:79], v[4:7]\n"
"ds_read_b64_tr_b16 v[218:219], v240 offset:4992\n"
"s_cmp_ge_u32 s32, 2\n"
"s_cbranch_scc1 .Lhx0_3008\n"
"s_add_u32 m0, s3, 0x22000\n"
"s_mov_b32 s41, 896\n"
"buffer_load_dwordx4 v249, s[4:7], s41 offen lds\n"
".Lhx0_3008:\n"
"v_mfma_f32_16x16x32_bf16 v[8:11], v[208:211], v[80:83], v[8:11]\n"
"ds_read_b128 v[92:95], v243 offset:36864\n"
"v_mfma_f32_16x16x32_bf16 v[12:15], v[208:211], v[84:87], v[12:15]\n"
"ds_read_b128 v[96:99], v243 offset:38912\n"
"s_waitcnt lgkmcnt(4)\n"
"v_mfma_f32_16x16x32_bf16 v[16:19], v[212:215], v[72:75], v[16:19]\n"
"v_add_u32 v241, s99, v241\n"
"ds_read_b64_tr_b16 v[220:221], v241 offset:0\n"
"s_add_u32 m0, s50, 0x1e000\n"
"s_add_u32 s41, s45, 896\n"
"buffer_load_dwordx4 v247, s[4:7], s41 offen lds\n"
"v_mfma_f32_16x16x32_bf16 v[20:23], v[212:215], v[76:79], v[20:23]\n"
"ds_read_b64_tr_b16 v[222:223], v241 offset:4352\n"
"v_mfma_f32_16x16x32_bf16 v[24:27], v[212:215], v[80:83], v[24:27]\n"
"ds_read_b128 v[100:103], v243 offset:40960\n"
"v_mfma_f32_16x16x32_bf16 v[28:31], v[212:215], v[84:87], v[28:31]\n"
"ds_read_b128 v[104:107], v243 offset:43008\n"
"v_mfma_f32_16x16x32_bf16 v[64:67], v[212:215], v[88:91], v[64:67]\n"
"ds_read_b128 v[108:111], v245 offset:36864\n"
"buffer_load_dwordx4 v248, s[4:7], s41 offen offset:1024 lds\n"
"s_waitcnt lgkmcnt(7)\n"
"v_mfma_f32_16x16x32_bf16 v[32:35], v[72:75], v[216:219], v[32:35]\n"
"ds_read_b64_tr_b16 v[208:209], v240 offset:17408\n"
"v_mfma_f32_16x16x32_bf16 v[36:39], v[76:79], v[216:219], v[36:39]\n"
"ds_read_b64_tr_b16 v[210:211], v240 offset:21760\n"
"v_mfma_f32_16x16x32_bf16 v[40:43], v[80:83], v[216:219], v[40:43]\n"
"s_add_u32 m0, s48, 0x11000\n"
"s_add_u32 s43, s46, 0x1a4000\n"
"buffer_load_dwordx4 v246, s[8:11], s43 offen lds\n"
"v_mfma_f32_16x16x32_bf16 v[44:47], v[84:87], v[216:219], v[44:47]\n"
"s_waitcnt lgkmcnt(5)\n"
"v_mfma_f32_16x16x32_bf16 v[48:51], v[220:223], v[72:75], v[48:51]\n"
"ds_read_b64_tr_b16 v[212:213], v240 offset:17728\n"
"v_mfma_f32_16x16x32_bf16 v[52:55], v[220:223], v[76:79], v[52:55]\n"
"ds_read_b64_tr_b16 v[214:215], v240 offset:22080\n"
"v_mfma_f32_16x16x32_bf16 v[56:59], v[220:223], v[80:83], v[56:59]\n"
"s_add_u32 s44, s46, 0x1a59c0\n"
"buffer_load_dwordx4 v246, s[8:11], s44 offen offset:1088 lds\n"
"v_mfma_f32_16x16x32_bf16 v[60:63], v[220:223], v[84:87], v[60:63]\n"
"v_mfma_f32_16x16x32_bf16 v[68:71], v[220:223], v[88:91], v[68:71]\n"
"s_waitcnt vmcnt(6)\n"
"s_waitcnt lgkmcnt(4)\n"
"s_barrier\n"
"s_waitcnt lgkmcnt(2)\n"
"v_mfma_f32_16x16x32_bf16 v[0:3], v[208:211], v[92:95], v[0:3]\n"
"ds_read_b64_tr_b16 v[216:217], v240 offset:18048\n"
"v_mfma_f32_16x16x32_bf16 v[4:7], v[208:211], v[96:99], v[4:7]\n"
"ds_read_b64_tr_b16 v[218:219], v240 offset:22400\n"
"v_mfma_f32_16x16x32_bf16 v[8:11], v[208:211], v[100:103], v[8:11]\n"
"ds_read_b128 v[72:75], v242 offset:0\n"
"s_add_u32 m0, s48, 0x0\n"
"s_add_u32 s43, s46, 0x1c2000\n"
"buffer_load_dwordx4 v246, s[8:11], s43 offen lds\n"
"v_mfma_f32_16x16x32_bf16 v[12:15], v[208:211], v[104:107], v[12:15]\n"
"ds_read_b128 v[76:79], v242 offset:2048\n"
"s_waitcnt lgkmcnt(4)\n"
"v_mfma_f32_16x16x32_bf16 v[16:19], v[212:215], v[92:95], v[16:19]\n"
"v_add_u32 v241, s47, v241\n"
"ds_read_b64_tr_b16 v[220:221], v241 offset:17408\n"
"v_mfma_f32_16x16x32_bf16 v[20:23], v[212:215], v[96:99], v[20:23]\n"
"ds_read_b64_tr_b16 v[222:223], v241 offset:21760\n"
"v_mfma_f32_16x16x32_bf16 v[24:27], v[212:215], v[100:103], v[24:27]\n"
"ds_read_b128 v[80:83], v242 offset:4096\n"
"v_mfma_f32_16x16x32_bf16 v[28:31], v[212:215], v[104:107], v[28:31]\n"
"ds_read_b128 v[84:87], v242 offset:6144\n"
"v_mfma_f32_16x16x32_bf16 v[64:67], v[212:215], v[108:111], v[64:67]\n"
"ds_read_b128 v[88:91], v244 offset:0\n"
"s_waitcnt lgkmcnt(7)\n"
"v_mfma_f32_16x16x32_bf16 v[32:35], v[92:95], v[216:219], v[32:35]\n"
"ds_read_b64_tr_b16 v[208:209], v240 offset:34816\n"
"v_mfma_f32_16x16x32_bf16 v[36:39], v[96:99], v[216:219], v[36:39]\n"
"ds_read_b64_tr_b16 v[210:211], v240 offset:39168\n"
"v_mfma_f32_16x16x32_bf16 v[40:43], v[100:103], v[216:219], v[40:43]\n"
"s_add_u32 s44, s46, 0x1c39c0\n"
"buffer_load_dwordx4 v246, s[8:11], s44 offen offset:1088 lds\n"
"v_mfma_f32_16x16x32_bf16 v[44:47], v[104:107], v[216:219], v[44:47]\n"
"s_waitcnt lgkmcnt(5)\n"
"v_mfma_f32_16x16x32_bf16 v[48:51], v[220:223], v[92:95], v[48:51]\n"
"ds_read_b64_tr_b16 v[212:213], v240 offset:35136\n"
"v_mfma_f32_16x16x32_bf16 v[52:55], v[220:223], v[96:99], v[52:55]\n"
"ds_read_b64_tr_b16 v[214:215], v240 offset:39488\n"
"v_mfma_f32_16x16x32_bf16 v[56:59], v[220:223], v[100:103], v[56:59]\n"
"v_mfma_f32_16x16x32_bf16 v[60:63], v[220:223], v[104:107], v[60:63]\n"
"v_mfma_f32_16x16x32_bf16 v[68:71], v[220:223], v[108:111], v[68:71]\n"
"s_waitcnt vmcnt(6)\n"
"s_barrier\n"
"s_waitcnt lgkmcnt(2)\n"
"v_mfma_f32_16x16x32_bf16 v[0:3], v[208:211], v[72:75], v[0:3]\n"
"ds_read_b64_tr_b16 v[216:217], v240 offset:35456\n"
"v_mfma_f32_16x16x32_bf16 v[4:7], v[208:211], v[76:79], v[4:7]\n"
"ds_read_b64_tr_b16 v[218:219], v240 offset:39808\n"
"s_cmp_ge_u32 s32, 2\n"
"s_cbranch_scc1 .Lhx0_3009\n"
"s_add_u32 m0, s3, 0x26800\n"
"s_mov_b32 s41, 1024\n"
"buffer_load_dwordx4 v249, s[4:7], s41 offen lds\n"
".Lhx0_3009:\n"
"v_mfma_f32_16x16x32_bf16 v[8:11], v[208:211], v[80:83], v[8:11]\n"
"ds_read_b128 v[92:95], v243 offset:0\n"
"v_mfma_f32_16x16x32_bf16 v[12:15], v[208:211], v[84:87], v[12:15]\n"
"ds_read_b128 v[96:99], v243 offset:2048\n"
"s_waitcnt lgkmcnt(4)\n"
"v_mfma_f32_16x16x32_bf16 v[16:19], v[212:215], v[72:75], v[16:19]\n"
"v_add_u32 v241, s47, v241\n"
"ds_read_b64_tr_b16 v[220:221], v241 offset:34816\n"
"s_add_u32 m0, s50, 0x22800\n"
"s_add_u32 s41, s45, 1024\n"
"buffer_load_dwordx4 v247, s[4:7], s41 offen lds\n"
"v_mfma_f32_16x16x32_bf16 v[20:23], v[212:215], v[76:79], v[20:23]\n"
"ds_read_b64_tr_b16 v[222:223], v241 offset:39168\n"
"v_mfma_f32_16x16x32_bf16 v[24:27], v[212:215], v[80:83], v[24:27]\n"
"ds_read_b128 v[100:103], v243 offset:4096\n"
"v_mfma_f32_16x16x32_bf16 v[28:31], v[212:215], v[84:87], v[28:31]\n"
"ds_read_b128 v[104:107], v243 offset:6144\n"
"v_mfma_f32_16x16x32_bf16 v[64:67], v[212:215], v[88:91], v[64:67]\n"
"ds_read_b128 v[108:111], v245 offset:0\n"
"buffer_load_dwordx4 v248, s[4:7], s41 offen offset:1024 lds\n"
"s_waitcnt lgkmcnt(7)\n"
"v_mfma_f32_16x16x32_bf16 v[32:35], v[72:75], v[216:219], v[32:35]\n"
"v_add_u32 v240, 0xcc00, v240\n"
"ds_read_b64_tr_b16 v[208:209], v240 offset:0\n"
"v_mfma_f32_16x16x32_bf16 v[36:39], v[76:79], v[216:219], v[36:39]\n"
"ds_read_b64_tr_b16 v[210:211], v240 offset:4352\n"
"v_mfma_f32_16x16x32_bf16 v[40:43], v[80:83], v[216:219], v[40:43]\n"
"s_add_u32 m0, s48, 0x4400\n"
"s_add_u32 s43, s46, 0x1e0000\n"
"buffer_load_dwordx4 v246, s[8:11], s43 offen lds\n"
"v_mfma_f32_16x16x32_bf16 v[44:47], v[84:87], v[216:219], v[44:47]\n"
"s_waitcnt lgkmcnt(5)\n"
"v_mfma_f32_16x16x32_bf16 v[48:51], v[220:223], v[72:75], v[48:51]\n"
"ds_read_b64_tr_b16 v[212:213], v240 offset:320\n"
"v_mfma_f32_16x16x32_bf16 v[52:55], v[220:223], v[76:79], v[52:55]\n"
"ds_read_b64_tr_b16 v[214:215], v240 offset:4672\n"
"v_mfma_f32_16x16x32_bf16 v[56:59], v[220:223], v[80:83], v[56:59]\n"
"s_add_u32 s44, s46, 0x1e19c0\n"
"buffer_load_dwordx4 v246, s[8:11], s44 offen offset:1088 lds\n"
"v_mfma_f32_16x16x32_bf16 v[60:63], v[220:223], v[84:87], v[60:63]\n"
"v_mfma_f32_16x16x32_bf16 v[68:71], v[220:223], v[88:91], v[68:71]\n"
"s_waitcnt vmcnt(6)\n"
"s_waitcnt lgkmcnt(4)\n"
"s_barrier\n"
"s_waitcnt lgkmcnt(2)\n"
"v_mfma_f32_16x16x32_bf16 v[0:3], v[208:211], v[92:95], v[0:3]\n"
"ds_read_b64_tr_b16 v[216:217], v240 offset:640\n"
"v_mfma_f32_16x16x32_bf16 v[4:7], v[208:211], v[96:99], v[4:7]\n"
"ds_read_b64_tr_b16 v[218:219], v240 offset:4992\n"
"v_mfma_f32_16x16x32_bf16 v[8:11], v[208:211], v[100:103], v[8:11]\n"
"ds_read_b128 v[72:75], v242 offset:18432\n"
"s_add_u32 m0, s48, 0x8800\n"
"s_add_u32 s43, s46, 0x1fe000\n"
"buffer_load_dwordx4 v246, s[8:11], s43 offen lds\n"
"v_mfma_f32_16x16x32_bf16 v[12:15], v[208:211], v[104:107], v[12:15]\n"
"ds_read_b128 v[76:79], v242 offset:20480\n"
"s_waitcnt lgkmcnt(4)\n"
"v_mfma_f32_16x16x32_bf16 v[16:19], v[212:215], v[92:95], v[16:19]\n"
"v_add_u32 v241, s95, v241\n"
"ds_read_b64_tr_b16 v[220:221], v241 offset:0\n"
"v_mfma_f32_16x16x32_bf16 v[20:23], v[212:215], v[96:99], v[20:23]\n"
"ds_read_b64_tr_b16 v[222:223], v241 offset:4352\n"
"v_mfma_f32_16x16x32_bf16 v[24:27], v[212:215], v[100:103], v[24:27]\n"
"ds_read_b128 v[80:83], v242 offset:22528\n"
"v_mfma_f32_16x16x32_bf16 v[28:31], v[212:215], v[104:107], v[28:31]\n"
"ds_read_b128 v[84:87], v242 offset:24576\n"
"v_mfma_f32_16x16x32_bf16 v[64:67], v[212:215], v[108:111], v[64:67]\n"
"ds_read_b128 v[88:91], v244 offset:18432\n"
"s_waitcnt lgkmcnt(7)\n"
"v_mfma_f32_16x16x32_bf16 v[32:35], v[92:95], v[216:219], v[32:35]\n"
"ds_read_b64_tr_b16 v[208:209], v240 offset:17408\n"
"v_mfma_f32_16x16x32_bf16 v[36:39], v[96:99], v[216:219], v[36:39]\n"
"ds_read_b64_tr_b16 v[210:211], v240 offset:21760\n"
"v_mfma_f32_16x16x32_bf16 v[40:43], v[100:103], v[216:219], v[40:43]\n"
"s_add_u32 s44, s46, 0x1ff9c0\n"
"buffer_load_dwordx4 v246, s[8:11], s44 offen offset:1088 lds\n"
"v_mfma_f32_16x16x32_bf16 v[44:47], v[104:107], v[216:219], v[44:47]\n"
"s_waitcnt lgkmcnt(5)\n"
"v_mfma_f32_16x16x32_bf16 v[48:51], v[220:223], v[92:95], v[48:51]\n"
"ds_read_b64_tr_b16 v[212:213], v240 offset:17728\n"
"v_mfma_f32_16x16x32_bf16 v[52:55], v[220:223], v[96:99], v[52:55]\n"
"ds_read_b64_tr_b16 v[214:215], v240 offset:22080\n"
"v_mfma_f32_16x16x32_bf16 v[56:59], v[220:223], v[100:103], v[56:59]\n"
"v_mfma_f32_16x16x32_bf16 v[60:63], v[220:223], v[104:107], v[60:63]\n"
"v_mfma_f32_16x16x32_bf16 v[68:71], v[220:223], v[108:111], v[68:71]\n"
"s_waitcnt vmcnt(6)\n"
"s_barrier\n"
"s_waitcnt lgkmcnt(2)\n"
"v_mfma_f32_16x16x32_bf16 v[0:3], v[208:211], v[72:75], v[0:3]\n"
"ds_read_b64_tr_b16 v[216:217], v240 offset:18048\n"
"s_lshr_b32 s35, s34, 1\n"
"v_mfma_f32_16x16x32_bf16 v[4:7], v[208:211], v[76:79], v[4:7]\n"
"ds_read_b64_tr_b16 v[218:219], v240 offset:22400\n"
"s_cmp_ge_u32 s32, 2\n"
"s_cbranch_scc1 .Lhx0_3010\n"
"s_add_u32 m0, s3, 0x1d800\n"
"s_mov_b32 s41, 1152\n"
"buffer_load_dwordx4 v249, s[4:7], s41 offen lds\n"
".Lhx0_3010:\n"
"v_mfma_f32_16x16x32_bf16 v[8:11], v[208:211], v[80:83], v[8:11]\n"
"ds_read_b128 v[92:95], v243 offset:18432\n"
"s_and_b32 s36, s34, 1\n"
"v_mfma_f32_16x16x32_bf16 v[12:15], v[208:211], v[84:87], v[12:15]\n"
"ds_read_b128 v[96:99], v243 offset:20480\n"
"s_add_u32 s35, s35, s36\n"
"s_waitcnt lgkmcnt(4)\n"
"v_mfma_f32_16x16x32_bf16 v[16:19], v[212:215], v[72:75], v[16:19]\n"
"v_add_u32 v241, s47, v241\n"
"ds_read_b64_tr_b16 v[220:221], v241 offset:17408\n"
"s_add_u32 m0, s50, 0x19800\n"
"s_add_u32 s41, s45, 1152\n"
"buffer_load_dwordx4 v247, s[4:7], s41 offen lds\n"
"v_mfma_f32_16x16x32_bf16 v[20:23], v[212:215], v[76:79], v[20:23]\n"
"ds_read_b64_tr_b16 v[222:223], v241 offset:21760\n"
"s_cmp_lt_u32 s34, 16\n"
"s_cselect_b32 s35, s34, s35\n"
"s_cmp_lt_u32 s34, 8\n"
"s_cselect_b32 s36, 2, 0\n"
"v_mfma_f32_16x16x32_bf16 v[24:27], v[212:215], v[80:83], v[24:27]\n"
"ds_read_b128 v[100:103], v243 offset:22528\n"
"v_mfma_f32_16x16x32_bf16 v[28:31], v[212:215], v[84:87], v[28:31]\n"
"ds_read_b128 v[104:107], v243 offset:24576\n"
"s_add_u32 s35, s35, s36\n"
"v_mfma_f32_16x16x32_bf16 v[64:67], v[212:215], v[88:91], v[64:67]\n"
"ds_read_b128 v[108:111], v245 offset:18432\n"
"buffer_load_dwordx4 v248, s[4:7], s41 offen offset:1024 lds\n"
"s_waitcnt lgkmcnt(7)\n"
"v_mfma_f32_16x16x32_bf16 v[32:35], v[72:75], v[216:219], v[32:35]\n"
"v_add_u32 v240, 0xffff3400, v240\n"
"ds_read_b64_tr_b16 v[208:209], v240 offset:0\n"
"s_add_u32 s0, s35, 0\n"
"v_mfma_f32_16x16x32_bf16 v[36:39], v[76:79], v[216:219], v[36:39]\n"
"ds_read_b64_tr_b16 v[210:211], v240 offset:4352\n"
"s_and_b32 s0, s0, 3\n"
"v_mfma_f32_16x16x32_bf16 v[40:43], v[80:83], v[216:219], v[40:43]\n"
"s_add_u32 m0, s48, 0xcc00\n"
"s_add_u32 s43, s46, 0x21c000\n"
"buffer_load_dwordx4 v246, s[8:11], s43 offen lds\n"
"v_mfma_f32_16x16x32_bf16 v[44:47], v[84:87], v[216:219], v[44:47]\n"
"s_add_u32 s1, s35, 1\n"
"s_and_b32 s1, s1, 3\n"
"s_waitcnt lgkmcnt(5)\n"
"v_mfma_f32_16x16x32_bf16 v[48:51], v[220:223], v[72:75], v[48:51]\n"
"ds_read_b64_tr_b16 v[212:213], v240 offset:320\n"
"s_add_u32 s58, s35, 2\n"
"v_mfma_f32_16x16x32_bf16 v[52:55], v[220:223], v[76:79], v[52:55]\n"
"ds_read_b64_tr_b16 v[214:215], v240 offset:4672\n"
"s_and_b32 s58, s58, 3\n"
"v_mfma_f32_16x16x32_bf16 v[56:59], v[220:223], v[80:83], v[56:59]\n"
"s_add_u32 s44, s46, 0x21d9c0\n"
"buffer_load_dwordx4 v246, s[8:11], s44 offen offset:1088 lds\n"
"s_add_u32 s59, s35, 3\n"
"v_mfma_f32_16x16x32_bf16 v[60:63], v[220:223], v[84:87], v[60:63]\n"
"s_and_b32 s59, s59, 3\n"
"v_mfma_f32_16x16x32_bf16 v[68:71], v[220:223], v[88:91], v[68:71]\n"
"s_waitcnt vmcnt(6)\n"
"s_waitcnt lgkmcnt(4)\n"
"s_barrier\n"
"s_waitcnt lgkmcnt(2)\n"
"v_mfma_f32_16x16x32_bf16 v[0:3], v[208:211], v[92:95], v[0:3]\n"
"ds_read_b64_tr_b16 v[216:217], v240 offset:640\n"
"v_mfma_f32_16x16x32_bf16 v[4:7], v[208:211], v[96:99], v[4:7]\n"
"ds_read_b64_tr_b16 v[218:219], v240 offset:4992\n"
"v_mfma_f32_16x16x32_bf16 v[8:11], v[208:211], v[100:103], v[8:11]\n"
"ds_read_b128 v[72:75], v242 offset:36864\n"
"s_add_u32 m0, s48, 0x11000\n"
"s_add_u32 s43, s46, 0x23a000\n"
"buffer_load_dwordx4 v246, s[8:11], s43 offen lds\n"
"v_mfma_f32_16x16x32_bf16 v[12:15], v[208:211], v[104:107], v[12:15]\n"
"ds_read_b128 v[76:79], v242 offset:38912\n"
"s_waitcnt lgkmcnt(4)\n"
"v_mfma_f32_16x16x32_bf16 v[16:19], v[212:215], v[92:95], v[16:19]\n"
"v_add_u32 v241, s99, v241\n"
"ds_read_b64_tr_b16 v[220:221], v241 offset:0\n"
"v_mfma_f32_16x16x32_bf16 v[20:23], v[212:215], v[96:99], v[20:23]\n"
"ds_read_b64_tr_b16 v[222:223], v241 offset:4352\n"
"v_mfma_f32_16x16x32_bf16 v[24:27], v[212:215], v[100:103], v[24:27]\n"
"ds_read_b128 v[80:83], v242 offset:40960\n"
"v_mfma_f32_16x16x32_bf16 v[28:31], v[212:215], v[104:107], v[28:31]\n"
"ds_read_b128 v[84:87], v242 offset:43008\n"
"v_mfma_f32_16x16x32_bf16 v[64:67], v[212:215], v[108:111], v[64:67]\n"
"ds_read_b128 v[88:91], v244 offset:36864\n"
"s_waitcnt lgkmcnt(7)\n"
"v_mfma_f32_16x16x32_bf16 v[32:35], v[92:95], v[216:219], v[32:35]\n"
"ds_read_b64_tr_b16 v[208:209], v240 offset:17408\n"
"v_mfma_f32_16x16x32_bf16 v[36:39], v[96:99], v[216:219], v[36:39]\n"
"ds_read_b64_tr_b16 v[210:211], v240 offset:21760\n"
"v_mfma_f32_16x16x32_bf16 v[40:43], v[100:103], v[216:219], v[40:43]\n"
"s_add_u32 s44, s46, 0x23b9c0\n"
"buffer_load_dwordx4 v246, s[8:11], s44 offen offset:1088 lds\n"
"v_mfma_f32_16x16x32_bf16 v[44:47], v[104:107], v[216:219], v[44:47]\n"
"s_waitcnt lgkmcnt(5)\n"
"v_mfma_f32_16x16x32_bf16 v[48:51], v[220:223], v[92:95], v[48:51]\n"
"ds_read_b64_tr_b16 v[212:213], v240 offset:17728\n"
"v_mfma_f32_16x16x32_bf16 v[52:55], v[220:223], v[96:99], v[52:55]\n"
"ds_read_b64_tr_b16 v[214:215], v240 offset:22080\n"
"v_mfma_f32_16x16x32_bf16 v[56:59], v[220:223], v[100:103], v[56:59]\n"
"v_mfma_f32_16x16x32_bf16 v[60:63], v[220:223], v[104:107], v[60:63]\n"
"v_mfma_f32_16x16x32_bf16 v[68:71], v[220:223], v[108:111], v[68:71]\n"
"s_waitcnt vmcnt(6)\n"
"s_barrier\n"
"s_waitcnt lgkmcnt(2)\n"
"v_mfma_f32_16x16x32_bf16 v[0:3], v[208:211], v[72:75], v[0:3]\n"
"ds_read_b64_tr_b16 v[216:217], v240 offset:18048\n"
"v_mfma_f32_16x16x32_bf16 v[4:7], v[208:211], v[76:79], v[4:7]\n"
"ds_read_b64_tr_b16 v[218:219], v240 offset:22400\n"
"v_mfma_f32_16x16x32_bf16 v[8:11], v[208:211], v[80:83], v[8:11]\n"
"ds_read_b128 v[92:95], v243 offset:36864\n"
"s_add_u32 m0, s88, 0x0\n"
"s_mul_i32 s96, s0, 491520\n"
"s_add_u32 s96, s96, s84\n"
"buffer_load_dwordx4 v253, s[20:23], s96 offen lds\n"
"v_mfma_f32_16x16x32_bf16 v[12:15], v[208:211], v[84:87], v[12:15]\n"
"ds_read_b128 v[96:99], v243 offset:38912\n"
"s_waitcnt lgkmcnt(4)\n"
"v_mfma_f32_16x16x32_bf16 v[16:19], v[212:215], v[72:75], v[16:19]\n"
"v_add_u32 v241, s47, v241\n"
"ds_read_b64_tr_b16 v[220:221], v241 offset:17408\n"
"v_mfma_f32_16x16x32_bf16 v[20:23], v[212:215], v[76:79], v[20:23]\n"
"ds_read_b64_tr_b16 v[222:223], v241 offset:21760\n"
"v_mfma_f32_16x16x32_bf16 v[24:27], v[212:215], v[80:83], v[24:27]\n"
"ds_read_b128 v[100:103], v243 offset:40960\n"
"v_mfma_f32_16x16x32_bf16 v[28:31], v[212:215], v[84:87], v[28:31]\n"
"ds_read_b128 v[104:107], v243 offset:43008\n"
"v_mfma_f32_16x16x32_bf16 v[64:67], v[212:215], v[88:91], v[64:67]\n"
"ds_read_b128 v[108:111], v245 offset:36864\n"
"s_waitcnt lgkmcnt(7)\n"
"v_mfma_f32_16x16x32_bf16 v[32:35], v[72:75], v[216:219], v[32:35]\n"
"ds_read_b64_tr_b16 v[208:209], v240 offset:34816\n"
"v_mfma_f32_16x16x32_bf16 v[36:39], v[76:79], v[216:219], v[36:39]\n"
"ds_read_b64_tr_b16 v[210:211], v240 offset:39168\n"
"v_mfma_f32_16x16x32_bf16 v[40:43], v[80:83], v[216:219], v[40:43]\n"
"s_add_u32 s97, s96, 0x4c00\n"
"buffer_load_dwordx4 v253, s[20:23], s97 offen offset:1024 lds\n"
"v_mfma_f32_16x16x32_bf16 v[44:47], v[84:87], v[216:219], v[44:47]\n"
"s_waitcnt lgkmcnt(5)\n"
"v_mfma_f32_16x16x32_bf16 v[48:51], v[220:223], v[72:75], v[48:51]\n"
"ds_read_b64_tr_b16 v[212:213], v240 offset:35136\n"
"v_mfma_f32_16x16x32_bf16 v[52:55], v[220:223], v[76:79], v[52:55]\n"
"ds_read_b64_tr_b16 v[214:215], v240 offset:39488\n"
"v_mfma_f32_16x16x32_bf16 v[56:59], v[220:223], v[80:83], v[56:59]\n"
"v_mfma_f32_16x16x32_bf16 v[60:63], v[220:223], v[84:87], v[60:63]\n"
"v_mfma_f32_16x16x32_bf16 v[68:71], v[220:223], v[88:91], v[68:71]\n"
"s_waitcnt vmcnt(4)\n"
"s_waitcnt lgkmcnt(4)\n"
"s_barrier\n"
"s_waitcnt lgkmcnt(2)\n"
"v_mfma_f32_16x16x32_bf16 v[0:3], v[208:211], v[92:95], v[0:3]\n"
"ds_read_b64_tr_b16 v[216:217], v240 offset:35456\n"
"v_mfma_f32_16x16x32_bf16 v[4:7], v[208:211], v[96:99], v[4:7]\n"
"ds_read_b64_tr_b16 v[218:219], v240 offset:39808\n"
"v_mfma_f32_16x16x32_bf16 v[8:11], v[208:211], v[100:103], v[8:11]\n"
"ds_read_b128 v[72:75], v242 offset:0\n"
"s_add_u32 m0, s88, 0x4400\n"
"s_mul_i32 s96, s1, 491520\n"
"s_add_u32 s96, s96, s84\n"
"buffer_load_dwordx4 v253, s[20:23], s96 offen lds\n"
"v_mfma_f32_16x16x32_bf16 v[12:15], v[208:211], v[104:107], v[12:15]\n"
"ds_read_b128 v[76:79], v242 offset:2048\n"
"s_waitcnt lgkmcnt(4)\n"
"v_mfma_f32_16x16x32_bf16 v[16:19], v[212:215], v[92:95], v[16:19]\n"
"v_add_u32 v241, s47, v241\n"
"ds_read_b64_tr_b16 v[220:221], v241 offset:34816\n"
"v_mfma_f32_16x16x32_bf16 v[20:23], v[212:215], v[96:99], v[20:23]\n"
"ds_read_b64_tr_b16 v[222:223], v241 offset:39168\n"
"v_mfma_f32_16x16x32_bf16 v[24:27], v[212:215], v[100:103], v[24:27]\n"
"ds_read_b128 v[80:83], v242 offset:4096\n"
"v_mfma_f32_16x16x32_bf16 v[28:31], v[212:215], v[104:107], v[28:31]\n"
"ds_read_b128 v[84:87], v242 offset:6144\n"
"v_mfma_f32_16x16x32_bf16 v[64:67], v[212:215], v[108:111], v[64:67]\n"
"ds_read_b128 v[88:91], v244 offset:0\n"
"s_waitcnt lgkmcnt(7)\n"
"v_mfma_f32_16x16x32_bf16 v[32:35], v[92:95], v[216:219], v[32:35]\n"
"v_add_u32 v240, 0xcc00, v240\n"
"ds_read_b64_tr_b16 v[208:209], v240 offset:0\n"
"v_mfma_f32_16x16x32_bf16 v[36:39], v[96:99], v[216:219], v[36:39]\n"
"ds_read_b64_tr_b16 v[210:211], v240 offset:4352\n"
"v_mfma_f32_16x16x32_bf16 v[40:43], v[100:103], v[216:219], v[40:43]\n"
"s_add_u32 s97, s96, 0x4c00\n"
"buffer_load_dwordx4 v253, s[20:23], s97 offen offset:1024 lds\n"
"v_mfma_f32_16x16x32_bf16 v[44:47], v[104:107], v[216:219], v[44:47]\n"
"s_waitcnt lgkmcnt(5)\n"
"v_mfma_f32_16x16x32_bf16 v[48:51], v[220:223], v[92:95], v[48:51]\n"
"ds_read_b64_tr_b16 v[212:213], v240 offset:320\n"
"v_mfma_f32_16x16x32_bf16 v[52:55], v[220:223], v[96:99], v[52:55]\n"
"ds_read_b64_tr_b16 v[214:215], v240 offset:4672\n"
"v_mfma_f32_16x16x32_bf16 v[56:59], v[220:223], v[100:103], v[56:59]\n"
"v_mfma_f32_16x16x32_bf16 v[60:63], v[220:223], v[104:107], v[60:63]\n"
"v_mfma_f32_16x16x32_bf16 v[68:71], v[220:223], v[108:111], v[68:71]\n"
"s_waitcnt vmcnt(4)\n"
"s_barrier\n"
"s_waitcnt lgkmcnt(2)\n"
"v_mfma_f32_16x16x32_bf16 v[0:3], v[208:211], v[72:75], v[0:3]\n"
"ds_read_b64_tr_b16 v[216:217], v240 offset:640\n"
"v_mfma_f32_16x16x32_bf16 v[4:7], v[208:211], v[76:79], v[4:7]\n"
"ds_read_b64_tr_b16 v[218:219], v240 offset:4992\n"
"v_mfma_f32_16x16x32_bf16 v[8:11], v[208:211], v[80:83], v[8:11]\n"
"ds_read_b128 v[92:95], v243 offset:0\n"
"s_add_u32 m0, s88, 0x8800\n"
"s_mul_i32 s96, s58, 491520\n"
"s_add_u32 s96, s96, s84\n"
"buffer_load_dwordx4 v253, s[20:23], s96 offen lds\n"
"v_mfma_f32_16x16x32_bf16 v[12:15], v[208:211], v[84:87], v[12:15]\n"
"ds_read_b128 v[96:99], v243 offset:2048\n"
"s_waitcnt lgkmcnt(4)\n"
"v_mfma_f32_16x16x32_bf16 v[16:19], v[212:215], v[72:75], v[16:19]\n"
"v_add_u32 v241, s95, v241\n"
"ds_read_b64_tr_b16 v[220:221], v241 offset:0\n"
"v_mfma_f32_16x16x32_bf16 v[20:23], v[212:215], v[76:79], v[20:23]\n"
"ds_read_b64_tr_b16 v[222:223], v241 offset:4352\n"
"v_mfma_f32_16x16x32_bf16 v[24:27], v[212:215], v[80:83], v[24:27]\n"
"ds_read_b128 v[100:103], v243 offset:4096\n"
"v_mfma_f32_16x16x32_bf16 v[28:31], v[212:215], v[84:87], v[28:31]\n"
"ds_read_b128 v[104:107], v243 offset:6144\n"
"v_mfma_f32_16x16x32_bf16 v[64:67], v[212:215], v[88:91], v[64:67]\n"
"ds_read_b128 v[108:111], v245 offset:0\n"
"s_waitcnt lgkmcnt(7)\n"
"v_mfma_f32_16x16x32_bf16 v[32:35], v[72:75], v[216:219], v[32:35]\n"
"v_add_u32 v241, s47, v241\n"
"ds_read_b64_tr_b16 v[208:209], v241 offset:17408\n"
"v_mfma_f32_16x16x32_bf16 v[36:39], v[76:79], v[216:219], v[36:39]\n"
"ds_read_b64_tr_b16 v[210:211], v241 offset:21760\n"
"v_mfma_f32_16x16x32_bf16 v[40:43], v[80:83], v[216:219], v[40:43]\n"
"s_add_u32 s97, s96, 0x4c00\n"
"buffer_load_dwordx4 v253, s[20:23], s97 offen offset:1024 lds\n"
"v_mfma_f32_16x16x32_bf16 v[44:47], v[84:87], v[216:219], v[44:47]\n"
"s_waitcnt lgkmcnt(5)\n"
"v_mfma_f32_16x16x32_bf16 v[48:51], v[220:223], v[72:75], v[48:51]\n"
"ds_read_b64_tr_b16 v[212:213], v240 offset:17728\n"
"v_mfma_f32_16x16x32_bf16 v[52:55], v[220:223], v[76:79], v[52:55]\n"
"ds_read_b64_tr_b16 v[214:215], v240 offset:22080\n"
"v_mfma_f32_16x16x32_bf16 v[56:59], v[220:223], v[80:83], v[56:59]\n"
"v_mfma_f32_16x16x32_bf16 v[60:63], v[220:223], v[84:87], v[60:63]\n"
"v_mfma_f32_16x16x32_bf16 v[68:71], v[220:223], v[88:91], v[68:71]\n"
"s_waitcnt lgkmcnt(4)\n"
"s_barrier\n"
"s_waitcnt lgkmcnt(2)\n"
"v_mfma_f32_16x16x32_bf16 v[48:51], v[208:211], v[92:95], v[48:51]\n"
"ds_read_b64_tr_b16 v[216:217], v240 offset:18048\n"
"v_mfma_f32_16x16x32_bf16 v[52:55], v[208:211], v[96:99], v[52:55]\n"
"ds_read_b64_tr_b16 v[218:219], v240 offset:22400\n"
"s_add_u32 m0, s88, 0xcc00\n"
"s_mul_i32 s96, s59, 491520\n"
"s_add_u32 s96, s96, s84\n"
"buffer_load_dwordx4 v253, s[20:23], s96 offen lds\n"
"v_mfma_f32_16x16x32_bf16 v[56:59], v[208:211], v[100:103], v[56:59]\n"
"v_mfma_f32_16x16x32_bf16 v[60:63], v[208:211], v[104:107], v[60:63]\n"
"v_mfma_f32_16x16x32_bf16 v[68:71], v[208:211], v[108:111], v[68:71]\n"
"s_waitcnt lgkmcnt(2)\n"
"v_mfma_f32_16x16x32_bf16 v[16:19], v[212:215], v[92:95], v[16:19]\n"
"ds_read_b64_tr_b16 v[220:221], v240 offset:17408\n"
"s_add_u32 s97, s96, 0x4c00\n"
"buffer_load_dwordx4 v253, s[20:23], s97 offen offset:1024 lds\n"
"v_mfma_f32_16x16x32_bf16 v[20:23], v[212:215], v[96:99], v[20:23]\n"
"ds_read_b64_tr_b16 v[222:223], v240 offset:21760\n"
"v_mfma_f32_16x16x32_bf16 v[24:27], v[212:215], v[100:103], v[24:27]\n"
"v_mfma_f32_16x16x32_bf16 v[28:31], v[212:215], v[104:107], v[28:31]\n"
"v_cvt_pk_bf16_f32 v196, v48, v49\n"
"v_cvt_pk_bf16_f32 v197, v50, v51\n"
"ds_write_b64 v156, v[196:197] offset:0\n"
"v_mfma_f32_16x16x32_bf16 v[64:67], v[212:215], v[108:111], v[64:67]\n"
"v_cvt_pk_bf16_f32 v198, v52, v53\n"
"v_cvt_pk_bf16_f32 v199, v54, v55\n"
"s_waitcnt lgkmcnt(3)\n"
"v_mfma_f32_16x16x32_bf16 v[32:35], v[92:95], v[216:219], v[32:35]\n"
"ds_write_b64 v156, v[198:199] offset:512\n"
"v_cvt_pk_bf16_f32 v200, v56, v57\n"
"v_cvt_pk_bf16_f32 v201, v58, v59\n"
"v_mfma_f32_16x16x32_bf16 v[36:39], v[96:99], v[216:219], v[36:39]\n"
"ds_write_b64 v156, v[200:201] offset:1024\n"
"v_cvt_pk_bf16_f32 v202, v60, v61\n"
"v_mfma_f32_16x16x32_bf16 v[40:43], v[100:103], v[216:219], v[40:43]\n"
"v_cvt_pk_bf16_f32 v203, v62, v63\n"
"ds_write_b64 v156, v[202:203] offset:1536\n"
"v_cvt_pk_bf16_f32 v204, v68, v69\n"
"v_mfma_f32_16x16x32_bf16 v[44:47], v[104:107], v[216:219], v[44:47]\n"
"v_cvt_pk_bf16_f32 v205, v70, v71\n"
"ds_write_b64 v157, v[204:205]\n"
"s_waitcnt lgkmcnt(5)\n"
"v_mfma_f32_16x16x32_bf16 v[0:3], v[220:223], v[92:95], v[0:3]\n"
"v_mfma_f32_16x16x32_bf16 v[4:7], v[220:223], v[96:99], v[4:7]\n"
"v_mfma_f32_16x16x32_bf16 v[8:11], v[220:223], v[100:103], v[8:11]\n"
"v_mfma_f32_16x16x32_bf16 v[12:15], v[220:223], v[104:107], v[12:15]\n"
"v_cvt_pk_bf16_f32 v180, v20, v21\n"
"v_cvt_pk_bf16_f32 v181, v22, v23\n"
"ds_write_b64 v159, v[180:181] offset:0\n"
"v_cvt_pk_bf16_f32 v192, v28, v29\n"
"v_cvt_pk_bf16_f32 v193, v30, v31\n"
"ds_write_b64 v159, v[192:193] offset:1024\n"
"v_cvt_pk_bf16_f32 v176, v16, v17\n"
"v_cvt_pk_bf16_f32 v177, v18, v19\n"
"v_cvt_pk_bf16_f32 v188, v24, v25\n"
"v_cvt_pk_bf16_f32 v189, v26, v27\n"
"v_cvt_pk_bf16_f32 v206, v64, v65\n"
"v_cvt_pk_bf16_f32 v207, v66, v67\n"
"ds_write_b64 v158, v[206:207] offset:0\n"
"v_cvt_pk_bf16_f32 v132, v36, v37\n"
"v_cvt_pk_bf16_f32 v133, v38, v39\n"
"ds_write_b64 v159, v[132:133] offset:8\n"
"v_cvt_pk_bf16_f32 v138, v44, v45\n"
"v_cvt_pk_bf16_f32 v139, v46, v47\n"
"ds_write_b64 v159, v[138:139] offset:1032\n"
".Lqtail:\n"
"s_nop 7\n"
"s_lshl_b32 s48, s55, 4\n"
"s_lshl_b64 s[44:45], 0xffff, s48\n"
"s_waitcnt lgkmcnt(0)\n"
"s_barrier\n"
"ds_read_b64 v[78:79], v162 offset:4096\n"
"ds_read_b64 v[178:179], v162 offset:0\n"
"ds_read_b64 v[82:83], v162 offset:4608\n"
"ds_read_b64 v[182:183], v162 offset:512\n"
"ds_read_b64 v[86:87], v162 offset:5120\n"
"ds_read_b64 v[190:191], v162 offset:1024\n"
"ds_read_b64 v[90:91], v162 offset:5632\n"
"ds_read_b64 v[194:195], v162 offset:1536\n"
"ds_read_b64 v[172:173], v160\n"
"ds_read_b64 v[174:175], v163\n"
"ds_read_b64 v[184:185], v161\n"
"ds_read_b64 v[186:187], v164\n"
"v_cvt_pk_bf16_f32 v76, v0, v1\n"
"v_cvt_pk_bf16_f32 v77, v2, v3\n"
"v_cvt_pk_bf16_f32 v80, v4, v5\n"
"v_cvt_pk_bf16_f32 v81, v6, v7\n"
"v_cvt_pk_bf16_f32 v84, v8, v9\n"
"v_cvt_pk_bf16_f32 v85, v10, v11\n"
"v_cvt_pk_bf16_f32 v88, v12, v13\n"
"v_cvt_pk_bf16_f32 v89, v14, v15\n"
"v_cvt_pk_bf16_f32 v130, v32, v33\n"
"v_cvt_pk_bf16_f32 v131, v34, v35\n"
"v_cvt_pk_bf16_f32 v136, v40, v41\n"
"v_cvt_pk_bf16_f32 v137, v42, v43\n"
"s_mov_b32 vcc_lo, 0xffff0000\n"
"s_mov_b32 vcc_hi, 0xffff0000\n"
"v_cndmask_b32 v72, v48, v52, vcc\n"
"v_cndmask_b32 v73, v56, v60, vcc\n"
"s_mov_b32 vcc_lo, 0\n"
"s_mov_b32 vcc_hi, -1\n"
"v_cndmask_b32 v72, v72, v73, vcc\n"
"v_mul_f32 v72, 0xbfb8aa3b, v72\n"
"v_exp_f32 v72, v72\n"
"s_nop 0\n"
"v_add_f32 v72, 1.0, v72\n"
"v_rcp_f32 v72, v72\n"
"ds_read_b64 v[128:129], v160 offset:8\n"
"ds_read_b64_tr_b16 v[140:141], v166\n"
"ds_read_b64_tr_b16 v[142:143], v165 offset:0\n"
"ds_read_b64_tr_b16 v[144:145], v165 offset:512\n"
"ds_read_b64 v[134:135], v161 offset:8\n"
"ds_read_b64_tr_b16 v[146:147], v167\n"
"ds_read_b64_tr_b16 v[148:149], v165 offset:1024\n"
"ds_read_b64_tr_b16 v[150:151], v165 offset:1536\n"
"v_mbcnt_lo_u32_b32 v246, -1, 0\n"
"v_mbcnt_hi_u32_b32 v246, -1, v246\n"
"s_and_b32 s35, s32, 3\n"
"s_lshl_b32 s35, s35, 12\n"
"v_bfe_u32 v244, v246, 2, 3\n"
"v_lshrrev_b32 v245, 1, v244\n"
"v_lshlrev_b32 v244, 7, v244\n"
"v_lshl_add_u32 v244, v245, 5, v244\n"
"v_and_b32 v245, 3, v246\n"
"v_lshl_add_u32 v244, v245, 3, v244\n"
"v_lshrrev_b32 v245, 5, v246\n"
"v_mul_u32_u24 v245, 2048, v245\n"
"v_add_u32 v244, v244, v245\n"
"v_add_u32 v240, s35, v244\n"
"v_xor_b32 v241, 32, v240\n"
"v_xor_b32 v242, 64, v240\n"
"v_xor_b32 v243, 96, v240\n"
"s_waitcnt lgkmcnt(15)\n"
"v_cndmask_b32 v82, 0, v82, s[44:45]\n"
"v_cndmask_b32 v83, 0, v83, s[44:45]\n"
"s_waitcnt lgkmcnt(13)\n"
"v_cndmask_b32 v90, 0, v90, s[44:45]\n"
"v_cndmask_b32 v91, 0, v91, s[44:45]\n"
"v_cndmask_b32 v78, 0, v78, s[44:45]\n"
"v_cndmask_b32 v79, 0, v79, s[44:45]\n"
"v_cndmask_b32 v86, 0, v86, s[44:45]\n"
"v_cndmask_b32 v87, 0, v87, s[44:45]\n"
"s_add_u32 m0, s88, 0x11000\n"
"s_mul_i32 s96, s0, 491520\n"
"s_add_u32 s96, s96, s84\n"
"s_add_u32 s96, s96, 0xa000\n"
"buffer_load_dwordx4 v253, s[20:23], s96 offen lds\n"
"s_waitcnt lgkmcnt(12)\n"
"v_mfma_f32_16x16x32_bf16 v[12:15], v[180:183], v[80:83], v[228:231]\n"
"v_mfma_f32_16x16x32_bf16 v[8:11], v[176:179], v[80:83], v[232:235]\n"
"v_mfma_f32_16x16x32_bf16 v[28:31], v[192:195], v[88:91], v[228:231]\n"
"v_mfma_f32_16x16x32_bf16 v[24:27], v[188:191], v[88:91], v[232:235]\n"
"s_waitcnt lgkmcnt(10)\n"
"v_mfma_f32_16x16x32_bf16 v[4:7], v[176:179], v[76:79], v[228:231]\n"
"v_mfma_f32_16x16x32_bf16 v[0:3], v[172:175], v[76:79], v[236:239]\n"
"s_waitcnt lgkmcnt(8)\n"
"v_and_b32 v112, 0xf0, v254\n"
"s_lshl_b32 s95, s33, 11\n"
"v_lshlrev_b32 v112, 3, v112\n"
"s_add_u32 s95, s95, 0x882000\n"
"buffer_load_dwordx2 v[114:115], v112, s[12:15], s95 offen sc1\n"
"v_mfma_f32_16x16x32_bf16 v[20:23], v[188:191], v[84:87], v[228:231]\n"
"v_mfma_f32_16x16x32_bf16 v[16:19], v[184:187], v[84:87], v[152:155]\n"
"v_max3_f32 v65, v12, v13, v14\n"
"v_max3_f32 v69, v15, v8, v9\n"
"v_max3_f32 v65, v65, v10, v11\n"
"v_max_f32 v65, v65, v69\n"
"v_max3_f32 v67, v28, v29, v30\n"
"v_max3_f32 v71, v31, v24, v25\n"
"v_max3_f32 v67, v67, v26, v27\n"
"s_add_u32 s97, s96, 0x4c00\n"
"buffer_load_dwordx4 v253, s[20:23], s97 offen offset:1024 lds\n"
"v_max_f32 v67, v67, v71\n"
"v_max3_f32 v64, v4, v5, v6\n"
"v_max3_f32 v68, v7, v0, v1\n"
"v_max3_f32 v64, v64, v2, v3\n"
"v_max_f32 v64, v64, v68\n"
"v_max3_f32 v66, v20, v21, v22\n"
"v_max3_f32 v70, v23, v16, v17\n"
"v_max3_f32 v66, v66, v18, v19\n"
"v_max_f32 v66, v66, v70\n"
"v_mov_b32 v69, v65\n"
"v_mov_b32 v71, v67\n"
"v_mov_b32 v68, v64\n"
"v_mov_b32 v70, v66\n"
"v_permlane32_swap_b32 v65, v69\n"
"v_permlane32_swap_b32 v67, v71\n"
"v_permlane32_swap_b32 v64, v68\n"
"v_permlane32_swap_b32 v66, v70\n"
"v_max_f32 v65, v65, v69\n"
"v_max_f32 v67, v67, v71\n"
"v_max_f32 v64, v64, v68\n"
"v_max_f32 v66, v66, v70\n"
"v_mov_b32 v69, v65\n"
"v_mov_b32 v71, v67\n"
"v_mov_b32 v68, v64\n"
"v_mov_b32 v70, v66\n"
"s_add_u32 m0, s88, 0x15400\n"
"s_mul_i32 s96, s1, 491520\n"
"s_add_u32 s96, s96, s84\n"
"s_add_u32 s96, s96, 0xa000\n"
"buffer_load_dwordx4 v253, s[20:23], s96 offen lds\n"
"v_permlane16_swap_b32 v65, v69\n"
"v_permlane16_swap_b32 v67, v71\n"
"v_permlane16_swap_b32 v64, v68\n"
"v_permlane16_swap_b32 v66, v70\n"
"v_max_f32 v65, v65, v69\n"
"v_max_f32 v67, v67, v71\n"
"v_max_f32 v64, v64, v68\n"
"v_max_f32 v66, v66, v70\n"
"v_mul_f32 v65, s53, v65\n"
"v_mul_f32 v67, s53, v67\n"
"v_mul_f32 v64, s53, v64\n"
"v_mul_f32 v66, s53, v66\n"
"v_fma_f32 v8, v8, s53, -v65\n"
"v_fma_f32 v9, v9, s53, -v65\n"
"v_fma_f32 v10, v10, s53, -v65\n"
"v_fma_f32 v11, v11, s53, -v65\n"
"v_fma_f32 v12, v12, s53, -v65\n"
"v_fma_f32 v13, v13, s53, -v65\n"
"v_fma_f32 v14, v14, s53, -v65\n"
"v_fma_f32 v15, v15, s53, -v65\n"
"v_exp_f32 v8, v8\n"
"v_exp_f32 v9, v9\n"
"v_exp_f32 v10, v10\n"
"v_exp_f32 v11, v11\n"
"s_add_u32 s97, s96, 0x4c00\n"
"buffer_load_dwordx4 v253, s[20:23], s97 offen offset:1024 lds\n"
"v_exp_f32 v12, v12\n"
"v_exp_f32 v13, v13\n"
"v_exp_f32 v14, v14\n"
"v_exp_f32 v15, v15\n"
"v_cvt_pk_bf16_f32 v36, v8, v9\n"
"v_cvt_pk_bf16_f32 v37, v10, v11\n"
"v_cvt_pk_bf16_f32 v38, v12, v13\n"
"v_cvt_pk_bf16_f32 v39, v14, v15\n"
"v_fma_f32 v24, v24, s53, -v67\n"
"v_fma_f32 v25, v25, s53, -v67\n"
"v_fma_f32 v26, v26, s53, -v67\n"
"v_fma_f32 v27, v27, s53, -v67\n"
"v_fma_f32 v28, v28, s53, -v67\n"
"v_fma_f32 v29, v29, s53, -v67\n"
"v_fma_f32 v30, v30, s53, -v67\n"
"v_fma_f32 v31, v31, s53, -v67\n"
"v_exp_f32 v24, v24\n"
"v_exp_f32 v25, v25\n"
"v_exp_f32 v26, v26\n"
"v_exp_f32 v27, v27\n"
"v_exp_f32 v28, v28\n"
"v_exp_f32 v29, v29\n"
"v_exp_f32 v30, v30\n"
"v_exp_f32 v31, v31\n"
"v_cvt_pk_bf16_f32 v44, v24, v25\n"
"s_add_u32 m0, s88, 0x19400\n"
"s_mul_i32 s96, s58, 491520\n"
"s_add_u32 s96, s96, s84\n"
"s_add_u32 s96, s96, 0xa000\n"
"buffer_load_dwordx4 v253, s[20:23], s96 offen lds\n"
"v_cvt_pk_bf16_f32 v45, v26, v27\n"
"v_cvt_pk_bf16_f32 v46, v28, v29\n"
"v_cvt_pk_bf16_f32 v47, v30, v31\n"
"s_waitcnt lgkmcnt(0)\n"
"v_mfma_f32_16x16x32_bf16 v[12:15], v[224:227], v[36:39], 0\n"
"v_mfma_f32_16x16x32_bf16 v[52:55], v[130:133], v[36:39], 0\n"
"v_mfma_f32_16x16x32_bf16 v[8:11], v[142:145], v[36:39], 0\n"
"v_fma_f32 v0, v0, s53, -v64\n"
"v_fma_f32 v1, v1, s53, -v64\n"
"v_fma_f32 v2, v2, s53, -v64\n"
"v_fma_f32 v3, v3, s53, -v64\n"
"v_fma_f32 v4, v4, s53, -v64\n"
"v_fma_f32 v5, v5, s53, -v64\n"
"v_fma_f32 v6, v6, s53, -v64\n"
"v_fma_f32 v7, v7, s53, -v64\n"
"v_exp_f32 v0, v0\n"
"v_exp_f32 v1, v1\n"
"v_exp_f32 v2, v2\n"
"v_exp_f32 v3, v3\n"
"v_exp_f32 v4, v4\n"
"v_exp_f32 v5, v5\n"
"v_exp_f32 v6, v6\n"
"v_exp_f32 v7, v7\n"
"v_cvt_pk_bf16_f32 v32, v0, v1\n"
"s_add_u32 s97, s96, 0x4c00\n"
"buffer_load_dwordx4 v253, s[20:23], s97 offen offset:1024 lds\n"
"v_cvt_pk_bf16_f32 v33, v2, v3\n"
"v_cvt_pk_bf16_f32 v34, v4, v5\n"
"v_cvt_pk_bf16_f32 v35, v6, v7\n"
"v_mfma_f32_16x16x32_bf16 v[28:31], v[224:227], v[44:47], 0\n"
"v_mfma_f32_16x16x32_bf16 v[60:63], v[136:139], v[44:47], 0\n"
"v_mfma_f32_16x16x32_bf16 v[24:27], v[148:151], v[44:47], 0\n"
"v_fma_f32 v16, v16, s53, -v66\n"
"v_fma_f32 v17, v17, s53, -v66\n"
"v_fma_f32 v18, v18, s53, -v66\n"
"v_fma_f32 v19, v19, s53, -v66\n"
"v_fma_f32 v20, v20, s53, -v66\n"
"v_fma_f32 v21, v21, s53, -v66\n"
"v_fma_f32 v22, v22, s53, -v66\n"
"s_waitcnt vmcnt(5)\n"
"v_cmp_ne_u64 vcc, s[56:57], v[114:115]\n"
"s_nop 1\n"
"s_cmp_eq_u64 vcc, 0\n"
"s_cbranch_scc1 .Lok5002\n"
"s_mov_b32 s94, 0\n"
"s_lshl_b32 s95, s33, 11\n"
"s_add_u32 s95, s95, 0x882000\n"
".Lagain5003:\n"
"s_sleep 1\n"
"buffer_load_dwordx2 v[114:115], v112, s[12:15], s95 offen sc1\n"
"s_add_u32 s94, s94, 1\n"
"s_waitcnt vmcnt(0)\n"
"v_cmp_ne_u64 vcc, s[56:57], v[114:115]\n"
"s_nop 1\n"
"s_cmp_eq_u64 vcc, 0\n"
"s_cbranch_scc1 .Lok5002\n"
"s_cmp_lt_u32 s94, 0x8000\n"
"s_cbranch_scc1 .Lagain5003\n"
".Lok5002:\n"
"v_fma_f32 v23, v23, s53, -v66\n"
"v_exp_f32 v16, v16\n"
"s_add_u32 m0, s88, 0x1d400\n"
"s_mul_i32 s96, s59, 491520\n"
"s_add_u32 s96, s96, s84\n"
"s_add_u32 s96, s96, 0xa000\n"
"buffer_load_dwordx4 v253, s[20:23], s96 offen lds\n"
"v_exp_f32 v17, v17\n"
"v_exp_f32 v18, v18\n"
"v_exp_f32 v19, v19\n"
"v_exp_f32 v20, v20\n"
"v_exp_f32 v21, v21\n"
"s_add_u32 s97, s96, 0x4c00\n"
"buffer_load_dwordx4 v253, s[20:23], s97 offen offset:1024 lds\n"
"v_exp_f32 v22, v22\n"
"v_exp_f32 v23, v23\n"
"v_cvt_pk_bf16_f32 v40, v16, v17\n"
"v_cvt_pk_bf16_f32 v41, v18, v19\n"
"v_cvt_pk_bf16_f32 v42, v20, v21\n"
"s_lshl_b32 s98, s0, 12\n"
"s_add_u32 s98, s98, s89\n"
"buffer_load_dwordx4 v[96:99], v254, s[12:15], s98 offen sc1\n"
"v_cvt_pk_bf16_f32 v43, v22, v23\n"
"v_mfma_f32_16x16x32_bf16 v[4:7], v[224:227], v[32:35], 0\n"
"v_mfma_f32_16x16x32_bf16 v[48:51], v[128:131], v[32:35], 0\n"
"v_mfma_f32_16x16x32_bf16 v[0:3], v[140:143], v[32:35], 0\n"
"s_mov_b32 s44, 0xffff0000\n"
"s_add_u32 s98, s98, 0x11000\n"
"buffer_load_dwordx4 v[100:103], v254, s[12:15], s98 offen sc1\n"
"s_mov_b32 s45, 0xffff0000\n"
"s_mov_b32 s46, 0\n"
"s_mov_b32 s47, -1\n"
"v_mfma_f32_16x16x32_bf16 v[20:23], v[224:227], v[40:43], 0\n"
"v_mfma_f32_16x16x32_bf16 v[56:59], v[134:137], v[40:43], 0\n"
"s_add_u32 s98, s98, 0x11000\n"
"buffer_load_dwordx4 v[104:107], v254, s[12:15], s98 offen sc1\n"
"v_mfma_f32_16x16x32_bf16 v[16:19], v[146:149], v[40:43], 0\n"
"v_rcp_f32 v65, v12\n"
"v_rcp_f32 v67, v28\n"
"v_rcp_f32 v64, v4\n"
"s_nop 0\n"
"v_rcp_f32 v66, v20\n"
"s_lshl_b32 s98, s1, 12\n"
"s_add_u32 s98, s98, s89\n"
"buffer_load_dwordx4 v[108:111], v254, s[12:15], s98 offen sc1\n"
"v_mul_f32 v52, v65, v52\n"
"v_mul_f32 v53, v65, v53\n"
"v_mul_f32 v54, v65, v54\n"
"v_mul_f32 v55, v65, v55\n"
"v_cvt_pk_bf16_f32 v78, v52, v53\n"
"s_add_u32 s98, s98, 0x11000\n"
"buffer_load_dwordx4 v[112:115], v254, s[12:15], s98 offen sc1\n"
"v_cvt_pk_bf16_f32 v79, v54, v55\n"
"v_mul_f32 v8, v65, v8\n"
"v_mul_f32 v9, v65, v9\n"
"v_mul_f32 v10, v65, v10\n"
"v_mul_f32 v11, v65, v11\n"
"s_add_u32 s98, s98, 0x11000\n"
"buffer_load_dwordx4 v[116:119], v254, s[12:15], s98 offen sc1\n"
"v_cvt_pk_bf16_f32 v86, v8, v9\n"
"v_cvt_pk_bf16_f32 v87, v10, v11\n"
"v_mul_f32 v60, v67, v60\n"
"v_mul_f32 v61, v67, v61\n"
"v_mul_f32 v62, v67, v62\n"
"s_lshl_b32 s98, s58, 12\n"
"s_add_u32 s98, s98, s89\n"
"buffer_load_dwordx4 v[196:199], v254, s[12:15], s98 offen sc1\n"
"v_mul_f32 v63, v67, v63\n"
"v_cvt_pk_bf16_f32 v82, v60, v61\n"
"v_cvt_pk_bf16_f32 v83, v62, v63\n"
"v_mul_f32 v24, v67, v24\n"
"v_mul_f32 v25, v67, v25\n"
"s_add_u32 s98, s98, 0x11000\n"
"buffer_load_dwordx4 v[200:203], v254, s[12:15], s98 offen sc1\n"
"v_mul_f32 v26, v67, v26\n"
"v_mul_f32 v27, v67, v27\n"
"v_cvt_pk_bf16_f32 v90, v24, v25\n"
"v_cvt_pk_bf16_f32 v91, v26, v27\n"
"v_mul_f32 v48, v64, v48\n"
"s_add_u32 s98, s98, 0x11000\n"
"buffer_load_dwordx4 v[204:207], v254, s[12:15], s98 offen sc1\n"
"v_mul_f32 v49, v64, v49\n"
"v_mul_f32 v50, v64, v50\n"
"v_mul_f32 v51, v64, v51\n"
"v_cvt_pk_bf16_f32 v76, v48, v49\n"
"v_cvt_pk_bf16_f32 v77, v50, v51\n"
"s_lshl_b32 s98, s59, 12\n"
"s_add_u32 s98, s98, s89\n"
"buffer_load_dwordx4 v[208:211], v254, s[12:15], s98 offen sc1\n"
"v_mul_f32 v0, v64, v0\n"
"v_mul_f32 v1, v64, v1\n"
"v_mul_f32 v2, v64, v2\n"
"v_mul_f32 v3, v64, v3\n"
"v_cvt_pk_bf16_f32 v84, v0, v1\n"
"s_add_u32 s98, s98, 0x11000\n"
"buffer_load_dwordx4 v[212:215], v254, s[12:15], s98 offen sc1\n"
"v_cvt_pk_bf16_f32 v85, v2, v3\n"
"v_mul_f32 v56, v66, v56\n"
"v_mul_f32 v57, v66, v57\n"
"v_mul_f32 v58, v66, v58\n"
"v_mul_f32 v59, v66, v59\n"
"s_add_u32 s98, s98, 0x11000\n"
"buffer_load_dwordx4 v[216:219], v254, s[12:15], s98 offen sc1\n"
"v_cvt_pk_bf16_f32 v80, v56, v57\n"
"v_cvt_pk_bf16_f32 v81, v58, v59\n"
"v_mul_f32 v16, v66, v16\n"
"v_mul_f32 v17, v66, v17\n"
"v_mul_f32 v18, v66, v18\n"
"s_cmp_lg_u32 s32, 7\n"
"s_cbranch_scc1 .Lpi5004\n"
"v_and_b32 v244, 0xf0, v254\n"
"s_lshl_b32 s95, s33, 11\n"
"v_lshlrev_b32 v244, 3, v244\n"
"s_add_u32 s95, s95, 0x88e000\n"
"buffer_load_dwordx2 v[246:247], v244, s[12:15], s95 offen sc1\n"
".Lpi5004:\n"
"v_mul_f32 v19, v66, v19\n"
"v_cvt_pk_bf16_f32 v88, v16, v17\n"
"v_cvt_pk_bf16_f32 v89, v18, v19\n"
"s_cmp_eq_u32 s49, 0\n"
"s_cbranch_scc1 .Lgst5001\n"
"buffer_store_dword v72, v170, s[12:15], s49 offen sc1\n"
".Lgst5001:\n"
"v_permlane16_swap_b32 v76, v78\n"
"v_permlane16_swap_b32 v77, v79\n"
"buffer_store_dwordx4 v[76:79], v168, s[12:15], s51 offen sc1\n"
"v_permlane16_swap_b32 v80, v82\n"
"v_permlane16_swap_b32 v81, v83\n"
"s_add_u32 s48, s51, 0x22000\n"
"buffer_store_dwordx4 v[80:83], v168, s[12:15], s48 offen sc1\n"
"v_cndmask_b32 v84, v84, v86, s[44:45]\n"
"v_cndmask_b32 v88, v88, v90, s[44:45]\n"
"v_cndmask_b32 v85, v85, v87, s[44:45]\n"
"v_cndmask_b32 v89, v89, v91, s[44:45]\n"
"v_cndmask_b32 v84, v84, v88, s[46:47]\n"
"v_cndmask_b32 v85, v85, v89, s[46:47]\n"
"buffer_store_dwordx2 v[84:85], v169, s[12:15], s42 offen sc1\n"
"s_barrier\n"
"ds_read_b64_tr_b16 v[224:225], v240 offset:0\n"
"ds_read_b64_tr_b16 v[226:227], v240 offset:1024\n"
"ds_read_b64_tr_b16 v[228:229], v241 offset:0\n"
"ds_read_b64_tr_b16 v[230:231], v241 offset:1024\n"
"s_waitcnt lgkmcnt(2)\n"
"s_waitcnt vmcnt(14)\n"
"v_mfma_f32_16x16x32_bf16 v[48:51], v[224:227], v[96:99], 0\n"
"ds_read_b64_tr_b16 v[232:233], v242 offset:0\n"
"s_waitcnt vmcnt(13)\n"
"v_mfma_f32_16x16x32_bf16 v[64:67], v[224:227], v[100:103], 0\n"
"ds_read_b64_tr_b16 v[234:235], v242 offset:1024\n"
"s_add_u32 m0, s88, 0x21400\n"
"s_mul_i32 s96, s0, 204800\n"
"s_add_u32 s96, s96, s85\n"
"buffer_load_dwordx4 v252, s[16:19], s96 offen lds\n"
"s_waitcnt vmcnt(13)\n"
"v_mfma_f32_16x16x32_bf16 v[80:83], v[224:227], v[104:107], 0\n"
"s_waitcnt lgkmcnt(2)\n"
"v_mfma_f32_16x16x32_bf16 v[52:55], v[228:231], v[96:99], 0\n"
"ds_read_b64_tr_b16 v[236:237], v243 offset:0\n"
"v_mfma_f32_16x16x32_bf16 v[68:71], v[228:231], v[100:103], 0\n"
"ds_read_b64_tr_b16 v[238:239], v243 offset:1024\n"
"v_mfma_f32_16x16x32_bf16 v[84:87], v[228:231], v[104:107], 0\n"
"s_waitcnt lgkmcnt(2)\n"
"v_mfma_f32_16x16x32_bf16 v[56:59], v[232:235], v[96:99], 0\n"
"ds_read_b64_tr_b16 v[224:225], v240 offset:17408\n"
"v_mfma_f32_16x16x32_bf16 v[72:75], v[232:235], v[100:103], 0\n"
"ds_read_b64_tr_b16 v[226:227], v240 offset:18432\n"
"s_add_u32 s97, s96, 0x1000\n"
"buffer_load_dwordx4 v252, s[16:19], s97 offen offset:1024 lds\n"
"v_mfma_f32_16x16x32_bf16 v[88:91], v[232:235], v[104:107], 0\n"
"s_waitcnt lgkmcnt(2)\n"
"v_mfma_f32_16x16x32_bf16 v[60:63], v[236:239], v[96:99], 0\n"
"ds_read_b64_tr_b16 v[228:229], v241 offset:17408\n"
"v_mfma_f32_16x16x32_bf16 v[76:79], v[236:239], v[100:103], 0\n"
"ds_read_b64_tr_b16 v[230:231], v241 offset:18432\n"
"v_mfma_f32_16x16x32_bf16 v[92:95], v[236:239], v[104:107], 0\n"
"s_barrier\n"
"s_waitcnt lgkmcnt(2)\n"
"s_waitcnt vmcnt(13)\n"
"v_mfma_f32_16x16x32_bf16 v[48:51], v[224:227], v[108:111], v[48:51]\n"
"ds_read_b64_tr_b16 v[232:233], v242 offset:17408\n"
"s_waitcnt vmcnt(12)\n"
"v_mfma_f32_16x16x32_bf16 v[64:67], v[224:227], v[112:115], v[64:67]\n"
"ds_read_b64_tr_b16 v[234:235], v242 offset:18432\n"
"s_add_u32 m0, s88, 0x0\n"
"s_mul_i32 s96, s1, 204800\n"
"s_add_u32 s96, s96, s85\n"
"buffer_load_dwordx4 v252, s[16:19], s96 offen lds\n"
"s_waitcnt vmcnt(12)\n"
"v_mfma_f32_16x16x32_bf16 v[80:83], v[224:227], v[116:119], v[80:83]\n"
"s_waitcnt lgkmcnt(2)\n"
"v_mfma_f32_16x16x32_bf16 v[52:55], v[228:231], v[108:111], v[52:55]\n"
"ds_read_b64_tr_b16 v[236:237], v243 offset:17408\n"
"v_mfma_f32_16x16x32_bf16 v[68:71], v[228:231], v[112:115], v[68:71]\n"
"ds_read_b64_tr_b16 v[238:239], v243 offset:18432\n"
"v_mfma_f32_16x16x32_bf16 v[84:87], v[228:231], v[116:119], v[84:87]\n"
"s_waitcnt lgkmcnt(2)\n"
"v_mfma_f32_16x16x32_bf16 v[56:59], v[232:235], v[108:111], v[56:59]\n"
"ds_read_b64_tr_b16 v[224:225], v240 offset:34816\n"
"v_mfma_f32_16x16x32_bf16 v[72:75], v[232:235], v[112:115], v[72:75]\n"
"ds_read_b64_tr_b16 v[226:227], v240 offset:35840\n"
"s_add_u32 s97, s96, 0x1000\n"
"buffer_load_dwordx4 v252, s[16:19], s97 offen offset:1024 lds\n"
"v_mfma_f32_16x16x32_bf16 v[88:91], v[232:235], v[116:119], v[88:91]\n"
"s_waitcnt lgkmcnt(2)\n"
"v_mfma_f32_16x16x32_bf16 v[60:63], v[236:239], v[108:111], v[60:63]\n"
"ds_read_b64_tr_b16 v[228:229], v241 offset:34816\n"
"v_mfma_f32_16x16x32_bf16 v[76:79], v[236:239], v[112:115], v[76:79]\n"
"ds_read_b64_tr_b16 v[230:231], v241 offset:35840\n"
"v_mfma_f32_16x16x32_bf16 v[92:95], v[236:239], v[116:119], v[92:95]\n"
"s_waitcnt vmcnt(4)\n"
"s_cmp_lg_u32 s32, 7\n"
"s_cbranch_scc1 .Lok5005\n"
"v_cmp_ne_u64 vcc, s[56:57], v[246:247]\n"
"s_nop 1\n"
"s_cmp_eq_u64 vcc, 0\n"
"s_cbranch_scc1 .Lok5005\n"
"s_mov_b32 s94, 0\n"
"s_lshl_b32 s95, s33, 11\n"
"s_add_u32 s95, s95, 0x88e000\n"
".Lagain5006:\n"
"s_sleep 1\n"
"buffer_load_dwordx2 v[246:247], v244, s[12:15], s95 offen sc1\n"
"s_add_u32 s94, s94, 1\n"
"s_waitcnt vmcnt(0)\n"
"v_cmp_ne_u64 vcc, s[56:57], v[246:247]\n"
"s_nop 1\n"
"s_cmp_eq_u64 vcc, 0\n"
"s_cbranch_scc1 .Lok5005\n"
"s_cmp_lt_u32 s94, 0x8000\n"
"s_cbranch_scc1 .Lagain5006\n"
".Lok5005:\n"
"s_barrier\n"
"s_waitcnt lgkmcnt(2)\n"
"v_mfma_f32_16x16x32_bf16 v[48:51], v[224:227], v[196:199], v[48:51]\n"
"ds_read_b64_tr_b16 v[232:233], v242 offset:34816\n"
"s_cmp_lg_u32 s32, 0\n"
"s_cbranch_scc1 .Lpub5007\n"
"v_mov_b32 v194, s56\n"
"v_mov_b32 v195, s57\n"
"s_add_u32 s94, s52, 0x88a000\n"
"s_mov_b64 exec, 1\n"
"buffer_store_dwordx2 v[194:195], off, s[12:15], s94 sc1\n"
"s_mov_b64 exec, -1\n"
".Lpub5007:\n"
"v_mfma_f32_16x16x32_bf16 v[64:67], v[224:227], v[200:203], v[64:67]\n"
"ds_read_b64_tr_b16 v[234:235], v242 offset:35840\n"
"v_mfma_f32_16x16x32_bf16 v[80:83], v[224:227], v[204:207], v[80:83]\n"
"s_lshl_b32 s98, s0, 12\n"
"s_add_u32 s98, s98, s89\n"
"s_add_u32 s98, s98, 0x4000\n"
"buffer_load_dwordx4 v[120:123], v254, s[12:15], s98 offen sc1\n"
"s_waitcnt lgkmcnt(2)\n"
"v_mfma_f32_16x16x32_bf16 v[52:55], v[228:231], v[196:199], v[52:55]\n"
"ds_read_b64_tr_b16 v[236:237], v243 offset:34816\n"
"s_add_u32 s98, s98, 0x11000\n"
"buffer_load_dwordx4 v[124:127], v254, s[12:15], s98 offen sc1\n"
"v_mfma_f32_16x16x32_bf16 v[68:71], v[228:231], v[200:203], v[68:71]\n"
"ds_read_b64_tr_b16 v[238:239], v243 offset:35840\n"
"v_mfma_f32_16x16x32_bf16 v[84:87], v[228:231], v[204:207], v[84:87]\n"
"s_add_u32 s98, s98, 0x11000\n"
"buffer_load_dwordx4 v[128:131], v254, s[12:15], s98 offen sc1\n"
"s_waitcnt lgkmcnt(2)\n"
"v_mfma_f32_16x16x32_bf16 v[56:59], v[232:235], v[196:199], v[56:59]\n"
"ds_read_b64_tr_b16 v[224:225], v240 offset:52224\n"
"v_mfma_f32_16x16x32_bf16 v[72:75], v[232:235], v[200:203], v[72:75]\n"
"ds_read_b64_tr_b16 v[226:227], v240 offset:53248\n"
"s_lshl_b32 s98, s1, 12\n"
"s_add_u32 s98, s98, s89\n"
"s_add_u32 s98, s98, 0x4000\n"
"buffer_load_dwordx4 v[132:135], v254, s[12:15], s98 offen sc1\n"
"v_mfma_f32_16x16x32_bf16 v[88:91], v[232:235], v[204:207], v[88:91]\n"
"s_waitcnt lgkmcnt(2)\n"
"v_mfma_f32_16x16x32_bf16 v[60:63], v[236:239], v[196:199], v[60:63]\n"
"ds_read_b64_tr_b16 v[228:229], v241 offset:52224\n"
"s_add_u32 s98, s98, 0x11000\n"
"buffer_load_dwordx4 v[136:139], v254, s[12:15], s98 offen sc1\n"
"v_mfma_f32_16x16x32_bf16 v[76:79], v[236:239], v[200:203], v[76:79]\n"
"ds_read_b64_tr_b16 v[230:231], v241 offset:53248\n"
"s_add_u32 s98, s98, 0x11000\n"
"buffer_load_dwordx4 v[140:143], v254, s[12:15], s98 offen sc1\n"
"v_mfma_f32_16x16x32_bf16 v[92:95], v[236:239], v[204:207], v[92:95]\n"
"s_barrier\n"
"s_waitcnt lgkmcnt(2)\n"
"v_mfma_f32_16x16x32_bf16 v[48:51], v[224:227], v[208:211], v[48:51]\n"
"ds_read_b64_tr_b16 v[232:233], v242 offset:52224\n"
"v_mfma_f32_16x16x32_bf16 v[64:67], v[224:227], v[212:215], v[64:67]\n"
"ds_read_b64_tr_b16 v[234:235], v242 offset:53248\n"
"s_lshl_b32 s98, s58, 12\n"
"s_add_u32 s98, s98, s89\n"
"s_add_u32 s98, s98, 0x4000\n"
"buffer_load_dwordx4 v[144:147], v254, s[12:15], s98 offen sc1\n"
"v_mfma_f32_16x16x32_bf16 v[80:83], v[224:227], v[216:219], v[80:83]\n"
"s_waitcnt lgkmcnt(2)\n"
"v_mfma_f32_16x16x32_bf16 v[52:55], v[228:231], v[208:211], v[52:55]\n"
"ds_read_b64_tr_b16 v[236:237], v243 offset:52224\n"
"v_mfma_f32_16x16x32_bf16 v[68:71], v[228:231], v[212:215], v[68:71]\n"
"ds_read_b64_tr_b16 v[238:239], v243 offset:53248\n"
"v_mfma_f32_16x16x32_bf16 v[84:87], v[228:231], v[216:219], v[84:87]\n"
"s_add_u32 s98, s98, 0x11000\n"
"buffer_load_dwordx4 v[148:151], v254, s[12:15], s98 offen sc1\n"
"s_waitcnt lgkmcnt(2)\n"
"v_mfma_f32_16x16x32_bf16 v[56:59], v[232:235], v[208:211], v[56:59]\n"
"v_add_u32 v240, 0x10000, v240\n"
"v_add_u32 v241, 0x10000, v241\n"
"v_add_u32 v242, 0x10000, v242\n"
"v_add_u32 v243, 0x10000, v243\n"
"ds_read_b64_tr_b16 v[224:225], v240 offset:4096\n"
"v_mfma_f32_16x16x32_bf16 v[72:75], v[232:235], v[212:215], v[72:75]\n"
"ds_read_b64_tr_b16 v[226:227], v240 offset:5120\n"
"v_mfma_f32_16x16x32_bf16 v[88:91], v[232:235], v[216:219], v[88:91]\n"
"s_waitcnt lgkmcnt(2)\n"
"v_mfma_f32_16x16x32_bf16 v[60:63], v[236:239], v[208:211], v[60:63]\n"
"ds_read_b64_tr_b16 v[228:229], v241 offset:4096\n"
"s_add_u32 s98, s98, 0x11000\n"
"buffer_load_dwordx4 v[152:155], v254, s[12:15], s98 offen sc1\n"
"v_mfma_f32_16x16x32_bf16 v[76:79], v[236:239], v[212:215], v[76:79]\n"
"ds_read_b64_tr_b16 v[230:231], v241 offset:5120\n"
"v_mfma_f32_16x16x32_bf16 v[92:95], v[236:239], v[216:219], v[92:95]\n"
"s_barrier\n"
"s_waitcnt lgkmcnt(2)\n"
"s_waitcnt vmcnt(8)\n"
"v_mfma_f32_16x16x32_bf16 v[48:51], v[224:227], v[120:123], v[48:51]\n"
"ds_read_b64_tr_b16 v[232:233], v242 offset:4096\n"
"s_lshl_b32 s98, s59, 12\n"
"s_add_u32 s98, s98, s89\n"
"s_add_u32 s98, s98, 0x4000\n"
"buffer_load_dwordx4 v[156:159], v254, s[12:15], s98 offen sc1\n"
"s_waitcnt vmcnt(8)\n"
"v_mfma_f32_16x16x32_bf16 v[64:67], v[224:227], v[124:127], v[64:67]\n"
"ds_read_b64_tr_b16 v[234:235], v242 offset:5120\n"
"s_waitcnt vmcnt(7)\n"
"v_mfma_f32_16x16x32_bf16 v[80:83], v[224:227], v[128:131], v[80:83]\n"
"s_waitcnt lgkmcnt(2)\n"
"v_mfma_f32_16x16x32_bf16 v[52:55], v[228:231], v[120:123], v[52:55]\n"
"ds_read_b64_tr_b16 v[236:237], v243 offset:4096\n"
"s_add_u32 s98, s98, 0x11000\n"
"buffer_load_dwordx4 v[160:163], v254, s[12:15], s98 offen sc1\n"
"v_mfma_f32_16x16x32_bf16 v[68:71], v[228:231], v[124:127], v[68:71]\n"
"ds_read_b64_tr_b16 v[238:239], v243 offset:5120\n"
"v_mfma_f32_16x16x32_bf16 v[84:87], v[228:231], v[128:131], v[84:87]\n"
"s_waitcnt lgkmcnt(2)\n"
"v_mfma_f32_16x16x32_bf16 v[56:59], v[232:235], v[120:123], v[56:59]\n"
"ds_read_b64_tr_b16 v[224:225], v240 offset:21504\n"
"s_add_u32 s98, s98, 0x11000\n"
"buffer_load_dwordx4 v[164:167], v254, s[12:15], s98 offen sc1\n"
"v_mfma_f32_16x16x32_bf16 v[72:75], v[232:235], v[124:127], v[72:75]\n"
"ds_read_b64_tr_b16 v[226:227], v240 offset:22528\n"
"v_mfma_f32_16x16x32_bf16 v[88:91], v[232:235], v[128:131], v[88:91]\n"
"s_waitcnt lgkmcnt(2)\n"
"v_mfma_f32_16x16x32_bf16 v[60:63], v[236:239], v[120:123], v[60:63]\n"
"ds_read_b64_tr_b16 v[228:229], v241 offset:21504\n"
"s_cmp_lg_u32 s32, 7\n"
"s_cbranch_scc1 .Lpi5008\n"
"v_and_b32 v186, 0xf0, v254\n"
"s_lshl_b32 s95, s33, 11\n"
"v_lshlrev_b32 v186, 3, v186\n"
"s_add_u32 s95, s95, 0x88a000\n"
"buffer_load_dwordx2 v[192:193], v186, s[12:15], s95 offen sc1\n"
".Lpi5008:\n"
"v_mfma_f32_16x16x32_bf16 v[76:79], v[236:239], v[124:127], v[76:79]\n"
"ds_read_b64_tr_b16 v[230:231], v241 offset:22528\n"
"v_mfma_f32_16x16x32_bf16 v[92:95], v[236:239], v[128:131], v[92:95]\n"
"s_barrier\n"
"s_waitcnt lgkmcnt(2)\n"
"s_waitcnt vmcnt(8)\n"
"v_mfma_f32_16x16x32_bf16 v[48:51], v[224:227], v[132:135], v[48:51]\n"
"ds_read_b64_tr_b16 v[232:233], v242 offset:21504\n"
"s_add_u32 m0, s88, 0x4400\n"
"s_mul_i32 s96, s58, 204800\n"
"s_add_u32 s96, s96, s85\n"
"buffer_load_dwordx4 v252, s[16:19], s96 offen lds\n"
"s_waitcnt vmcnt(8)\n"
"v_mfma_f32_16x16x32_bf16 v[64:67], v[224:227], v[136:139], v[64:67]\n"
"ds_read_b64_tr_b16 v[234:235], v242 offset:22528\n"
"s_waitcnt vmcnt(7)\n"
"v_mfma_f32_16x16x32_bf16 v[80:83], v[224:227], v[140:143], v[80:83]\n"
"s_waitcnt lgkmcnt(2)\n"
"v_mfma_f32_16x16x32_bf16 v[52:55], v[228:231], v[132:135], v[52:55]\n"
"ds_read_b64_tr_b16 v[236:237], v243 offset:21504\n"
"s_add_u32 s97, s96, 0x1000\n"
"buffer_load_dwordx4 v252, s[16:19], s97 offen offset:1024 lds\n"
"v_mfma_f32_16x16x32_bf16 v[68:71], v[228:231], v[136:139], v[68:71]\n"
"ds_read_b64_tr_b16 v[238:239], v243 offset:22528\n"
"v_mfma_f32_16x16x32_bf16 v[84:87], v[228:231], v[140:143], v[84:87]\n"
"s_waitcnt lgkmcnt(2)\n"
"v_mfma_f32_16x16x32_bf16 v[56:59], v[232:235], v[132:135], v[56:59]\n"
"ds_read_b64_tr_b16 v[224:225], v240 offset:37888\n"
"s_add_u32 m0, s88, 0x8800\n"
"s_mul_i32 s96, s59, 204800\n"
"s_add_u32 s96, s96, s85\n"
"buffer_load_dwordx4 v252, s[16:19], s96 offen lds\n"
"v_mfma_f32_16x16x32_bf16 v[72:75], v[232:235], v[136:139], v[72:75]\n"
"ds_read_b64_tr_b16 v[226:227], v240 offset:38912\n"
"v_mfma_f32_16x16x32_bf16 v[88:91], v[232:235], v[140:143], v[88:91]\n"
"s_waitcnt lgkmcnt(2)\n"
"v_mfma_f32_16x16x32_bf16 v[60:63], v[236:239], v[132:135], v[60:63]\n"
"ds_read_b64_tr_b16 v[228:229], v241 offset:37888\n"
"s_add_u32 s97, s96, 0x1000\n"
"buffer_load_dwordx4 v252, s[16:19], s97 offen offset:1024 lds\n"
"v_mfma_f32_16x16x32_bf16 v[76:79], v[236:239], v[136:139], v[76:79]\n"
"ds_read_b64_tr_b16 v[230:231], v241 offset:38912\n"
"v_mfma_f32_16x16x32_bf16 v[92:95], v[236:239], v[140:143], v[92:95]\n"
"s_cmp_lg_u32 s32, 7\n"
"s_cbranch_scc1 .Lok5009\n"
"s_waitcnt vmcnt(4)\n"
"v_cmp_ne_u64 vcc, s[56:57], v[192:193]\n"
"s_nop 1\n"
"s_cmp_eq_u64 vcc, 0\n"
"s_cbranch_scc1 .Lok5009\n"
"s_mov_b32 s94, 0\n"
"s_lshl_b32 s95, s33, 11\n"
"s_add_u32 s95, s95, 0x88a000\n"
".Lagain5010:\n"
"s_sleep 1\n"
"buffer_load_dwordx2 v[192:193], v186, s[12:15], s95 offen sc1\n"
"s_add_u32 s94, s94, 1\n"
"s_waitcnt vmcnt(0)\n"
"v_cmp_ne_u64 vcc, s[56:57], v[192:193]\n"
"s_nop 1\n"
"s_cmp_eq_u64 vcc, 0\n"
"s_cbranch_scc1 .Lok5009\n"
"s_cmp_lt_u32 s94, 0x8000\n"
"s_cbranch_scc1 .Lagain5010\n"
".Lok5009:\n"
"s_barrier\n"
"s_waitcnt lgkmcnt(2)\n"
"s_waitcnt vmcnt(9)\n"
"v_mfma_f32_16x16x32_bf16 v[48:51], v[224:227], v[144:147], v[48:51]\n"
"ds_read_b64_tr_b16 v[232:233], v242 offset:37888\n"
"v_lshrrev_b32 v183, 2, v254\n"
"s_lshl_b32 s98, s90, 6\n"
"s_add_u32 s98, s98, 0x880000\n"
"buffer_load_dword v184, v183, s[12:15], s98 offen sc1\n"
"s_waitcnt vmcnt(9)\n"
"v_mfma_f32_16x16x32_bf16 v[64:67], v[224:227], v[148:151], v[64:67]\n"
"ds_read_b64_tr_b16 v[234:235], v242 offset:38912\n"
"s_waitcnt vmcnt(8)\n"
"v_mfma_f32_16x16x32_bf16 v[80:83], v[224:227], v[152:155], v[80:83]\n"
"s_waitcnt lgkmcnt(2)\n"
"v_mfma_f32_16x16x32_bf16 v[52:55], v[228:231], v[144:147], v[52:55]\n"
"ds_read_b64_tr_b16 v[236:237], v243 offset:37888\n"
"s_lshl_b32 s98, s0, 12\n"
"s_add_u32 s98, s98, s89\n"
"s_add_u32 s98, s98, 0x8000\n"
"buffer_load_dwordx4 v[168:171], v254, s[12:15], s98 offen sc1\n"
"v_mfma_f32_16x16x32_bf16 v[68:71], v[228:231], v[148:151], v[68:71]\n"
"ds_read_b64_tr_b16 v[238:239], v243 offset:38912\n"
"v_mfma_f32_16x16x32_bf16 v[84:87], v[228:231], v[152:155], v[84:87]\n"
"s_waitcnt lgkmcnt(2)\n"
"v_mfma_f32_16x16x32_bf16 v[56:59], v[232:235], v[144:147], v[56:59]\n"
"ds_read_b64_tr_b16 v[224:225], v240 offset:54272\n"
"s_add_u32 s98, s98, 0x11000\n"
"buffer_load_dwordx4 v[172:175], v254, s[12:15], s98 offen sc1\n"
"v_mfma_f32_16x16x32_bf16 v[72:75], v[232:235], v[148:151], v[72:75]\n"
"ds_read_b64_tr_b16 v[226:227], v240 offset:55296\n"
"v_mfma_f32_16x16x32_bf16 v[88:91], v[232:235], v[152:155], v[88:91]\n"
"s_waitcnt lgkmcnt(2)\n"
"v_mfma_f32_16x16x32_bf16 v[60:63], v[236:239], v[144:147], v[60:63]\n"
"ds_read_b64_tr_b16 v[228:229], v241 offset:54272\n"
"s_add_u32 s98, s98, 0x11000\n"
"buffer_load_dwordx4 v[176:179], v254, s[12:15], s98 offen sc1\n"
"v_mfma_f32_16x16x32_bf16 v[76:79], v[236:239], v[148:151], v[76:79]\n"
"ds_read_b64_tr_b16 v[230:231], v241 offset:55296\n"
"v_mfma_f32_16x16x32_bf16 v[92:95], v[236:239], v[152:155], v[92:95]\n"
"s_barrier\n"
"s_waitcnt lgkmcnt(2)\n"
"s_waitcnt vmcnt(10)\n"
"v_mfma_f32_16x16x32_bf16 v[48:51], v[224:227], v[156:159], v[48:51]\n"
"ds_read_b64_tr_b16 v[232:233], v242 offset:54272\n"
"s_lshl_b32 s98, s1, 12\n"
"s_add_u32 s98, s98, s89\n"
"s_add_u32 s98, s98, 0x8000\n"
"buffer_load_dwordx4 v[96:99], v254, s[12:15], s98 offen sc1\n"
"s_waitcnt vmcnt(10)\n"
"v_mfma_f32_16x16x32_bf16 v[64:67], v[224:227], v[160:163], v[64:67]\n"
"ds_read_b64_tr_b16 v[234:235], v242 offset:55296\n"
"s_waitcnt vmcnt(9)\n"
"v_mfma_f32_16x16x32_bf16 v[80:83], v[224:227], v[164:167], v[80:83]\n"
"s_add_u32 s98, s98, 0x11000\n"
"buffer_load_dwordx4 v[100:103], v254, s[12:15], s98 offen sc1\n"
"s_waitcnt lgkmcnt(2)\n"
"v_mfma_f32_16x16x32_bf16 v[52:55], v[228:231], v[156:159], v[52:55]\n"
"ds_read_b64_tr_b16 v[236:237], v243 offset:54272\n"
"v_mfma_f32_16x16x32_bf16 v[68:71], v[228:231], v[160:163], v[68:71]\n"
"ds_read_b64_tr_b16 v[238:239], v243 offset:55296\n"
"s_add_u32 s98, s98, 0x11000\n"
"buffer_load_dwordx4 v[104:107], v254, s[12:15], s98 offen sc1\n"
"v_mfma_f32_16x16x32_bf16 v[84:87], v[228:231], v[164:167], v[84:87]\n"
"s_waitcnt lgkmcnt(2)\n"
"v_mfma_f32_16x16x32_bf16 v[56:59], v[232:235], v[156:159], v[56:59]\n"
"v_add_u32 v240, 0x10000, v240\n"
"v_add_u32 v241, 0x10000, v241\n"
"v_add_u32 v242, 0x10000, v242\n"
"v_add_u32 v243, 0x10000, v243\n"
"ds_read_b64_tr_b16 v[224:225], v240 offset:5120\n"
"s_lshl_b32 s98, s58, 12\n"
"s_add_u32 s98, s98, s89\n"
"s_add_u32 s98, s98, 0x8000\n"
"buffer_load_dwordx4 v[108:111], v254, s[12:15], s98 offen sc1\n"
"v_mfma_f32_16x16x32_bf16 v[72:75], v[232:235], v[160:163], v[72:75]\n"
"ds_read_b64_tr_b16 v[226:227], v240 offset:6144\n"
"v_mfma_f32_16x16x32_bf16 v[88:91], v[232:235], v[164:167], v[88:91]\n"
"s_add_u32 s98, s98, 0x11000\n"
"buffer_load_dwordx4 v[112:115], v254, s[12:15], s98 offen sc1\n"
"s_waitcnt lgkmcnt(2)\n"
"v_mfma_f32_16x16x32_bf16 v[60:63], v[236:239], v[156:159], v[60:63]\n"
"ds_read_b64_tr_b16 v[228:229], v241 offset:5120\n"
"v_mfma_f32_16x16x32_bf16 v[76:79], v[236:239], v[160:163], v[76:79]\n"
"ds_read_b64_tr_b16 v[230:231], v241 offset:6144\n"
"s_add_u32 s98, s98, 0x11000\n"
"buffer_load_dwordx4 v[116:119], v254, s[12:15], s98 offen sc1\n"
"v_mfma_f32_16x16x32_bf16 v[92:95], v[236:239], v[164:167], v[92:95]\n"
"s_barrier\n"
"s_waitcnt lgkmcnt(2)\n"
"s_waitcnt vmcnt(8)\n"
"v_mfma_f32_16x16x32_bf16 v[0:3], v[224:227], v[168:171], 0\n"
"ds_read_b64_tr_b16 v[232:233], v242 offset:5120\n"
"s_waitcnt vmcnt(7)\n"
"v_mfma_f32_16x16x32_bf16 v[16:19], v[224:227], v[172:175], 0\n"
"ds_read_b64_tr_b16 v[234:235], v242 offset:6144\n"
"s_add_u32 m0, s88, 0xcc00\n"
"s_add_u32 s96, s86, 0x0\n"
"buffer_load_dwordx4 v251, s[16:19], s96 offen lds\n"
"s_waitcnt vmcnt(7)\n"
"v_mfma_f32_16x16x32_bf16 v[32:35], v[224:227], v[176:179], 0\n"
"s_waitcnt lgkmcnt(2)\n"
"v_mfma_f32_16x16x32_bf16 v[4:7], v[228:231], v[168:171], 0\n"
"ds_read_b64_tr_b16 v[236:237], v243 offset:5120\n"
"v_mfma_f32_16x16x32_bf16 v[20:23], v[228:231], v[172:175], 0\n"
"ds_read_b64_tr_b16 v[238:239], v243 offset:6144\n"
"v_mfma_f32_16x16x32_bf16 v[36:39], v[228:231], v[176:179], 0\n"
"s_waitcnt lgkmcnt(2)\n"
"v_mfma_f32_16x16x32_bf16 v[8:11], v[232:235], v[168:171], 0\n"
"v_add_u32 v240, 0xfffe0000, v240\n"
"v_add_u32 v241, 0xfffe0000, v241\n"
"v_add_u32 v242, 0xfffe0000, v242\n"
"v_add_u32 v243, 0xfffe0000, v243\n"
"ds_read_b64_tr_b16 v[224:225], v240 offset:0\n"
"v_mfma_f32_16x16x32_bf16 v[24:27], v[232:235], v[172:175], 0\n"
"ds_read_b64_tr_b16 v[226:227], v240 offset:1024\n"
"s_add_u32 s97, s96, 0x6000\n"
"buffer_load_dwordx4 v251, s[16:19], s97 offen offset:1024 lds\n"
"v_mfma_f32_16x16x32_bf16 v[40:43], v[232:235], v[176:179], 0\n"
"s_waitcnt lgkmcnt(2)\n"
"v_mfma_f32_16x16x32_bf16 v[12:15], v[236:239], v[168:171], 0\n"
"ds_read_b64_tr_b16 v[228:229], v241 offset:0\n"
"v_mfma_f32_16x16x32_bf16 v[28:31], v[236:239], v[172:175], 0\n"
"ds_read_b64_tr_b16 v[230:231], v241 offset:1024\n"
"v_mfma_f32_16x16x32_bf16 v[44:47], v[236:239], v[176:179], 0\n"
"s_barrier\n"
"s_waitcnt lgkmcnt(2)\n"
"s_waitcnt vmcnt(7)\n"
"v_mfma_f32_16x16x32_bf16 v[0:3], v[224:227], v[96:99], v[0:3]\n"
"ds_read_b64_tr_b16 v[232:233], v242 offset:0\n"
"s_lshl_b32 s98, s59, 12\n"
"s_add_u32 s98, s98, s89\n"
"s_add_u32 s98, s98, 0x8000\n"
"buffer_load_dwordx4 v[196:199], v254, s[12:15], s98 offen sc1\n"
"s_waitcnt vmcnt(7)\n"
"v_mfma_f32_16x16x32_bf16 v[16:19], v[224:227], v[100:103], v[16:19]\n"
"ds_read_b64_tr_b16 v[234:235], v242 offset:1024\n"
"v_mbcnt_lo_u32_b32 v174, -1, 0\n"
"s_waitcnt vmcnt(6)\n"
"v_mfma_f32_16x16x32_bf16 v[32:35], v[224:227], v[104:107], v[32:35]\n"
"v_mbcnt_hi_u32_b32 v174, -1, v174\n"
"s_mov_b32 s40, 0x3f80\n"
"s_waitcnt lgkmcnt(2)\n"
"v_mfma_f32_16x16x32_bf16 v[4:7], v[228:231], v[96:99], v[4:7]\n"
"ds_read_b64_tr_b16 v[236:237], v243 offset:0\n"
"s_add_u32 s98, s98, 0x11000\n"
"buffer_load_dwordx4 v[200:203], v254, s[12:15], s98 offen sc1\n"
"v_mfma_f32_16x16x32_bf16 v[20:23], v[228:231], v[100:103], v[20:23]\n"
"ds_read_b64_tr_b16 v[238:239], v243 offset:1024\n"
"s_mov_b32 s41, 0x3f800000\n"
"s_and_b32 s35, s32, 3\n"
"v_mfma_f32_16x16x32_bf16 v[36:39], v[228:231], v[104:107], v[36:39]\n"
"s_mul_i32 s35, s35, 13824\n"
"s_waitcnt lgkmcnt(2)\n"
"v_mfma_f32_16x16x32_bf16 v[8:11], v[232:235], v[96:99], v[8:11]\n"
"ds_read_b64_tr_b16 v[224:225], v240 offset:17408\n"
"s_add_u32 s98, s98, 0x11000\n"
"buffer_load_dwordx4 v[204:207], v254, s[12:15], s98 offen sc1\n"
"v_mfma_f32_16x16x32_bf16 v[24:27], v[232:235], v[100:103], v[24:27]\n"
"ds_read_b64_tr_b16 v[226:227], v240 offset:18432\n"
"s_lshr_b32 s36, s32, 2\n"
"s_mul_i32 s37, s36, 6912\n"
"v_mfma_f32_16x16x32_bf16 v[40:43], v[232:235], v[104:107], v[40:43]\n"
"s_add_u32 s35, s35, s37\n"
"s_waitcnt lgkmcnt(2)\n"
"v_mfma_f32_16x16x32_bf16 v[12:15], v[236:239], v[96:99], v[12:15]\n"
"ds_read_b64_tr_b16 v[228:229], v241 offset:17408\n"
"s_cmp_lg_u32 s32, 7\n"
"s_cbranch_scc1 .Lpi5011\n"
"v_and_b32 v186, 0xf0, v254\n"
"s_lshl_b32 s95, s33, 11\n"
"v_lshlrev_b32 v186, 3, v186\n"
"s_add_u32 s95, s95, 0x886000\n"
"buffer_load_dwordx2 v[190:191], v186, s[12:15], s95 offen sc1\n"
".Lpi5011:\n"
"v_mfma_f32_16x16x32_bf16 v[28:31], v[236:239], v[100:103], v[28:31]\n"
"ds_read_b64_tr_b16 v[230:231], v241 offset:18432\n"
"s_add_u32 s35, s35, 0x0\n"
"v_and_b32 v168, 15, v174\n"
"v_mfma_f32_16x16x32_bf16 v[44:47], v[236:239], v[104:107], v[44:47]\n"
"v_mul_u32_u24 v168, 144, v168\n"
"s_barrier\n"
"s_waitcnt lgkmcnt(2)\n"
"s_waitcnt vmcnt(7)\n"
"v_mfma_f32_16x16x32_bf16 v[0:3], v[224:227], v[108:111], v[0:3]\n"
"ds_read_b64_tr_b16 v[232:233], v242 offset:17408\n"
"s_add_u32 m0, s88, 0x11000\n"
"s_mul_i32 s96, s0, 491520\n"
"s_add_u32 s96, s96, s87\n"
"buffer_load_dwordx4 v253, s[20:23], s96 offen lds\n"
"s_waitcnt vmcnt(7)\n"
"v_mfma_f32_16x16x32_bf16 v[16:19], v[224:227], v[112:115], v[16:19]\n"
"ds_read_b64_tr_b16 v[234:235], v242 offset:18432\n"
"v_lshrrev_b32 v169, 4, v174\n"
"s_waitcnt vmcnt(6)\n"
"v_mfma_f32_16x16x32_bf16 v[32:35], v[224:227], v[116:119], v[32:35]\n"
"v_lshl_add_u32 v168, v169, 3, v168\n"
"v_add_u32 v220, s35, v168\n"
"s_waitcnt lgkmcnt(2)\n"
"v_mfma_f32_16x16x32_bf16 v[4:7], v[228:231], v[108:111], v[4:7]\n"
"ds_read_b64_tr_b16 v[236:237], v243 offset:17408\n"
"s_add_u32 s97, s96, 0x4c00\n"
"buffer_load_dwordx4 v253, s[20:23], s97 offen offset:1024 lds\n"
"v_mfma_f32_16x16x32_bf16 v[20:23], v[228:231], v[112:115], v[20:23]\n"
"ds_read_b64_tr_b16 v[238:239], v243 offset:18432\n"
"s_mul_i32 s35, s32, 12\n"
"v_lshrrev_b32 v169, 3, v174\n"
"v_mfma_f32_16x16x32_bf16 v[36:39], v[228:231], v[116:119], v[36:39]\n"
"s_add_u32 s98, s89, 0xc000\n"
"buffer_load_dwordx4 v[208:211], v254, s[12:15], s98 offen sc1\n"
"s_waitcnt lgkmcnt(2)\n"
"v_mfma_f32_16x16x32_bf16 v[8:11], v[232:235], v[108:111], v[8:11]\n"
"ds_read_b64_tr_b16 v[224:225], v240 offset:34816\n"
"v_add_u32 v169, s35, v169\n"
"v_mul_u32_u24 v168, 144, v169\n"
"v_mfma_f32_16x16x32_bf16 v[24:27], v[232:235], v[112:115], v[24:27]\n"
"ds_read_b64_tr_b16 v[226:227], v240 offset:35840\n"
"s_add_u32 s98, s89, 0x1d000\n"
"buffer_load_dwordx4 v[212:215], v254, s[12:15], s98 offen sc1\n"
"v_and_b32 v170, 7, v174\n"
"v_mfma_f32_16x16x32_bf16 v[40:43], v[232:235], v[116:119], v[40:43]\n"
"v_lshl_add_u32 v168, v170, 4, v168\n"
"v_add_u32 v221, 0x0, v168\n"
"s_waitcnt lgkmcnt(2)\n"
"v_mfma_f32_16x16x32_bf16 v[12:15], v[236:239], v[108:111], v[12:15]\n"
"ds_read_b64_tr_b16 v[228:229], v241 offset:34816\n"
"s_mul_i32 s36, s36, 3\n"
"v_mfma_f32_16x16x32_bf16 v[28:31], v[236:239], v[112:115], v[28:31]\n"
"ds_read_b64_tr_b16 v[230:231], v241 offset:35840\n"
"s_add_u32 s98, s89, 0x2e000\n"
"buffer_load_dwordx4 v[216:219], v254, s[12:15], s98 offen sc1\n"
"v_mfma_f32_16x16x32_bf16 v[44:47], v[236:239], v[116:119], v[44:47]\n"
"s_sub_u32 s36, s90, s36\n"
"v_add_u32 v171, 0, v169\n"
"s_waitcnt vmcnt(8)\n"
"s_cmp_lg_u32 s32, 7\n"
"s_cbranch_scc1 .Lok5012\n"
"s_waitcnt vmcnt(5)\n"
"v_cmp_ne_u64 vcc, s[56:57], v[190:191]\n"
"s_nop 1\n"
"s_cmp_eq_u64 vcc, 0\n"
"s_cbranch_scc1 .Lok5012\n"
"s_mov_b32 s94, 0\n"
"s_lshl_b32 s95, s33, 11\n"
"s_add_u32 s95, s95, 0x886000\n"
".Lagain5013:\n"
"s_sleep 1\n"
"buffer_load_dwordx2 v[190:191], v186, s[12:15], s95 offen sc1\n"
"s_add_u32 s94, s94, 1\n"
"s_waitcnt vmcnt(0)\n"
"v_cmp_ne_u64 vcc, s[56:57], v[190:191]\n"
"s_nop 1\n"
"s_cmp_eq_u64 vcc, 0\n"
"s_cbranch_scc1 .Lok5012\n"
"s_cmp_lt_u32 s94, 0x8000\n"
"s_cbranch_scc1 .Lagain5013\n"
".Lok5012:\n"
"s_barrier\n"
"s_waitcnt lgkmcnt(2)\n"
"s_waitcnt vmcnt(7)\n"
"v_mfma_f32_16x16x32_bf16 v[0:3], v[224:227], v[196:199], v[0:3]\n"
"ds_read_b64_tr_b16 v[232:233], v242 offset:34816\n"
"s_add_u32 m0, s88, 0x15400\n"
"s_mul_i32 s96, s1, 491520\n"
"s_add_u32 s96, s96, s87\n"
"buffer_load_dwordx4 v253, s[20:23], s96 offen lds\n"
"s_waitcnt vmcnt(7)\n"
"v_mfma_f32_16x16x32_bf16 v[16:19], v[224:227], v[200:203], v[16:19]\n"
"ds_read_b64_tr_b16 v[234:235], v242 offset:35840\n"
"v_lshrrev_b32 v172, 4, v171\n"
"v_add_u32 v172, s36, v172\n"
"s_waitcnt vmcnt(6)\n"
"v_mfma_f32_16x16x32_bf16 v[32:35], v[224:227], v[204:207], v[32:35]\n"
"v_and_b32 v171, 15, v171\n"
"v_lshlrev_b32 v171, 1, v171\n"
"s_waitcnt lgkmcnt(2)\n"
"v_mfma_f32_16x16x32_bf16 v[4:7], v[228:231], v[196:199], v[4:7]\n"
"ds_read_b64_tr_b16 v[236:237], v243 offset:34816\n"
"s_add_u32 s97, s96, 0x4c00\n"
"buffer_load_dwordx4 v253, s[20:23], s97 offen offset:1024 lds\n"
"v_mfma_f32_16x16x32_bf16 v[20:23], v[228:231], v[200:203], v[20:23]\n"
"ds_read_b64_tr_b16 v[238:239], v243 offset:35840\n"
"v_lshrrev_b32 v173, 2, v172\n"
"v_lshl_add_u32 v171, v173, 6, v171\n"
"v_mfma_f32_16x16x32_bf16 v[36:39], v[228:231], v[204:207], v[36:39]\n"
"s_lshl_b32 s98, s0, 12\n"
"s_add_u32 s98, s98, s89\n"
"s_add_u32 s98, s98, 0xd000\n"
"buffer_load_dwordx4 v[120:123], v254, s[12:15], s98 offen sc1\n"
"v_and_b32 v173, 1, v172\n"
"s_waitcnt lgkmcnt(2)\n"
"v_mfma_f32_16x16x32_bf16 v[8:11], v[232:235], v[196:199], v[8:11]\n"
"ds_read_b64_tr_b16 v[224:225], v240 offset:52224\n"
"v_lshl_add_u32 v171, v173, 5, v171\n"
"v_bfe_u32 v173, v172, 1, 1\n"
"v_mfma_f32_16x16x32_bf16 v[24:27], v[232:235], v[200:203], v[24:27]\n"
"ds_read_b64_tr_b16 v[226:227], v240 offset:53248\n"
"s_add_u32 s98, s98, 0x11000\n"
"buffer_load_dwordx4 v[124:127], v254, s[12:15], s98 offen sc1\n"
"v_add_u32 v171, v171, v173\n"
"v_mfma_f32_16x16x32_bf16 v[40:43], v[232:235], v[204:207], v[40:43]\n"
"v_mul_u32_u24 v171, 1280, v171\n"
"v_lshl_add_u32 v222, v170, 4, v171\n"
"s_waitcnt lgkmcnt(2)\n"
"v_mfma_f32_16x16x32_bf16 v[12:15], v[236:239], v[196:199], v[12:15]\n"
"ds_read_b64_tr_b16 v[228:229], v241 offset:52224\n"
"v_and_b32 v183, 63, v183\n"
"ds_bpermute_b32 v180, v183, v184 offset:0\n"
"v_mfma_f32_16x16x32_bf16 v[28:31], v[236:239], v[200:203], v[28:31]\n"
"ds_read_b64_tr_b16 v[230:231], v241 offset:53248\n"
"s_add_u32 s98, s98, 0x11000\n"
"buffer_load_dwordx4 v[128:131], v254, s[12:15], s98 offen sc1\n"
"v_mfma_f32_16x16x32_bf16 v[44:47], v[236:239], v[204:207], v[44:47]\n"
"ds_bpermute_b32 v181, v183, v184 offset:64\n"
"ds_bpermute_b32 v182, v183, v184 offset:128\n"
"s_waitcnt vmcnt(8)\n"
"s_barrier\n"
"s_waitcnt lgkmcnt(5)\n"
"s_waitcnt vmcnt(7)\n"
"v_mfma_f32_16x16x32_bf16 v[0:3], v[224:227], v[208:211], v[0:3]\n"
"ds_read_b64_tr_b16 v[232:233], v242 offset:52224\n"
"s_add_u32 m0, s88, 0x19400\n"
"s_mul_i32 s96, s58, 491520\n"
"s_add_u32 s96, s96, s87\n"
"buffer_load_dwordx4 v253, s[20:23], s96 offen lds\n"
"s_waitcnt vmcnt(7)\n"
"v_mfma_f32_16x16x32_bf16 v[16:19], v[224:227], v[212:215], v[16:19]\n"
"ds_read_b64_tr_b16 v[234:235], v242 offset:53248\n"
"v_add_u32 v171, 8, v169\n"
"v_lshrrev_b32 v172, 4, v171\n"
"s_waitcnt vmcnt(6)\n"
"v_mfma_f32_16x16x32_bf16 v[32:35], v[224:227], v[216:219], v[32:35]\n"
"v_add_u32 v172, s36, v172\n"
"v_and_b32 v171, 15, v171\n"
"s_waitcnt lgkmcnt(4)\n"
"v_mfma_f32_16x16x32_bf16 v[4:7], v[228:231], v[208:211], v[4:7]\n"
"ds_read_b64_tr_b16 v[236:237], v243 offset:52224\n"
"s_add_u32 s97, s96, 0x4c00\n"
"buffer_load_dwordx4 v253, s[20:23], s97 offen offset:1024 lds\n"
"v_mfma_f32_16x16x32_bf16 v[20:23], v[228:231], v[212:215], v[20:23]\n"
"ds_read_b64_tr_b16 v[238:239], v243 offset:53248\n"
"v_lshlrev_b32 v171, 1, v171\n"
"v_lshrrev_b32 v173, 2, v172\n"
"v_mfma_f32_16x16x32_bf16 v[36:39], v[228:231], v[216:219], v[36:39]\n"
"s_lshl_b32 s98, s1, 12\n"
"s_add_u32 s98, s98, s89\n"
"s_add_u32 s98, s98, 0xd000\n"
"buffer_load_dwordx4 v[132:135], v254, s[12:15], s98 offen sc1\n"
"v_lshl_add_u32 v171, v173, 6, v171\n"
"s_waitcnt lgkmcnt(2)\n"
"v_mfma_f32_16x16x32_bf16 v[8:11], v[232:235], v[208:211], v[8:11]\n"
"v_add_u32 v240, 0x10000, v240\n"
"v_add_u32 v241, 0x10000, v241\n"
"v_add_u32 v242, 0x10000, v242\n"
"v_add_u32 v243, 0x10000, v243\n"
"ds_read_b64_tr_b16 v[224:225], v240 offset:4096\n"
"v_and_b32 v173, 1, v172\n"
"v_mfma_f32_16x16x32_bf16 v[24:27], v[232:235], v[212:215], v[24:27]\n"
"ds_read_b64_tr_b16 v[226:227], v240 offset:5120\n"
"s_add_u32 s98, s98, 0x11000\n"
"buffer_load_dwordx4 v[136:139], v254, s[12:15], s98 offen sc1\n"
"v_lshl_add_u32 v171, v173, 5, v171\n"
"v_bfe_u32 v173, v172, 1, 1\n"
"v_mfma_f32_16x16x32_bf16 v[40:43], v[232:235], v[216:219], v[40:43]\n"
"v_add_u32 v171, v171, v173\n"
"v_mul_u32_u24 v171, 1280, v171\n"
"s_waitcnt lgkmcnt(2)\n"
"v_mfma_f32_16x16x32_bf16 v[12:15], v[236:239], v[208:211], v[12:15]\n"
"ds_read_b64_tr_b16 v[228:229], v241 offset:4096\n"
"v_lshl_add_u32 v223, v170, 4, v171\n"
"v_sub_f32 v183, 1.0, v180\n"
"v_mfma_f32_16x16x32_bf16 v[28:31], v[236:239], v[212:215], v[28:31]\n"
"ds_read_b64_tr_b16 v[230:231], v241 offset:5120\n"
"s_add_u32 s98, s98, 0x11000\n"
"buffer_load_dwordx4 v[140:143], v254, s[12:15], s98 offen sc1\n"
"v_mfma_f32_16x16x32_bf16 v[44:47], v[236:239], v[216:219], v[44:47]\n"
"v_sub_f32 v184, 1.0, v181\n"
"v_sub_f32 v185, 1.0, v182\n"
"s_waitcnt vmcnt(8)\n"
"s_barrier\n"
"s_waitcnt lgkmcnt(2)\n"
"s_waitcnt vmcnt(7)\n"
"v_mfma_f32_16x16x32_bf16 v[48:51], v[224:227], v[120:123], v[48:51]\n"
"ds_read_b64_tr_b16 v[232:233], v242 offset:4096\n"
"s_add_u32 m0, s88, 0x1d400\n"
"s_mul_i32 s96, s59, 491520\n"
"s_add_u32 s96, s96, s87\n"
"buffer_load_dwordx4 v253, s[20:23], s96 offen lds\n"
"s_waitcnt vmcnt(7)\n"
"v_mfma_f32_16x16x32_bf16 v[64:67], v[224:227], v[124:127], v[64:67]\n"
"ds_read_b64_tr_b16 v[234:235], v242 offset:5120\n"
"v_mul_f32 v0, v180, v0\n"
"v_mul_f32 v1, v180, v1\n"
"s_waitcnt vmcnt(6)\n"
"v_mfma_f32_16x16x32_bf16 v[80:83], v[224:227], v[128:131], v[80:83]\n"
"v_mul_f32 v2, v180, v2\n"
"v_mul_f32 v3, v180, v3\n"
"s_waitcnt lgkmcnt(2)\n"
"v_mfma_f32_16x16x32_bf16 v[52:55], v[228:231], v[120:123], v[52:55]\n"
"ds_read_b64_tr_b16 v[236:237], v243 offset:4096\n"
"s_add_u32 s97, s96, 0x4c00\n"
"buffer_load_dwordx4 v253, s[20:23], s97 offen offset:1024 lds\n"
"v_mfma_f32_16x16x32_bf16 v[68:71], v[228:231], v[124:127], v[68:71]\n"
"ds_read_b64_tr_b16 v[238:239], v243 offset:5120\n"
"v_mul_f32 v4, v180, v4\n"
"v_mul_f32 v5, v180, v5\n"
"v_mfma_f32_16x16x32_bf16 v[84:87], v[228:231], v[128:131], v[84:87]\n"
"s_lshl_b32 s98, s58, 12\n"
"s_add_u32 s98, s98, s89\n"
"s_add_u32 s98, s98, 0xd000\n"
"buffer_load_dwordx4 v[144:147], v254, s[12:15], s98 offen sc1\n"
"v_mul_f32 v6, v180, v6\n"
"s_waitcnt lgkmcnt(2)\n"
"v_mfma_f32_16x16x32_bf16 v[56:59], v[232:235], v[120:123], v[56:59]\n"
"ds_read_b64_tr_b16 v[224:225], v240 offset:21504\n"
"v_mul_f32 v7, v180, v7\n"
"v_mul_f32 v8, v180, v8\n"
"v_mfma_f32_16x16x32_bf16 v[72:75], v[232:235], v[124:127], v[72:75]\n"
"ds_read_b64_tr_b16 v[226:227], v240 offset:22528\n"
"s_add_u32 s98, s98, 0x11000\n"
"buffer_load_dwordx4 v[148:151], v254, s[12:15], s98 offen sc1\n"
"v_mul_f32 v9, v180, v9\n"
"v_mfma_f32_16x16x32_bf16 v[88:91], v[232:235], v[128:131], v[88:91]\n"
"v_mul_f32 v10, v180, v10\n"
"v_mul_f32 v11, v180, v11\n"
"s_waitcnt lgkmcnt(2)\n"
"v_mfma_f32_16x16x32_bf16 v[60:63], v[236:239], v[120:123], v[60:63]\n"
"ds_read_b64_tr_b16 v[228:229], v241 offset:21504\n"
"v_mul_f32 v12, v180, v12\n"
"v_mul_f32 v13, v180, v13\n"
"v_mfma_f32_16x16x32_bf16 v[76:79], v[236:239], v[124:127], v[76:79]\n"
"ds_read_b64_tr_b16 v[230:231], v241 offset:22528\n"
"s_add_u32 s98, s98, 0x11000\n"
"buffer_load_dwordx4 v[152:155], v254, s[12:15], s98 offen sc1\n"
"v_mfma_f32_16x16x32_bf16 v[92:95], v[236:239], v[128:131], v[92:95]\n"
"v_mul_f32 v14, v180, v14\n"
"v_mul_f32 v15, v180, v15\n"
"s_waitcnt vmcnt(8)\n"
"s_barrier\n"
"s_waitcnt lgkmcnt(2)\n"
"s_waitcnt vmcnt(7)\n"
"v_mfma_f32_16x16x32_bf16 v[48:51], v[224:227], v[132:135], v[48:51]\n"
"ds_read_b64_tr_b16 v[232:233], v242 offset:21504\n"
"v_mul_f32 v16, v181, v16\n"
"s_waitcnt vmcnt(6)\n"
"v_mfma_f32_16x16x32_bf16 v[64:67], v[224:227], v[136:139], v[64:67]\n"
"ds_read_b64_tr_b16 v[234:235], v242 offset:22528\n"
"s_lshl_b32 s98, s59, 12\n"
"s_add_u32 s98, s98, s89\n"
"s_add_u32 s98, s98, 0xd000\n"
"buffer_load_dwordx4 v[156:159], v254, s[12:15], s98 offen sc1\n"
"v_mul_f32 v17, v181, v17\n"
"s_waitcnt vmcnt(6)\n"
"v_mfma_f32_16x16x32_bf16 v[80:83], v[224:227], v[140:143], v[80:83]\n"
"v_mul_f32 v18, v181, v18\n"
"v_mul_f32 v19, v181, v19\n"
"s_waitcnt lgkmcnt(2)\n"
"v_mfma_f32_16x16x32_bf16 v[52:55], v[228:231], v[132:135], v[52:55]\n"
"ds_read_b64_tr_b16 v[236:237], v243 offset:21504\n"
"v_mul_f32 v20, v181, v20\n"
"v_mul_f32 v21, v181, v21\n"
"v_mfma_f32_16x16x32_bf16 v[68:71], v[228:231], v[136:139], v[68:71]\n"
"ds_read_b64_tr_b16 v[238:239], v243 offset:22528\n"
"v_mul_f32 v22, v181, v22\n"
"v_mfma_f32_16x16x32_bf16 v[84:87], v[228:231], v[140:143], v[84:87]\n"
"s_add_u32 s98, s98, 0x11000\n"
"buffer_load_dwordx4 v[160:163], v254, s[12:15], s98 offen sc1\n"
"s_waitcnt lgkmcnt(2)\n"
"v_mfma_f32_16x16x32_bf16 v[56:59], v[232:235], v[132:135], v[56:59]\n"
"ds_read_b64_tr_b16 v[224:225], v240 offset:37888\n"
"v_mul_f32 v23, v181, v23\n"
"v_mul_f32 v24, v181, v24\n"
"v_mfma_f32_16x16x32_bf16 v[72:75], v[232:235], v[136:139], v[72:75]\n"
"ds_read_b64_tr_b16 v[226:227], v240 offset:38912\n"
"v_mul_f32 v25, v181, v25\n"
"v_mul_f32 v26, v181, v26\n"
"v_mfma_f32_16x16x32_bf16 v[88:91], v[232:235], v[140:143], v[88:91]\n"
"v_mul_f32 v27, v181, v27\n"
"s_waitcnt lgkmcnt(2)\n"
"v_mfma_f32_16x16x32_bf16 v[60:63], v[236:239], v[132:135], v[60:63]\n"
"ds_read_b64_tr_b16 v[228:229], v241 offset:37888\n"
"s_add_u32 s98, s98, 0x11000\n"
"buffer_load_dwordx4 v[164:167], v254, s[12:15], s98 offen sc1\n"
"v_mfma_f32_16x16x32_bf16 v[76:79], v[236:239], v[136:139], v[76:79]\n"
"ds_read_b64_tr_b16 v[230:231], v241 offset:38912\n"
"v_mul_f32 v28, v181, v28\n"
"v_mul_f32 v29, v181, v29\n"
"v_mfma_f32_16x16x32_bf16 v[92:95], v[236:239], v[140:143], v[92:95]\n"
"v_mul_f32 v30, v181, v30\n"
"v_mul_f32 v31, v181, v31\n"
"s_waitcnt vmcnt(6)\n"
"s_barrier\n"
"s_waitcnt lgkmcnt(2)\n"
"s_waitcnt vmcnt(5)\n"
"v_mfma_f32_16x16x32_bf16 v[48:51], v[224:227], v[144:147], v[48:51]\n"
"ds_read_b64_tr_b16 v[232:233], v242 offset:37888\n"
"v_mul_f32 v32, v182, v32\n"
"s_waitcnt vmcnt(4)\n"
"v_mfma_f32_16x16x32_bf16 v[64:67], v[224:227], v[148:151], v[64:67]\n"
"ds_read_b64_tr_b16 v[234:235], v242 offset:38912\n"
"v_mul_f32 v33, v182, v33\n"
"s_waitcnt vmcnt(3)\n"
"v_mfma_f32_16x16x32_bf16 v[80:83], v[224:227], v[152:155], v[80:83]\n"
"v_mul_f32 v34, v182, v34\n"
"v_mul_f32 v35, v182, v35\n"
"s_waitcnt lgkmcnt(2)\n"
"v_mfma_f32_16x16x32_bf16 v[52:55], v[228:231], v[144:147], v[52:55]\n"
"ds_read_b64_tr_b16 v[236:237], v243 offset:37888\n"
"v_mul_f32 v36, v182, v36\n"
"v_mul_f32 v37, v182, v37\n"
"v_mfma_f32_16x16x32_bf16 v[68:71], v[228:231], v[148:151], v[68:71]\n"
"ds_read_b64_tr_b16 v[238:239], v243 offset:38912\n"
"v_mul_f32 v38, v182, v38\n"
"v_mfma_f32_16x16x32_bf16 v[84:87], v[228:231], v[152:155], v[84:87]\n"
"v_mul_f32 v39, v182, v39\n"
"s_waitcnt lgkmcnt(2)\n"
"v_mfma_f32_16x16x32_bf16 v[56:59], v[232:235], v[144:147], v[56:59]\n"
"ds_read_b64_tr_b16 v[224:225], v240 offset:54272\n"
"v_mul_f32 v40, v182, v40\n"
"v_mul_f32 v41, v182, v41\n"
"v_mfma_f32_16x16x32_bf16 v[72:75], v[232:235], v[148:151], v[72:75]\n"
"ds_read_b64_tr_b16 v[226:227], v240 offset:55296\n"
"v_mul_f32 v42, v182, v42\n"
"v_mfma_f32_16x16x32_bf16 v[88:91], v[232:235], v[152:155], v[88:91]\n"
"v_mul_f32 v43, v182, v43\n"
"s_waitcnt lgkmcnt(2)\n"
"v_mfma_f32_16x16x32_bf16 v[60:63], v[236:239], v[144:147], v[60:63]\n"
"ds_read_b64_tr_b16 v[228:229], v241 offset:54272\n"
"v_mul_f32 v44, v182, v44\n"
"v_mul_f32 v45, v182, v45\n"
"v_mfma_f32_16x16x32_bf16 v[76:79], v[236:239], v[148:151], v[76:79]\n"
"ds_read_b64_tr_b16 v[230:231], v241 offset:55296\n"
"v_mul_f32 v46, v182, v46\n"
"v_mfma_f32_16x16x32_bf16 v[92:95], v[236:239], v[152:155], v[92:95]\n"
"v_mul_f32 v47, v182, v47\n"
"s_waitcnt lgkmcnt(2)\n"
"s_waitcnt vmcnt(2)\n"
"v_mfma_f32_16x16x32_bf16 v[48:51], v[224:227], v[156:159], v[48:51]\n"
"ds_read_b64_tr_b16 v[232:233], v242 offset:54272\n"
"s_waitcnt vmcnt(1)\n"
"v_mfma_f32_16x16x32_bf16 v[64:67], v[224:227], v[160:163], v[64:67]\n"
"ds_read_b64_tr_b16 v[234:235], v242 offset:55296\n"
"s_waitcnt vmcnt(0)\n"
"v_mfma_f32_16x16x32_bf16 v[80:83], v[224:227], v[164:167], v[80:83]\n"
"s_waitcnt lgkmcnt(2)\n"
"v_mfma_f32_16x16x32_bf16 v[52:55], v[228:231], v[156:159], v[52:55]\n"
"ds_read_b64_tr_b16 v[236:237], v243 offset:54272\n"
"v_mfma_f32_16x16x32_bf16 v[68:71], v[228:231], v[160:163], v[68:71]\n"
"ds_read_b64_tr_b16 v[238:239], v243 offset:55296\n"
"v_mfma_f32_16x16x32_bf16 v[84:87], v[228:231], v[164:167], v[84:87]\n"
"s_waitcnt lgkmcnt(2)\n"
"v_mfma_f32_16x16x32_bf16 v[56:59], v[232:235], v[156:159], v[56:59]\n"
"v_fmac_f32 v0, v183, v48\n"
"v_fmac_f32 v1, v183, v49\n"
"v_fmac_f32 v2, v183, v50\n"
"v_fmac_f32 v3, v183, v51\n"
"v_cvt_pk_bf16_f32 v0, v0, v1\n"
"v_cvt_pk_bf16_f32 v1, v2, v3\n"
"ds_write_b64 v220, v[0:1] offset:0\n"
"v_mfma_f32_16x16x32_bf16 v[72:75], v[232:235], v[160:163], v[72:75]\n"
"v_fmac_f32 v16, v184, v64\n"
"v_fmac_f32 v17, v184, v65\n"
"v_fmac_f32 v18, v184, v66\n"
"v_fmac_f32 v19, v184, v67\n"
"v_cvt_pk_bf16_f32 v16, v16, v17\n"
"v_cvt_pk_bf16_f32 v17, v18, v19\n"
"ds_write_b64 v220, v[16:17] offset:2304\n"
"v_mfma_f32_16x16x32_bf16 v[88:91], v[232:235], v[164:167], v[88:91]\n"
"v_fmac_f32 v32, v185, v80\n"
"v_fmac_f32 v33, v185, v81\n"
"v_fmac_f32 v34, v185, v82\n"
"v_fmac_f32 v35, v185, v83\n"
"v_cvt_pk_bf16_f32 v32, v32, v33\n"
"v_cvt_pk_bf16_f32 v33, v34, v35\n"
"ds_write_b64 v220, v[32:33] offset:4608\n"
"s_waitcnt lgkmcnt(3)\n"
"v_mfma_f32_16x16x32_bf16 v[60:63], v[236:239], v[156:159], v[60:63]\n"
"v_fmac_f32 v4, v183, v52\n"
"v_fmac_f32 v5, v183, v53\n"
"v_fmac_f32 v6, v183, v54\n"
"v_fmac_f32 v7, v183, v55\n"
"v_cvt_pk_bf16_f32 v4, v4, v5\n"
"v_cvt_pk_bf16_f32 v5, v6, v7\n"
"ds_write_b64 v220, v[4:5] offset:32\n"
"v_mfma_f32_16x16x32_bf16 v[76:79], v[236:239], v[160:163], v[76:79]\n"
"v_fmac_f32 v20, v184, v68\n"
"v_fmac_f32 v21, v184, v69\n"
"v_fmac_f32 v22, v184, v70\n"
"v_fmac_f32 v23, v184, v71\n"
"v_cvt_pk_bf16_f32 v20, v20, v21\n"
"v_cvt_pk_bf16_f32 v21, v22, v23\n"
"ds_write_b64 v220, v[20:21] offset:2336\n"
"v_mfma_f32_16x16x32_bf16 v[92:95], v[236:239], v[164:167], v[92:95]\n"
"v_fmac_f32 v36, v185, v84\n"
"v_fmac_f32 v37, v185, v85\n"
"v_fmac_f32 v38, v185, v86\n"
"v_fmac_f32 v39, v185, v87\n"
"v_cvt_pk_bf16_f32 v36, v36, v37\n"
"v_cvt_pk_bf16_f32 v37, v38, v39\n"
"ds_write_b64 v220, v[36:37] offset:4640\n"
"v_fmac_f32 v8, v183, v56\n"
"v_fmac_f32 v9, v183, v57\n"
"v_fmac_f32 v10, v183, v58\n"
"v_fmac_f32 v11, v183, v59\n"
"v_cvt_pk_bf16_f32 v8, v8, v9\n"
"v_cvt_pk_bf16_f32 v9, v10, v11\n"
"ds_write_b64 v220, v[8:9] offset:64\n"
"v_fmac_f32 v24, v184, v72\n"
"v_fmac_f32 v25, v184, v73\n"
"v_fmac_f32 v26, v184, v74\n"
"v_fmac_f32 v27, v184, v75\n"
"v_cvt_pk_bf16_f32 v24, v24, v25\n"
"v_cvt_pk_bf16_f32 v25, v26, v27\n"
"ds_write_b64 v220, v[24:25] offset:2368\n"
"v_fmac_f32 v40, v185, v88\n"
"v_fmac_f32 v41, v185, v89\n"
"v_fmac_f32 v42, v185, v90\n"
"v_fmac_f32 v43, v185, v91\n"
"v_cvt_pk_bf16_f32 v40, v40, v41\n"
"v_cvt_pk_bf16_f32 v41, v42, v43\n"
"ds_write_b64 v220, v[40:41] offset:4672\n"
"v_fmac_f32 v12, v183, v60\n"
"v_fmac_f32 v13, v183, v61\n"
"v_fmac_f32 v14, v183, v62\n"
"v_fmac_f32 v15, v183, v63\n"
"v_cvt_pk_bf16_f32 v12, v12, v13\n"
"v_cvt_pk_bf16_f32 v13, v14, v15\n"
"ds_write_b64 v220, v[12:13] offset:96\n"
"v_fmac_f32 v28, v184, v76\n"
"v_fmac_f32 v29, v184, v77\n"
"v_fmac_f32 v30, v184, v78\n"
"v_fmac_f32 v31, v184, v79\n"
"v_cvt_pk_bf16_f32 v28, v28, v29\n"
"v_cvt_pk_bf16_f32 v29, v30, v31\n"
"ds_write_b64 v220, v[28:29] offset:2400\n"
"v_fmac_f32 v44, v185, v92\n"
"v_fmac_f32 v45, v185, v93\n"
"v_fmac_f32 v46, v185, v94\n"
"v_fmac_f32 v47, v185, v95\n"
"v_cvt_pk_bf16_f32 v44, v44, v45\n"
"v_cvt_pk_bf16_f32 v45, v46, v47\n"
"ds_write_b64 v220, v[44:45] offset:4704\n"
"s_waitcnt lgkmcnt(0)\n"
"s_barrier\n"
"ds_read_b128 v[96:99], v221 offset:0\n"
"ds_read_b128 v[100:103], v221 offset:13824\n"
"ds_read_b128 v[104:107], v221 offset:27648\n"
"ds_read_b128 v[108:111], v221 offset:41472\n"
"ds_read_b128 v[120:123], v221 offset:1152\n"
"ds_read_b128 v[124:127], v221 offset:14976\n"
"ds_read_b128 v[128:131], v221 offset:28800\n"
"ds_read_b128 v[132:135], v221 offset:42624\n"
"s_waitcnt lgkmcnt(7)\n"
"v_lshlrev_b32 v112, 16, v96\n"
"v_and_b32 v113, 0xffff0000, v96\n"
"v_lshlrev_b32 v114, 16, v97\n"
"v_and_b32 v115, 0xffff0000, v97\n"
"v_lshlrev_b32 v116, 16, v98\n"
"v_and_b32 v117, 0xffff0000, v98\n"
"v_lshlrev_b32 v118, 16, v99\n"
"v_and_b32 v119, 0xffff0000, v99\n"
"s_waitcnt lgkmcnt(6)\n"
"v_dot2c_f32_bf16 v112, s40, v100\n"
"v_dot2c_f32_bf16 v113, s41, v100\n"
"v_dot2c_f32_bf16 v114, s40, v101\n"
"v_dot2c_f32_bf16 v115, s41, v101\n"
"v_dot2c_f32_bf16 v116, s40, v102\n"
"v_dot2c_f32_bf16 v117, s41, v102\n"
"v_dot2c_f32_bf16 v118, s40, v103\n"
"v_dot2c_f32_bf16 v119, s41, v103\n"
"s_waitcnt lgkmcnt(5)\n"
"v_dot2c_f32_bf16 v112, s40, v104\n"
"v_dot2c_f32_bf16 v113, s41, v104\n"
"v_dot2c_f32_bf16 v114, s40, v105\n"
"v_dot2c_f32_bf16 v115, s41, v105\n"
"v_dot2c_f32_bf16 v116, s40, v106\n"
"v_dot2c_f32_bf16 v117, s41, v106\n"
"v_dot2c_f32_bf16 v118, s40, v107\n"
"v_dot2c_f32_bf16 v119, s41, v107\n"
"s_waitcnt lgkmcnt(4)\n"
"v_dot2c_f32_bf16 v112, s40, v108\n"
"v_dot2c_f32_bf16 v113, s41, v108\n"
"v_dot2c_f32_bf16 v114, s40, v109\n"
"v_dot2c_f32_bf16 v115, s41, v109\n"
"v_dot2c_f32_bf16 v116, s40, v110\n"
"v_dot2c_f32_bf16 v117, s41, v110\n"
"v_dot2c_f32_bf16 v118, s40, v111\n"
"v_dot2c_f32_bf16 v119, s41, v111\n"
"s_nop 2\n"
"v_cvt_pk_bf16_f32 v96, v112, v113\n"
"v_cvt_pk_bf16_f32 v97, v114, v115\n"
"v_cvt_pk_bf16_f32 v98, v116, v117\n"
"v_cvt_pk_bf16_f32 v99, v118, v119\n"
"buffer_store_dwordx4 v[96:99], v222, s[24:27], s91 offen sc1\n"
"s_waitcnt lgkmcnt(3)\n"
"v_lshlrev_b32 v136, 16, v120\n"
"v_and_b32 v137, 0xffff0000, v120\n"
"v_lshlrev_b32 v138, 16, v121\n"
"v_and_b32 v139, 0xffff0000, v121\n"
"v_lshlrev_b32 v140, 16, v122\n"
"v_and_b32 v141, 0xffff0000, v122\n"
"v_lshlrev_b32 v142, 16, v123\n"
"v_and_b32 v143, 0xffff0000, v123\n"
"s_waitcnt lgkmcnt(2)\n"
"v_dot2c_f32_bf16 v136, s40, v124\n"
"v_dot2c_f32_bf16 v137, s41, v124\n"
"v_dot2c_f32_bf16 v138, s40, v125\n"
"v_dot2c_f32_bf16 v139, s41, v125\n"
"v_dot2c_f32_bf16 v140, s40, v126\n"
"v_dot2c_f32_bf16 v141, s41, v126\n"
"v_dot2c_f32_bf16 v142, s40, v127\n"
"v_dot2c_f32_bf16 v143, s41, v127\n"
"s_waitcnt lgkmcnt(1)\n"
"v_dot2c_f32_bf16 v136, s40, v128\n"
"v_dot2c_f32_bf16 v137, s41, v128\n"
"v_dot2c_f32_bf16 v138, s40, v129\n"
"v_dot2c_f32_bf16 v139, s41, v129\n"
"v_dot2c_f32_bf16 v140, s40, v130\n"
"v_dot2c_f32_bf16 v141, s41, v130\n"
"v_dot2c_f32_bf16 v142, s40, v131\n"
"v_dot2c_f32_bf16 v143, s41, v131\n"
"s_waitcnt lgkmcnt(0)\n"
"v_dot2c_f32_bf16 v136, s40, v132\n"
"v_dot2c_f32_bf16 v137, s41, v132\n"
"v_dot2c_f32_bf16 v138, s40, v133\n"
"v_dot2c_f32_bf16 v139, s41, v133\n"
"v_dot2c_f32_bf16 v140, s40, v134\n"
"v_dot2c_f32_bf16 v141, s41, v134\n"
"v_dot2c_f32_bf16 v142, s40, v135\n"
"v_dot2c_f32_bf16 v143, s41, v135\n"
"s_nop 2\n"
"v_cvt_pk_bf16_f32 v120, v136, v137\n"
"v_cvt_pk_bf16_f32 v121, v138, v139\n"
"v_cvt_pk_bf16_f32 v122, v140, v141\n"
"v_cvt_pk_bf16_f32 v123, v142, v143\n"
"s_mov_b32 exec_hi, 0\n"
"buffer_store_dwordx4 v[120:123], v223, s[24:27], s91 offen sc1\n"
"s_mov_b32 exec_hi, -1\n"
"s_endpgm\n"
".Lqloop1:\n"
"s_waitcnt lgkmcnt(2)\n"
"v_mfma_f32_16x16x32_bf16 v[0:3], v[208:211], v[72:75], 0\n"
"ds_read_b64_tr_b16 v[216:217], v240 offset:640\n"
"v_mfma_f32_16x16x32_bf16 v[4:7], v[208:211], v[76:79], 0\n"
"ds_read_b64_tr_b16 v[218:219], v240 offset:4992\n"
"s_add_u32 m0, s50, 0x22800\n"
"s_add_u32 s41, s45, 256\n"
"buffer_load_dwordx4 v247, s[4:7], s41 offen lds\n"
"v_mfma_f32_16x16x32_bf16 v[8:11], v[208:211], v[80:83], 0\n"
"ds_read_b128 v[92:95], v243 offset:0\n"
"v_mfma_f32_16x16x32_bf16 v[12:15], v[208:211], v[84:87], 0\n"
"ds_read_b128 v[96:99], v243 offset:2048\n"
"s_waitcnt lgkmcnt(4)\n"
"v_mfma_f32_16x16x32_bf16 v[16:19], v[212:215], v[72:75], 0\n"
"ds_read_b64_tr_b16 v[220:221], v241 offset:0\n"
"v_mfma_f32_16x16x32_bf16 v[20:23], v[212:215], v[76:79], 0\n"
"ds_read_b64_tr_b16 v[222:223], v241 offset:4352\n"
"buffer_load_dwordx4 v248, s[4:7], s41 offen offset:1024 lds\n"
"v_mfma_f32_16x16x32_bf16 v[24:27], v[212:215], v[80:83], 0\n"
"ds_read_b128 v[100:103], v243 offset:4096\n"
"v_mfma_f32_16x16x32_bf16 v[28:31], v[212:215], v[84:87], 0\n"
"ds_read_b128 v[104:107], v243 offset:6144\n"
"s_waitcnt lgkmcnt(6)\n"
"v_mfma_f32_16x16x32_bf16 v[32:35], v[72:75], v[216:219], 0\n"
"ds_read_b64_tr_b16 v[208:209], v240 offset:17408\n"
"v_mfma_f32_16x16x32_bf16 v[36:39], v[76:79], v[216:219], 0\n"
"ds_read_b64_tr_b16 v[210:211], v240 offset:21760\n"
"s_add_u32 m0, s48, 0x11000\n"
"s_add_u32 s43, s46, 0x78000\n"
"buffer_load_dwordx4 v246, s[8:11], s43 offen lds\n"
"v_mfma_f32_16x16x32_bf16 v[40:43], v[80:83], v[216:219], 0\n"
"ds_read_b128 v[108:111], v245 offset:0\n"
"v_mfma_f32_16x16x32_bf16 v[44:47], v[84:87], v[216:219], 0\n"
"v_mfma_f32_16x16x32_bf16 v[64:67], v[88:91], v[216:219], 0\n"
"s_waitcnt lgkmcnt(5)\n"
"v_mfma_f32_16x16x32_bf16 v[48:51], v[220:223], v[72:75], 0\n"
"ds_read_b64_tr_b16 v[212:213], v240 offset:17728\n"
"v_mfma_f32_16x16x32_bf16 v[52:55], v[220:223], v[76:79], 0\n"
"ds_read_b64_tr_b16 v[214:215], v240 offset:22080\n"
"s_add_u32 s44, s46, 0x799c0\n"
"buffer_load_dwordx4 v246, s[8:11], s44 offen offset:1088 lds\n"
"v_mfma_f32_16x16x32_bf16 v[56:59], v[220:223], v[80:83], 0\n"
"v_mfma_f32_16x16x32_bf16 v[60:63], v[220:223], v[84:87], 0\n"
"s_waitcnt vmcnt(7)\n"
"s_waitcnt lgkmcnt(2)\n"
"s_barrier\n"
"v_mfma_f32_16x16x32_bf16 v[0:3], v[208:211], v[92:95], v[0:3]\n"
"ds_read_b64_tr_b16 v[216:217], v240 offset:18048\n"
"s_lshl_b32 s52, s39, 3\n"
"v_mfma_f32_16x16x32_bf16 v[4:7], v[208:211], v[96:99], v[4:7]\n"
"ds_read_b64_tr_b16 v[218:219], v240 offset:22400\n"
"s_add_u32 s49, s52, s40\n"
"s_lshl_b32 s52, s49, 7\n"
"v_mfma_f32_16x16x32_bf16 v[8:11], v[208:211], v[100:103], v[8:11]\n"
"ds_read_b128 v[72:75], v242 offset:18432\n"
"s_add_u32 m0, s48, 0x0\n"
"s_add_u32 s43, s46, 0x96000\n"
"buffer_load_dwordx4 v246, s[8:11], s43 offen lds\n"
"s_mov_b32 s12, s80\n"
"s_and_b32 s13, s81, 0xffff\n"
"s_mov_b32 s14, 0xa00000\n"
"v_mfma_f32_16x16x32_bf16 v[12:15], v[208:211], v[104:107], v[12:15]\n"
"ds_read_b128 v[76:79], v242 offset:20480\n"
"s_mov_b32 s15, 0x20000\n"
"s_mov_b32 s16, s68\n"
"s_waitcnt lgkmcnt(4)\n"
"v_mfma_f32_16x16x32_bf16 v[16:19], v[212:215], v[92:95], v[16:19]\n"
"v_add_u32 v241, s47, v241\n"
"ds_read_b64_tr_b16 v[220:221], v241 offset:17408\n"
"s_and_b32 s17, s69, 0xffff\n"
"s_mov_b32 s18, 0xc8000\n"
"v_mfma_f32_16x16x32_bf16 v[20:23], v[212:215], v[96:99], v[20:23]\n"
"ds_read_b64_tr_b16 v[222:223], v241 offset:21760\n"
"s_mov_b32 s19, 0x20000\n"
"s_mov_b32 s20, s74\n"
"v_mfma_f32_16x16x32_bf16 v[24:27], v[212:215], v[100:103], v[24:27]\n"
"ds_read_b128 v[80:83], v242 offset:22528\n"
"s_and_b32 s21, s75, 0xffff\n"
"s_mov_b32 s22, 0x1e0000\n"
"v_mfma_f32_16x16x32_bf16 v[28:31], v[212:215], v[104:107], v[28:31]\n"
"ds_read_b128 v[84:87], v242 offset:24576\n"
"s_mov_b32 s23, 0x20000\n"
"s_mov_b32 s24, s78\n"
"s_waitcnt lgkmcnt(6)\n"
"v_mfma_f32_16x16x32_bf16 v[32:35], v[92:95], v[216:219], v[32:35]\n"
"ds_read_b64_tr_b16 v[208:209], v240 offset:34816\n"
"s_and_b32 s25, s79, 0xffff\n"
"s_mov_b32 s26, 0x280000\n"
"s_mov_b32 s27, 0x20000\n"
"v_mfma_f32_16x16x32_bf16 v[36:39], v[96:99], v[216:219], v[36:39]\n"
"ds_read_b64_tr_b16 v[210:211], v240 offset:39168\n"
"s_sub_u32 s35, s34, 16\n"
"s_lshr_b32 s91, s35, 1\n"
"v_mfma_f32_16x16x32_bf16 v[40:43], v[100:103], v[216:219], v[40:43]\n"
"ds_read_b128 v[88:91], v244 offset:18432\n"
"s_and_b32 s35, s35, 1\n"
"s_mul_i32 s35, s35, 6\n"
"v_mfma_f32_16x16x32_bf16 v[44:47], v[104:107], v[216:219], v[44:47]\n"
"s_add_u32 s44, s46, 0x979c0\n"
"buffer_load_dwordx4 v246, s[8:11], s44 offen offset:1088 lds\n"
"s_lshl_b32 s90, s33, 4\n"
"s_add_u32 s90, s90, s35\n"
"v_mfma_f32_16x16x32_bf16 v[64:67], v[108:111], v[216:219], v[64:67]\n"
"s_add_u32 s90, s90, 4\n"
"s_and_b32 s35, s32, 3\n"
"s_waitcnt lgkmcnt(5)\n"
"v_mfma_f32_16x16x32_bf16 v[48:51], v[220:223], v[92:95], v[48:51]\n"
"ds_read_b64_tr_b16 v[212:213], v240 offset:35136\n"
"s_lshr_b32 s36, s32, 2\n"
"s_lshl_b32 s91, s91, 7\n"
"v_mfma_f32_16x16x32_bf16 v[52:55], v[220:223], v[96:99], v[52:55]\n"
"ds_read_b64_tr_b16 v[214:215], v240 offset:39488\n"
"s_mul_i32 s38, s36, 25600\n"
"s_mul_i32 s85, s35, 51200\n"
"s_add_u32 s85, s85, s38\n"
"v_mfma_f32_16x16x32_bf16 v[56:59], v[220:223], v[100:103], v[56:59]\n"
"s_add_u32 s85, s85, s91\n"
"s_mul_i32 s38, s36, 10240\n"
"s_add_u32 s38, s38, s91\n"
"v_mfma_f32_16x16x32_bf16 v[60:63], v[220:223], v[104:107], v[60:63]\n"
"s_mul_i32 s87, s35, 122880\n"
"s_add_u32 s87, s87, s38\n"
"s_waitcnt vmcnt(7)\n"
"s_barrier\n"
"s_waitcnt lgkmcnt(3)\n"
"v_mfma_f32_16x16x32_bf16 v[0:3], v[208:211], v[72:75], v[0:3]\n"
"ds_read_b64_tr_b16 v[216:217], v240 offset:35456\n"
"s_bitcmp1_b32 s35, 0\n"
"s_cselect_b32 s38, 40960, 0\n"
"v_mfma_f32_16x16x32_bf16 v[4:7], v[208:211], v[76:79], v[4:7]\n"
"ds_read_b64_tr_b16 v[218:219], v240 offset:39808\n"
"s_add_u32 m0, s50, 0x19800\n"
"s_add_u32 s41, s45, 384\n"
"buffer_load_dwordx4 v247, s[4:7], s41 offen lds\n"
"s_add_u32 s84, s87, s38\n"
"v_mfma_f32_16x16x32_bf16 v[8:11], v[208:211], v[80:83], v[8:11]\n"
"ds_read_b128 v[92:95], v243 offset:18432\n"
"s_bitcmp1_b32 s35, 0\n"
"s_cselect_b32 s38, 0, 81920\n"
"s_add_u32 s87, s87, s38\n"
"v_mfma_f32_16x16x32_bf16 v[12:15], v[208:211], v[84:87], v[12:15]\n"
"ds_read_b128 v[96:99], v243 offset:20480\n"
"s_lshl_b32 s86, s35, 1\n"
"s_add_u32 s86, s86, s36\n"
"s_waitcnt lgkmcnt(4)\n"
"v_mfma_f32_16x16x32_bf16 v[16:19], v[212:215], v[72:75], v[16:19]\n"
"v_add_u32 v241, s47, v241\n"
"ds_read_b64_tr_b16 v[220:221], v241 offset:34816\n"
"s_mul_i32 s86, s86, 102400\n"
"s_add_u32 s86, s86, s91\n"
"v_mfma_f32_16x16x32_bf16 v[20:23], v[212:215], v[76:79], v[20:23]\n"
"ds_read_b64_tr_b16 v[222:223], v241 offset:39168\n"
"buffer_load_dwordx4 v248, s[4:7], s41 offen offset:1024 lds\n"
"s_add_u32 s86, s86, 20480\n"
"s_lshl_b32 s88, s35, 12\n"
"v_mfma_f32_16x16x32_bf16 v[24:27], v[212:215], v[80:83], v[24:27]\n"
"ds_read_b128 v[100:103], v243 offset:22528\n"
"s_lshl_b32 s37, s36, 11\n"
"s_add_u32 s88, s88, s37\n"
"v_mfma_f32_16x16x32_bf16 v[28:31], v[212:215], v[84:87], v[28:31]\n"
"ds_read_b128 v[104:107], v243 offset:24576\n"
"s_mul_i32 s37, s36, 3\n"
"s_add_u32 s90, s90, s37\n"
"s_waitcnt lgkmcnt(6)\n"
"v_mfma_f32_16x16x32_bf16 v[32:35], v[72:75], v[216:219], v[32:35]\n"
"v_add_u32 v240, 0xcc00, v240\n"
"ds_read_b64_tr_b16 v[208:209], v240 offset:0\n"
"s_mul_i32 s89, s90, 69632\n"
"s_lshl_b32 s37, s35, 10\n"
"s_add_u32 s89, s89, s37\n"
"v_mfma_f32_16x16x32_bf16 v[36:39], v[76:79], v[216:219], v[36:39]\n"
"ds_read_b64_tr_b16 v[210:211], v240 offset:4352\n"
"s_add_u32 m0, s48, 0x4400\n"
"s_add_u32 s43, s46, 0xb4000\n"
"buffer_load_dwordx4 v246, s[8:11], s43 offen lds\n"
"v_lshrrev_b32 v120, 5, v127\n"
"v_mul_u32_u24 v120, 4, v120\n"
"v_mfma_f32_16x16x32_bf16 v[40:43], v[80:83], v[216:219], v[40:43]\n"
"ds_read_b128 v[108:111], v245 offset:18432\n"
"v_bfe_u32 v121, v127, 3, 2\n"
"v_add_u32 v120, v120, v121\n"
"v_mfma_f32_16x16x32_bf16 v[44:47], v[84:87], v[216:219], v[44:47]\n"
"v_mul_u32_u24 v120, 1280, v120\n"
"v_bfe_u32 v121, v127, 1, 2\n"
"v_mfma_f32_16x16x32_bf16 v[64:67], v[88:91], v[216:219], v[64:67]\n"
"v_lshrrev_b32 v122, 4, v127\n"
"v_xor_b32 v121, v121, v122\n"
"s_waitcnt lgkmcnt(5)\n"
"v_mfma_f32_16x16x32_bf16 v[48:51], v[220:223], v[72:75], v[48:51]\n"
"ds_read_b64_tr_b16 v[212:213], v240 offset:320\n"
"v_lshl_add_u32 v120, v121, 5, v120\n"
"v_and_b32 v121, 1, v127\n"
"v_mfma_f32_16x16x32_bf16 v[52:55], v[220:223], v[76:79], v[52:55]\n"
"ds_read_b64_tr_b16 v[214:215], v240 offset:4672\n"
"s_add_u32 s44, s46, 0xb59c0\n"
"buffer_load_dwordx4 v246, s[8:11], s44 offen offset:1088 lds\n"
"v_lshl_add_u32 v253, v121, 4, v120\n"
"v_lshrrev_b32 v120, 5, v127\n"
"v_mul_u32_u24 v120, 8, v120\n"
"v_mfma_f32_16x16x32_bf16 v[56:59], v[220:223], v[80:83], v[56:59]\n"
"v_bfe_u32 v121, v127, 3, 2\n"
"v_add_u32 v120, v120, v121\n"
"v_mul_u32_u24 v120, 1280, v120\n"
"v_mfma_f32_16x16x32_bf16 v[60:63], v[220:223], v[84:87], v[60:63]\n"
"v_bfe_u32 v121, v127, 1, 2\n"
"v_lshrrev_b32 v122, 4, v127\n"
"s_waitcnt vmcnt(6)\n"
"s_waitcnt lgkmcnt(2)\n"
"s_barrier\n"
"v_mfma_f32_16x16x32_bf16 v[0:3], v[208:211], v[92:95], v[0:3]\n"
"ds_read_b64_tr_b16 v[216:217], v240 offset:640\n"
"v_xor_b32 v121, v121, v122\n"
"v_mfma_f32_16x16x32_bf16 v[4:7], v[208:211], v[96:99], v[4:7]\n"
"ds_read_b64_tr_b16 v[218:219], v240 offset:4992\n"
"s_add_u32 m0, s48, 0x8800\n"
"s_add_u32 s43, s46, 0xd2000\n"
"buffer_load_dwordx4 v246, s[8:11], s43 offen lds\n"
"v_lshl_add_u32 v120, v121, 5, v120\n"
"v_and_b32 v121, 1, v127\n"
"v_mfma_f32_16x16x32_bf16 v[8:11], v[208:211], v[100:103], v[8:11]\n"
"ds_read_b128 v[72:75], v242 offset:36864\n"
"v_lshl_add_u32 v252, v121, 4, v120\n"
"v_lshrrev_b32 v120, 5, v127\n"
"v_mul_u32_u24 v120, 40, v120\n"
"v_mfma_f32_16x16x32_bf16 v[12:15], v[208:211], v[104:107], v[12:15]\n"
"ds_read_b128 v[76:79], v242 offset:38912\n"
"v_bfe_u32 v121, v127, 3, 2\n"
"v_add_u32 v120, v120, v121\n"
"s_waitcnt lgkmcnt(4)\n"
"v_mfma_f32_16x16x32_bf16 v[16:19], v[212:215], v[92:95], v[16:19]\n"
"v_add_u32 v241, s95, v241\n"
"ds_read_b64_tr_b16 v[220:221], v241 offset:0\n"
"v_mul_u32_u24 v120, 1280, v120\n"
"v_bfe_u32 v121, v127, 1, 2\n"
"v_mfma_f32_16x16x32_bf16 v[20:23], v[212:215], v[96:99], v[20:23]\n"
"ds_read_b64_tr_b16 v[222:223], v241 offset:4352\n"
"v_lshrrev_b32 v122, 4, v127\n"
"v_xor_b32 v121, v121, v122\n"
"v_mfma_f32_16x16x32_bf16 v[24:27], v[212:215], v[100:103], v[24:27]\n"
"ds_read_b128 v[80:83], v242 offset:40960\n"
"v_lshl_add_u32 v120, v121, 5, v120\n"
"v_and_b32 v121, 1, v127\n"
"v_lshl_add_u32 v251, v121, 4, v120\n"
"v_mfma_f32_16x16x32_bf16 v[28:31], v[212:215], v[104:107], v[28:31]\n"
"ds_read_b128 v[84:87], v242 offset:43008\n"
"s_add_u32 s44, s46, 0xd39c0\n"
"buffer_load_dwordx4 v246, s[8:11], s44 offen offset:1088 lds\n"
"s_lshr_b32 s54, s32, 2\n"
"s_and_b32 s55, s32, 3\n"
"s_waitcnt lgkmcnt(6)\n"
"v_mfma_f32_16x16x32_bf16 v[32:35], v[92:95], v[216:219], v[32:35]\n"
"ds_read_b64_tr_b16 v[208:209], v240 offset:17408\n"
"v_mov_b32 v224, 0x3f803f80\n"
"v_mov_b32 v225, 0x3f803f80\n"
"v_mfma_f32_16x16x32_bf16 v[36:39], v[96:99], v[216:219], v[36:39]\n"
"ds_read_b64_tr_b16 v[210:211], v240 offset:21760\n"
"v_mov_b32 v226, 0x3f803f80\n"
"v_mov_b32 v227, 0x3f803f80\n"
"v_mfma_f32_16x16x32_bf16 v[40:43], v[100:103], v[216:219], v[40:43]\n"
"ds_read_b128 v[88:91], v244 offset:36864\n"
"v_lshrrev_b32 v120, 1, v254\n"
"v_and_b32 v121, 15, v127\n"
"v_lshrrev_b32 v122, 4, v127\n"
"v_mfma_f32_16x16x32_bf16 v[44:47], v[104:107], v[216:219], v[44:47]\n"
"v_lshlrev_b32 v123, 5, v121\n"
"v_lshl_add_u32 v123, v122, 3, v123\n"
"v_mfma_f32_16x16x32_bf16 v[64:67], v[108:111], v[216:219], v[64:67]\n"
"s_lshl_b32 s35, s54, 11\n"
"s_mov_b32 s36, 0x22600\n"
"s_waitcnt lgkmcnt(5)\n"
"v_mfma_f32_16x16x32_bf16 v[48:51], v[220:223], v[92:95], v[48:51]\n"
"ds_read_b64_tr_b16 v[212:213], v240 offset:17728\n"
"s_mov_b32 s94, 0x892000\n"
"buffer_load_dwordx2 v[112:113], off, s[12:15], s94 sc1\n"
"s_mov_b32 s37, 0x27800\n"
"s_cmp_eq_u32 s55, 1\n"
"s_cselect_b32 s36, 0x21600, s36\n"
"s_cselect_b32 s37, 0x21400, s37\n"
"v_mfma_f32_16x16x32_bf16 v[52:55], v[220:223], v[96:99], v[52:55]\n"
"ds_read_b64_tr_b16 v[214:215], v240 offset:22080\n"
"s_cmp_eq_u32 s55, 2\n"
"s_cselect_b32 s36, 0x23800, s36\n"
"s_cselect_b32 s37, 0x23600, s37\n"
"s_cselect_b64 vcc, -1, 0\n"
"v_mfma_f32_16x16x32_bf16 v[56:59], v[220:223], v[100:103], v[56:59]\n"
"v_mfma_f32_16x16x32_bf16 v[60:63], v[220:223], v[104:107], v[60:63]\n"
"v_cndmask_b32 v124, v120, v123, vcc\n"
"s_add_u32 s36, s36, s35\n"
"s_waitcnt vmcnt(7)\n"
"s_barrier\n"
"s_waitcnt lgkmcnt(3)\n"
"v_mfma_f32_16x16x32_bf16 v[0:3], v[208:211], v[72:75], v[0:3]\n"
"ds_read_b64_tr_b16 v[216:217], v240 offset:18048\n"
"s_cmp_eq_u32 s55, 3\n"
"s_cselect_b32 s36, 0x27800, s36\n"
"v_mfma_f32_16x16x32_bf16 v[4:7], v[208:211], v[76:79], v[4:7]\n"
"ds_read_b64_tr_b16 v[218:219], v240 offset:22400\n"
"s_add_u32 m0, s50, 0x1e000\n"
"s_add_u32 s41, s45, 512\n"
"buffer_load_dwordx4 v247, s[4:7], s41 offen lds\n"
"v_add_u32 v156, s36, v124\n"
"v_mfma_f32_16x16x32_bf16 v[8:11], v[208:211], v[80:83], v[8:11]\n"
"ds_read_b128 v[92:95], v243 offset:36864\n"
"v_add_u32 v157, s37, v124\n"
"s_mul_i32 s36, s55, 3072\n"
"s_add_u32 s36, s36, 0x24800\n"
"v_mfma_f32_16x16x32_bf16 v[12:15], v[208:211], v[84:87], v[12:15]\n"
"ds_read_b128 v[96:99], v243 offset:38912\n"
"v_add_u32 v158, s36, v254\n"
"s_add_u32 s37, s36, 1024\n"
"s_waitcnt lgkmcnt(4)\n"
"v_mfma_f32_16x16x32_bf16 v[16:19], v[212:215], v[72:75], v[16:19]\n"
"v_add_u32 v241, s47, v241\n"
"ds_read_b64_tr_b16 v[220:221], v241 offset:17408\n"
"s_cmp_eq_u32 s54, 0\n"
"s_cselect_b32 s37, s37, 0x27800\n"
"v_mfma_f32_16x16x32_bf16 v[20:23], v[212:215], v[76:79], v[20:23]\n"
"ds_read_b64_tr_b16 v[222:223], v241 offset:21760\n"
"buffer_load_dwordx4 v248, s[4:7], s41 offen offset:1024 lds\n"
"v_add_u32 v159, s37, v254\n"
"s_lshl_b32 s37, s54, 10\n"
"v_mfma_f32_16x16x32_bf16 v[24:27], v[212:215], v[80:83], v[24:27]\n"
"ds_read_b128 v[100:103], v243 offset:40960\n"
"v_add_u32 v160, s37, v158\n"
"v_add_u32 v161, s37, v160\n"
"v_mfma_f32_16x16x32_bf16 v[28:31], v[212:215], v[84:87], v[28:31]\n"
"ds_read_b128 v[104:107], v243 offset:43008\n"
"s_add_u32 s36, s35, 0x21600\n"
"v_add_u32 v162, s36, v120\n"
"s_waitcnt lgkmcnt(6)\n"
"v_mfma_f32_16x16x32_bf16 v[32:35], v[72:75], v[216:219], v[32:35]\n"
"v_add_u32 v240, 0xffff3400, v240\n"
"ds_read_b64_tr_b16 v[208:209], v240 offset:0\n"
"s_add_u32 s36, s37, 0x21400\n"
"v_add_u32 v163, s36, v120\n"
"v_add_u32 v164, s37, v163\n"
"v_mfma_f32_16x16x32_bf16 v[36:39], v[76:79], v[216:219], v[36:39]\n"
"ds_read_b64_tr_b16 v[210:211], v240 offset:4352\n"
"s_add_u32 m0, s48, 0xcc00\n"
"s_add_u32 s43, s46, 0xf0000\n"
"buffer_load_dwordx4 v246, s[8:11], s43 offen lds\n"
"v_lshrrev_b32 v124, 2, v127\n"
"v_lshlrev_b32 v124, 5, v124\n"
"v_mfma_f32_16x16x32_bf16 v[40:43], v[80:83], v[216:219], v[40:43]\n"
"ds_read_b128 v[108:111], v245 offset:36864\n"
"s_lshl_b32 s38, s55, 3\n"
"s_add_u32 s36, s35, s38\n"
"v_mfma_f32_16x16x32_bf16 v[44:47], v[84:87], v[216:219], v[44:47]\n"
"s_add_u32 s36, s36, 0x23800\n"
"v_add_u32 v165, s36, v124\n"
"v_mfma_f32_16x16x32_bf16 v[64:67], v[88:91], v[216:219], v[64:67]\n"
"s_add_u32 s36, s37, s38\n"
"s_add_u32 s36, s36, 0x23600\n"
"s_waitcnt lgkmcnt(5)\n"
"v_mfma_f32_16x16x32_bf16 v[48:51], v[220:223], v[72:75], v[48:51]\n"
"ds_read_b64_tr_b16 v[212:213], v240 offset:320\n"
"v_add_u32 v166, s36, v124\n"
"v_add_u32 v167, s37, v166\n"
"v_mfma_f32_16x16x32_bf16 v[52:55], v[220:223], v[76:79], v[52:55]\n"
"ds_read_b64_tr_b16 v[214:215], v240 offset:4672\n"
"s_add_u32 s44, s46, 0xf19c0\n"
"buffer_load_dwordx4 v246, s[8:11], s44 offen offset:1088 lds\n"
"v_and_b32 v123, 1, v122\n"
"v_mul_u32_u24 v123, 0x11000, v123\n"
"v_lshrrev_b32 v124, 1, v122\n"
"v_mfma_f32_16x16x32_bf16 v[56:59], v[220:223], v[80:83], v[56:59]\n"
"v_lshl_add_u32 v124, v124, 4, v121\n"
"v_lshl_add_u32 v168, v124, 4, v123\n"
"v_mul_u32_u24 v123, 0x11000, v122\n"
"v_mfma_f32_16x16x32_bf16 v[60:63], v[220:223], v[84:87], v[60:63]\n"
"v_lshl_add_u32 v169, v121, 4, v123\n"
"s_lshl_b32 s35, s39, 3\n"
"s_waitcnt vmcnt(7)\n"
"s_waitcnt lgkmcnt(2)\n"
"s_barrier\n"
"v_mfma_f32_16x16x32_bf16 v[0:3], v[208:211], v[92:95], v[0:3]\n"
"ds_read_b64_tr_b16 v[216:217], v240 offset:640\n"
"s_lshl_b32 s36, s54, 2\n"
"v_mfma_f32_16x16x32_bf16 v[4:7], v[208:211], v[96:99], v[4:7]\n"
"ds_read_b64_tr_b16 v[218:219], v240 offset:4992\n"
"s_add_u32 s35, s35, s36\n"
"s_mul_i32 s35, s35, 69632\n"
"v_mfma_f32_16x16x32_bf16 v[8:11], v[208:211], v[100:103], v[8:11]\n"
"ds_read_b128 v[72:75], v242 offset:0\n"
"s_add_u32 m0, s48, 0x11000\n"
"s_add_u32 s43, s46, 0x10e000\n"
"buffer_load_dwordx4 v246, s[8:11], s43 offen lds\n"
"s_lshl_b32 s36, s40, 2\n"
"s_add_u32 s36, s36, s55\n"
"s_lshl_b32 s37, s36, 9\n"
"v_mfma_f32_16x16x32_bf16 v[12:15], v[208:211], v[104:107], v[12:15]\n"
"ds_read_b128 v[76:79], v242 offset:2048\n"
"s_add_u32 s51, s35, s37\n"
"s_add_u32 s51, s51, 0x8000\n"
"s_waitcnt lgkmcnt(4)\n"
"v_mfma_f32_16x16x32_bf16 v[16:19], v[212:215], v[92:95], v[16:19]\n"
"v_add_u32 v241, s99, v241\n"
"ds_read_b64_tr_b16 v[220:221], v241 offset:0\n"
"s_lshr_b32 s37, s36, 1\n"
"s_lshl_b32 s37, s37, 8\n"
"v_mfma_f32_16x16x32_bf16 v[20:23], v[212:215], v[96:99], v[20:23]\n"
"ds_read_b64_tr_b16 v[222:223], v241 offset:4352\n"
"s_and_b32 s36, s36, 1\n"
"s_lshl_b32 s36, s36, 3\n"
"v_mfma_f32_16x16x32_bf16 v[24:27], v[212:215], v[100:103], v[24:27]\n"
"ds_read_b128 v[80:83], v242 offset:4096\n"
"s_add_u32 s37, s37, s36\n"
"s_add_u32 s42, s35, s37\n"
"v_mfma_f32_16x16x32_bf16 v[28:31], v[212:215], v[104:107], v[28:31]\n"
"ds_read_b128 v[84:87], v242 offset:6144\n"
"s_add_u32 s42, s42, 0xc000\n"
"v_lshlrev_b32 v122, 2, v122\n"
"s_waitcnt lgkmcnt(6)\n"
"v_mfma_f32_16x16x32_bf16 v[32:35], v[92:95], v[216:219], v[32:35]\n"
"ds_read_b64_tr_b16 v[208:209], v240 offset:17408\n"
"v_sub_u32 v123, v121, v122\n"
"v_add_u32 v124, 9, v123\n"
"v_lshlrev_b32 v125, 1, v121\n"
"v_mfma_f32_16x16x32_bf16 v[36:39], v[96:99], v[216:219], v[36:39]\n"
"ds_read_b64_tr_b16 v[210:211], v240 offset:21760\n"
"v_sub_u32 v125, v125, v122\n"
"v_add_u32 v125, 2, v125\n"
"v_mfma_f32_16x16x32_bf16 v[40:43], v[100:103], v[216:219], v[40:43]\n"
"ds_read_b128 v[88:91], v244 offset:0\n"
"s_cmp_eq_u32 s54, 0\n"
"s_cselect_b64 vcc, -1, 0\n"
"s_cselect_b32 s35, 0xff800000, 0\n"
"v_mfma_f32_16x16x32_bf16 v[44:47], v[104:107], v[216:219], v[44:47]\n"
"s_add_u32 s44, s46, 0x10f9c0\n"
"buffer_load_dwordx4 v246, s[8:11], s44 offen offset:1088 lds\n"
"v_cndmask_b32 v125, v124, v125, vcc\n"
"v_mfma_f32_16x16x32_bf16 v[64:67], v[108:111], v[216:219], v[64:67]\n"
"s_or_b32 s38, s54, s39\n"
"s_cmp_eq_u32 s38, 0\n"
"s_cselect_b32 s38, 0xcf8547b1, 0\n"
"s_waitcnt lgkmcnt(5)\n"
"v_mfma_f32_16x16x32_bf16 v[48:51], v[220:223], v[92:95], v[48:51]\n"
"ds_read_b64_tr_b16 v[212:213], v240 offset:17728\n"
"s_xor_b32 s37, s54, 1\n"
"v_mov_b32 v120, 0xff800000\n"
"v_mfma_f32_16x16x32_bf16 v[52:55], v[220:223], v[96:99], v[52:55]\n"
"ds_read_b64_tr_b16 v[214:215], v240 offset:22080\n"
"v_mov_b32 v121, s38\n"
"v_subrev_u32 v126, 0, v123\n"
"v_mfma_f32_16x16x32_bf16 v[56:59], v[220:223], v[100:103], v[56:59]\n"
"v_cmp_le_u32 vcc, 8, v126\n"
"s_nop 1\n"
"v_cndmask_b32 v228, 0, v120, vcc\n"
"v_cmp_lt_i32 vcc, 0, v124\n"
"v_mfma_f32_16x16x32_bf16 v[60:63], v[220:223], v[104:107], v[60:63]\n"
"s_nop 0\n"
"v_cndmask_b32 v232, 0, v120, vcc\n"
"v_cmp_lt_i32 vcc, 0, v125\n"
"s_waitcnt vmcnt(7)\n"
"s_barrier\n"
"s_waitcnt vmcnt(6)\n"
"v_readfirstlane_b32 s58, v112\n"
"v_readfirstlane_b32 s59, v113\n"
"s_nop 0\n"
"s_mul_i32 s56, s58, 0x7F4A7C15\n"
"s_mul_hi_u32 s57, s58, 0x7F4A7C15\n"
"s_mul_i32 s58, s58, 0x9E3779B9\n"
"s_add_u32 s57, s57, s58\n"
"s_mul_i32 s59, s59, 0x7F4A7C15\n"
"s_add_u32 s57, s57, s59\n"
"s_add_u32 s56, s56, 0xD9B4E019\n"
"s_addc_u32 s57, s57, 0x632BE59B\n"
"s_waitcnt lgkmcnt(3)\n"
"v_mfma_f32_16x16x32_bf16 v[0:3], v[208:211], v[72:75], v[0:3]\n"
"ds_read_b64_tr_b16 v[216:217], v240 offset:18048\n"
"v_cndmask_b32 v236, v121, v120, vcc\n"
"v_mfma_f32_16x16x32_bf16 v[4:7], v[208:211], v[76:79], v[4:7]\n"
"ds_read_b64_tr_b16 v[218:219], v240 offset:22400\n"
"s_add_u32 m0, s50, 0x22800\n"
"s_add_u32 s41, s45, 640\n"
"buffer_load_dwordx4 v247, s[4:7], s41 offen lds\n"
"v_add_f32 v152, s35, v236\n"
"v_subrev_u32 v126, 1, v123\n"
"v_mfma_f32_16x16x32_bf16 v[8:11], v[208:211], v[80:83], v[8:11]\n"
"ds_read_b128 v[92:95], v243 offset:0\n"
"v_cmp_le_u32 vcc, 8, v126\n"
"s_nop 1\n"
"v_cndmask_b32 v229, 0, v120, vcc\n"
"v_cmp_lt_i32 vcc, 1, v124\n"
"v_mfma_f32_16x16x32_bf16 v[12:15], v[208:211], v[84:87], v[12:15]\n"
"ds_read_b128 v[96:99], v243 offset:2048\n"
"v_cndmask_b32 v233, 0, v120, vcc\n"
"v_add_u32 v126, s37, v125\n"
"s_waitcnt lgkmcnt(4)\n"
"v_mfma_f32_16x16x32_bf16 v[16:19], v[212:215], v[72:75], v[16:19]\n"
"v_add_u32 v241, s47, v241\n"
"ds_read_b64_tr_b16 v[220:221], v241 offset:17408\n"
"v_cmp_lt_i32 vcc, 1, v126\n"
"s_nop 1\n"
"v_cndmask_b32 v153, v121, v120, vcc\n"
"v_mfma_f32_16x16x32_bf16 v[20:23], v[212:215], v[76:79], v[20:23]\n"
"ds_read_b64_tr_b16 v[222:223], v241 offset:21760\n"
"buffer_load_dwordx4 v248, s[4:7], s41 offen offset:1024 lds\n"
"v_add_f32 v237, s35, v153\n"
"v_subrev_u32 v126, 2, v123\n"
"v_mfma_f32_16x16x32_bf16 v[24:27], v[212:215], v[80:83], v[24:27]\n"
"ds_read_b128 v[100:103], v243 offset:4096\n"
"v_cmp_le_u32 vcc, 8, v126\n"
"s_nop 1\n"
"v_cndmask_b32 v230, 0, v120, vcc\n"
"v_cmp_lt_i32 vcc, 2, v124\n"
"v_mfma_f32_16x16x32_bf16 v[28:31], v[212:215], v[84:87], v[28:31]\n"
"ds_read_b128 v[104:107], v243 offset:6144\n"
"v_cndmask_b32 v234, 0, v120, vcc\n"
"v_cmp_lt_i32 vcc, 2, v125\n"
"s_waitcnt lgkmcnt(6)\n"
"v_mfma_f32_16x16x32_bf16 v[32:35], v[72:75], v[216:219], v[32:35]\n"
"ds_read_b64_tr_b16 v[208:209], v240 offset:34816\n"
"v_cndmask_b32 v238, v121, v120, vcc\n"
"v_add_f32 v154, s35, v238\n"
"v_mfma_f32_16x16x32_bf16 v[36:39], v[76:79], v[216:219], v[36:39]\n"
"ds_read_b64_tr_b16 v[210:211], v240 offset:39168\n"
"s_add_u32 m0, s48, 0x0\n"
"s_add_u32 s43, s46, 0x12c000\n"
"buffer_load_dwordx4 v246, s[8:11], s43 offen lds\n"
"v_subrev_u32 v126, 3, v123\n"
"v_cmp_le_u32 vcc, 8, v126\n"
"v_mfma_f32_16x16x32_bf16 v[40:43], v[80:83], v[216:219], v[40:43]\n"
"ds_read_b128 v[108:111], v245 offset:0\n"
"v_cndmask_b32 v231, 0, v120, vcc\n"
"v_cmp_lt_i32 vcc, 3, v124\n"
"s_nop 1\n"
"v_cndmask_b32 v235, 0, v120, vcc\n"
"v_mfma_f32_16x16x32_bf16 v[44:47], v[84:87], v[216:219], v[44:47]\n"
"v_add_u32 v126, s37, v125\n"
"v_cmp_lt_i32 vcc, 3, v126\n"
"v_mfma_f32_16x16x32_bf16 v[64:67], v[88:91], v[216:219], v[64:67]\n"
"s_nop 0\n"
"v_cndmask_b32 v155, v121, v120, vcc\n"
"v_add_f32 v239, s35, v155\n"
"s_waitcnt lgkmcnt(5)\n"
"v_mfma_f32_16x16x32_bf16 v[48:51], v[220:223], v[72:75], v[48:51]\n"
"ds_read_b64_tr_b16 v[212:213], v240 offset:35136\n"
"s_lshl_b32 s37, s39, 1\n"
"s_add_u32 s37, s37, s54\n"
"v_mfma_f32_16x16x32_bf16 v[52:55], v[220:223], v[76:79], v[52:55]\n"
"ds_read_b64_tr_b16 v[214:215], v240 offset:39488\n"
"s_add_u32 s44, s46, 0x12d9c0\n"
"buffer_load_dwordx4 v246, s[8:11], s44 offen offset:1088 lds\n"
"s_lshl_b32 s37, s37, 8\n"
"s_add_u32 s49, s37, 0x880000\n"
"s_xor_b32 s38, s55, 3\n"
"v_mfma_f32_16x16x32_bf16 v[56:59], v[220:223], v[80:83], v[56:59]\n"
"s_or_b32 s38, s38, s40\n"
"s_cmp_eq_u32 s38, 0\n"
"s_cselect_b32 s49, s49, 0\n"
"v_mfma_f32_16x16x32_bf16 v[60:63], v[220:223], v[84:87], v[60:63]\n"
"v_lshrrev_b32 v170, 2, v254\n"
"s_mov_b32 s53, 0x3ea52b5c\n"
"s_waitcnt vmcnt(6)\n"
"s_waitcnt lgkmcnt(2)\n"
"s_barrier\n"
"v_mfma_f32_16x16x32_bf16 v[0:3], v[208:211], v[92:95], v[0:3]\n"
"ds_read_b64_tr_b16 v[216:217], v240 offset:35456\n"
"v_mfma_f32_16x16x32_bf16 v[4:7], v[208:211], v[96:99], v[4:7]\n"
"ds_read_b64_tr_b16 v[218:219], v240 offset:39808\n"
"v_mfma_f32_16x16x32_bf16 v[8:11], v[208:211], v[100:103], v[8:11]\n"
"ds_read_b128 v[72:75], v242 offset:18432\n"
"s_add_u32 m0, s48, 0x4400\n"
"s_add_u32 s43, s46, 0x14a000\n"
"buffer_load_dwordx4 v246, s[8:11], s43 offen lds\n"
"v_mfma_f32_16x16x32_bf16 v[12:15], v[208:211], v[104:107], v[12:15]\n"
"ds_read_b128 v[76:79], v242 offset:20480\n"
"s_waitcnt lgkmcnt(4)\n"
"v_mfma_f32_16x16x32_bf16 v[16:19], v[212:215], v[92:95], v[16:19]\n"
"v_add_u32 v241, s47, v241\n"
"ds_read_b64_tr_b16 v[220:221], v241 offset:34816\n"
"v_mfma_f32_16x16x32_bf16 v[20:23], v[212:215], v[96:99], v[20:23]\n"
"ds_read_b64_tr_b16 v[222:223], v241 offset:39168\n"
"v_mfma_f32_16x16x32_bf16 v[24:27], v[212:215], v[100:103], v[24:27]\n"
"ds_read_b128 v[80:83], v242 offset:22528\n"
"v_mfma_f32_16x16x32_bf16 v[28:31], v[212:215], v[104:107], v[28:31]\n"
"ds_read_b128 v[84:87], v242 offset:24576\n"
"s_waitcnt lgkmcnt(6)\n"
"v_mfma_f32_16x16x32_bf16 v[32:35], v[92:95], v[216:219], v[32:35]\n"
"v_add_u32 v240, 0xcc00, v240\n"
"ds_read_b64_tr_b16 v[208:209], v240 offset:0\n"
"v_mfma_f32_16x16x32_bf16 v[36:39], v[96:99], v[216:219], v[36:39]\n"
"ds_read_b64_tr_b16 v[210:211], v240 offset:4352\n"
"v_mfma_f32_16x16x32_bf16 v[40:43], v[100:103], v[216:219], v[40:43]\n"
"ds_read_b128 v[88:91], v244 offset:18432\n"
"v_mfma_f32_16x16x32_bf16 v[44:47], v[104:107], v[216:219], v[44:47]\n"
"s_add_u32 s44, s46, 0x14b9c0\n"
"buffer_load_dwordx4 v246, s[8:11], s44 offen offset:1088 lds\n"
"v_mfma_f32_16x16x32_bf16 v[64:67], v[108:111], v[216:219], v[64:67]\n"
"s_waitcnt lgkmcnt(5)\n"
"v_mfma_f32_16x16x32_bf16 v[48:51], v[220:223], v[92:95], v[48:51]\n"
"ds_read_b64_tr_b16 v[212:213], v240 offset:320\n"
"v_mfma_f32_16x16x32_bf16 v[52:55], v[220:223], v[96:99], v[52:55]\n"
"ds_read_b64_tr_b16 v[214:215], v240 offset:4672\n"
"v_mfma_f32_16x16x32_bf16 v[56:59], v[220:223], v[100:103], v[56:59]\n"
"v_mfma_f32_16x16x32_bf16 v[60:63], v[220:223], v[104:107], v[60:63]\n"
"s_waitcnt vmcnt(6)\n"
"s_barrier\n"
"s_waitcnt lgkmcnt(3)\n"
"v_mfma_f32_16x16x32_bf16 v[0:3], v[208:211], v[72:75], v[0:3]\n"
"ds_read_b64_tr_b16 v[216:217], v240 offset:640\n"
"v_mfma_f32_16x16x32_bf16 v[4:7], v[208:211], v[76:79], v[4:7]\n"
"ds_read_b64_tr_b16 v[218:219], v240 offset:4992\n"
"s_add_u32 m0, s50, 0x19800\n"
"s_add_u32 s41, s45, 768\n"
"buffer_load_dwordx4 v247, s[4:7], s41 offen lds\n"
"v_mfma_f32_16x16x32_bf16 v[8:11], v[208:211], v[80:83], v[8:11]\n"
"ds_read_b128 v[92:95], v243 offset:18432\n"
"v_mfma_f32_16x16x32_bf16 v[12:15], v[208:211], v[84:87], v[12:15]\n"
"ds_read_b128 v[96:99], v243 offset:20480\n"
"s_waitcnt lgkmcnt(4)\n"
"v_mfma_f32_16x16x32_bf16 v[16:19], v[212:215], v[72:75], v[16:19]\n"
"v_add_u32 v241, s95, v241\n"
"ds_read_b64_tr_b16 v[220:221], v241 offset:0\n"
"v_mfma_f32_16x16x32_bf16 v[20:23], v[212:215], v[76:79], v[20:23]\n"
"ds_read_b64_tr_b16 v[222:223], v241 offset:4352\n"
"buffer_load_dwordx4 v248, s[4:7], s41 offen offset:1024 lds\n"
"v_mfma_f32_16x16x32_bf16 v[24:27], v[212:215], v[80:83], v[24:27]\n"
"ds_read_b128 v[100:103], v243 offset:22528\n"
"v_mfma_f32_16x16x32_bf16 v[28:31], v[212:215], v[84:87], v[28:31]\n"
"ds_read_b128 v[104:107], v243 offset:24576\n"
"s_waitcnt lgkmcnt(6)\n"
"v_mfma_f32_16x16x32_bf16 v[32:35], v[72:75], v[216:219], v[32:35]\n"
"ds_read_b64_tr_b16 v[208:209], v240 offset:17408\n"
"v_mfma_f32_16x16x32_bf16 v[36:39], v[76:79], v[216:219], v[36:39]\n"
"ds_read_b64_tr_b16 v[210:211], v240 offset:21760\n"
"s_add_u32 m0, s48, 0x8800\n"
"s_add_u32 s43, s46, 0x168000\n"
"buffer_load_dwordx4 v246, s[8:11], s43 offen lds\n"
"v_mfma_f32_16x16x32_bf16 v[40:43], v[80:83], v[216:219], v[40:43]\n"
"ds_read_b128 v[108:111], v245 offset:18432\n"
"v_mfma_f32_16x16x32_bf16 v[44:47], v[84:87], v[216:219], v[44:47]\n"
"v_mfma_f32_16x16x32_bf16 v[64:67], v[88:91], v[216:219], v[64:67]\n"
"s_waitcnt lgkmcnt(5)\n"
"v_mfma_f32_16x16x32_bf16 v[48:51], v[220:223], v[72:75], v[48:51]\n"
"ds_read_b64_tr_b16 v[212:213], v240 offset:17728\n"
"v_mfma_f32_16x16x32_bf16 v[52:55], v[220:223], v[76:79], v[52:55]\n"
"ds_read_b64_tr_b16 v[214:215], v240 offset:22080\n"
"s_add_u32 s44, s46, 0x1699c0\n"
"buffer_load_dwordx4 v246, s[8:11], s44 offen offset:1088 lds\n"
"v_mfma_f32_16x16x32_bf16 v[56:59], v[220:223], v[80:83], v[56:59]\n"
"v_mfma_f32_16x16x32_bf16 v[60:63], v[220:223], v[84:87], v[60:63]\n"
"s_waitcnt vmcnt(6)\n"
"s_waitcnt lgkmcnt(2)\n"
"s_barrier\n"
"v_mfma_f32_16x16x32_bf16 v[0:3], v[208:211], v[92:95], v[0:3]\n"
"ds_read_b64_tr_b16 v[216:217], v240 offset:18048\n"
"v_mfma_f32_16x16x32_bf16 v[4:7], v[208:211], v[96:99], v[4:7]\n"
"ds_read_b64_tr_b16 v[218:219], v240 offset:22400\n"
"v_mfma_f32_16x16x32_bf16 v[8:11], v[208:211], v[100:103], v[8:11]\n"
"ds_read_b128 v[72:75], v242 offset:36864\n"
"s_add_u32 m0, s48, 0xcc00\n"
"s_add_u32 s43, s46, 0x186000\n"
"buffer_load_dwordx4 v246, s[8:11], s43 offen lds\n"
"v_mfma_f32_16x16x32_bf16 v[12:15], v[208:211], v[104:107], v[12:15]\n"
"ds_read_b128 v[76:79], v242 offset:38912\n"
"s_waitcnt lgkmcnt(4)\n"
"v_mfma_f32_16x16x32_bf16 v[16:19], v[212:215], v[92:95], v[16:19]\n"
"v_add_u32 v241, s47, v241\n"
"ds_read_b64_tr_b16 v[220:221], v241 offset:17408\n"
"v_mfma_f32_16x16x32_bf16 v[20:23], v[212:215], v[96:99], v[20:23]\n"
"ds_read_b64_tr_b16 v[222:223], v241 offset:21760\n"
"v_mfma_f32_16x16x32_bf16 v[24:27], v[212:215], v[100:103], v[24:27]\n"
"ds_read_b128 v[80:83], v242 offset:40960\n"
"v_mfma_f32_16x16x32_bf16 v[28:31], v[212:215], v[104:107], v[28:31]\n"
"ds_read_b128 v[84:87], v242 offset:43008\n"
"s_waitcnt lgkmcnt(6)\n"
"v_mfma_f32_16x16x32_bf16 v[32:35], v[92:95], v[216:219], v[32:35]\n"
"v_add_u32 v240, 0xffff3400, v240\n"
"ds_read_b64_tr_b16 v[208:209], v240 offset:0\n"
"v_mfma_f32_16x16x32_bf16 v[36:39], v[96:99], v[216:219], v[36:39]\n"
"ds_read_b64_tr_b16 v[210:211], v240 offset:4352\n"
"v_mfma_f32_16x16x32_bf16 v[40:43], v[100:103], v[216:219], v[40:43]\n"
"ds_read_b128 v[88:91], v244 offset:36864\n"
"v_mfma_f32_16x16x32_bf16 v[44:47], v[104:107], v[216:219], v[44:47]\n"
"s_add_u32 s44, s46, 0x1879c0\n"
"buffer_load_dwordx4 v246, s[8:11], s44 offen offset:1088 lds\n"
"v_mfma_f32_16x16x32_bf16 v[64:67], v[108:111], v[216:219], v[64:67]\n"
"s_waitcnt lgkmcnt(5)\n"
"v_mfma_f32_16x16x32_bf16 v[48:51], v[220:223], v[92:95], v[48:51]\n"
"ds_read_b64_tr_b16 v[212:213], v240 offset:320\n"
"v_mfma_f32_16x16x32_bf16 v[52:55], v[220:223], v[96:99], v[52:55]\n"
"ds_read_b64_tr_b16 v[214:215], v240 offset:4672\n"
"v_mfma_f32_16x16x32_bf16 v[56:59], v[220:223], v[100:103], v[56:59]\n"
"v_mfma_f32_16x16x32_bf16 v[60:63], v[220:223], v[104:107], v[60:63]\n"
"s_waitcnt vmcnt(6)\n"
"s_barrier\n"
"s_waitcnt lgkmcnt(3)\n"
"v_mfma_f32_16x16x32_bf16 v[0:3], v[208:211], v[72:75], v[0:3]\n"
"ds_read_b64_tr_b16 v[216:217], v240 offset:640\n"
"v_mfma_f32_16x16x32_bf16 v[4:7], v[208:211], v[76:79], v[4:7]\n"
"ds_read_b64_tr_b16 v[218:219], v240 offset:4992\n"
"s_add_u32 m0, s50, 0x1e000\n"
"s_add_u32 s41, s45, 896\n"
"buffer_load_dwordx4 v247, s[4:7], s41 offen lds\n"
"v_mfma_f32_16x16x32_bf16 v[8:11], v[208:211], v[80:83], v[8:11]\n"
"ds_read_b128 v[92:95], v243 offset:36864\n"
"v_mfma_f32_16x16x32_bf16 v[12:15], v[208:211], v[84:87], v[12:15]\n"
"ds_read_b128 v[96:99], v243 offset:38912\n"
"s_waitcnt lgkmcnt(4)\n"
"v_mfma_f32_16x16x32_bf16 v[16:19], v[212:215], v[72:75], v[16:19]\n"
"v_add_u32 v241, s99, v241\n"
"ds_read_b64_tr_b16 v[220:221], v241 offset:0\n"
"v_mfma_f32_16x16x32_bf16 v[20:23], v[212:215], v[76:79], v[20:23]\n"
"ds_read_b64_tr_b16 v[222:223], v241 offset:4352\n"
"buffer_load_dwordx4 v248, s[4:7], s41 offen offset:1024 lds\n"
"v_mfma_f32_16x16x32_bf16 v[24:27], v[212:215], v[80:83], v[24:27]\n"
"ds_read_b128 v[100:103], v243 offset:40960\n"
"v_mfma_f32_16x16x32_bf16 v[28:31], v[212:215], v[84:87], v[28:31]\n"
"ds_read_b128 v[104:107], v243 offset:43008\n"
"s_waitcnt lgkmcnt(6)\n"
"v_mfma_f32_16x16x32_bf16 v[32:35], v[72:75], v[216:219], v[32:35]\n"
"ds_read_b64_tr_b16 v[208:209], v240 offset:17408\n"
"v_mfma_f32_16x16x32_bf16 v[36:39], v[76:79], v[216:219], v[36:39]\n"
"ds_read_b64_tr_b16 v[210:211], v240 offset:21760\n"
"s_add_u32 m0, s48, 0x11000\n"
"s_add_u32 s43, s46, 0x1a4000\n"
"buffer_load_dwordx4 v246, s[8:11], s43 offen lds\n"
"v_mfma_f32_16x16x32_bf16 v[40:43], v[80:83], v[216:219], v[40:43]\n"
"ds_read_b128 v[108:111], v245 offset:36864\n"
"v_mfma_f32_16x16x32_bf16 v[44:47], v[84:87], v[216:219], v[44:47]\n"
"v_mfma_f32_16x16x32_bf16 v[64:67], v[88:91], v[216:219], v[64:67]\n"
"s_waitcnt lgkmcnt(5)\n"
"v_mfma_f32_16x16x32_bf16 v[48:51], v[220:223], v[72:75], v[48:51]\n"
"ds_read_b64_tr_b16 v[212:213], v240 offset:17728\n"
"v_mfma_f32_16x16x32_bf16 v[52:55], v[220:223], v[76:79], v[52:55]\n"
"ds_read_b64_tr_b16 v[214:215], v240 offset:22080\n"
"s_add_u32 s44, s46, 0x1a59c0\n"
"buffer_load_dwordx4 v246, s[8:11], s44 offen offset:1088 lds\n"
"v_mfma_f32_16x16x32_bf16 v[56:59], v[220:223], v[80:83], v[56:59]\n"
"v_mfma_f32_16x16x32_bf16 v[60:63], v[220:223], v[84:87], v[60:63]\n"
"s_waitcnt vmcnt(6)\n"
"s_waitcnt lgkmcnt(2)\n"
"s_barrier\n"
"v_mfma_f32_16x16x32_bf16 v[0:3], v[208:211], v[92:95], v[0:3]\n"
"ds_read_b64_tr_b16 v[216:217], v240 offset:18048\n"
"v_mfma_f32_16x16x32_bf16 v[4:7], v[208:211], v[96:99], v[4:7]\n"
"ds_read_b64_tr_b16 v[218:219], v240 offset:22400\n"
"v_mfma_f32_16x16x32_bf16 v[8:11], v[208:211], v[100:103], v[8:11]\n"
"ds_read_b128 v[72:75], v242 offset:0\n"
"s_add_u32 m0, s48, 0x0\n"
"s_add_u32 s43, s46, 0x1c2000\n"
"buffer_load_dwordx4 v246, s[8:11], s43 offen lds\n"
"v_mfma_f32_16x16x32_bf16 v[12:15], v[208:211], v[104:107], v[12:15]\n"
"ds_read_b128 v[76:79], v242 offset:2048\n"
"s_waitcnt lgkmcnt(4)\n"
"v_mfma_f32_16x16x32_bf16 v[16:19], v[212:215], v[92:95], v[16:19]\n"
"v_add_u32 v241, s47, v241\n"
"ds_read_b64_tr_b16 v[220:221], v241 offset:17408\n"
"v_mfma_f32_16x16x32_bf16 v[20:23], v[212:215], v[96:99], v[20:23]\n"
"ds_read_b64_tr_b16 v[222:223], v241 offset:21760\n"
"v_mfma_f32_16x16x32_bf16 v[24:27], v[212:215], v[100:103], v[24:27]\n"
"ds_read_b128 v[80:83], v242 offset:4096\n"
"v_mfma_f32_16x16x32_bf16 v[28:31], v[212:215], v[104:107], v[28:31]\n"
"ds_read_b128 v[84:87], v242 offset:6144\n"
"s_waitcnt lgkmcnt(6)\n"
"v_mfma_f32_16x16x32_bf16 v[32:35], v[92:95], v[216:219], v[32:35]\n"
"ds_read_b64_tr_b16 v[208:209], v240 offset:34816\n"
"v_mfma_f32_16x16x32_bf16 v[36:39], v[96:99], v[216:219], v[36:39]\n"
"ds_read_b64_tr_b16 v[210:211], v240 offset:39168\n"
"v_mfma_f32_16x16x32_bf16 v[40:43], v[100:103], v[216:219], v[40:43]\n"
"ds_read_b128 v[88:91], v244 offset:0\n"
"v_mfma_f32_16x16x32_bf16 v[44:47], v[104:107], v[216:219], v[44:47]\n"
"s_add_u32 s44, s46, 0x1c39c0\n"
"buffer_load_dwordx4 v246, s[8:11], s44 offen offset:1088 lds\n"
"v_mfma_f32_16x16x32_bf16 v[64:67], v[108:111], v[216:219], v[64:67]\n"
"s_waitcnt lgkmcnt(5)\n"
"v_mfma_f32_16x16x32_bf16 v[48:51], v[220:223], v[92:95], v[48:51]\n"
"ds_read_b64_tr_b16 v[212:213], v240 offset:35136\n"
"v_mfma_f32_16x16x32_bf16 v[52:55], v[220:223], v[96:99], v[52:55]\n"
"ds_read_b64_tr_b16 v[214:215], v240 offset:39488\n"
"v_mfma_f32_16x16x32_bf16 v[56:59], v[220:223], v[100:103], v[56:59]\n"
"v_mfma_f32_16x16x32_bf16 v[60:63], v[220:223], v[104:107], v[60:63]\n"
"s_waitcnt vmcnt(6)\n"
"s_barrier\n"
"s_waitcnt lgkmcnt(3)\n"
"v_mfma_f32_16x16x32_bf16 v[0:3], v[208:211], v[72:75], v[0:3]\n"
"ds_read_b64_tr_b16 v[216:217], v240 offset:35456\n"
"v_mfma_f32_16x16x32_bf16 v[4:7], v[208:211], v[76:79], v[4:7]\n"
"ds_read_b64_tr_b16 v[218:219], v240 offset:39808\n"
"s_add_u32 m0, s50, 0x22800\n"
"s_add_u32 s41, s45, 1024\n"
"buffer_load_dwordx4 v247, s[4:7], s41 offen lds\n"
"v_mfma_f32_16x16x32_bf16 v[8:11], v[208:211], v[80:83], v[8:11]\n"
"ds_read_b128 v[92:95], v243 offset:0\n"
"v_mfma_f32_16x16x32_bf16 v[12:15], v[208:211], v[84:87], v[12:15]\n"
"ds_read_b128 v[96:99], v243 offset:2048\n"
"s_waitcnt lgkmcnt(4)\n"
"v_mfma_f32_16x16x32_bf16 v[16:19], v[212:215], v[72:75], v[16:19]\n"
"v_add_u32 v241, s47, v241\n"
"ds_read_b64_tr_b16 v[220:221], v241 offset:34816\n"
"v_mfma_f32_16x16x32_bf16 v[20:23], v[212:215], v[76:79], v[20:23]\n"
"ds_read_b64_tr_b16 v[222:223], v241 offset:39168\n"
"buffer_load_dwordx4 v248, s[4:7], s41 offen offset:1024 lds\n"
"v_mfma_f32_16x16x32_bf16 v[24:27], v[212:215], v[80:83], v[24:27]\n"
"ds_read_b128 v[100:103], v243 offset:4096\n"
"v_mfma_f32_16x16x32_bf16 v[28:31], v[212:215], v[84:87], v[28:31]\n"
"ds_read_b128 v[104:107], v243 offset:6144\n"
"s_waitcnt lgkmcnt(6)\n"
"v_mfma_f32_16x16x32_bf16 v[32:35], v[72:75], v[216:219], v[32:35]\n"
"v_add_u32 v240, 0xcc00, v240\n"
"ds_read_b64_tr_b16 v[208:209], v240 offset:0\n"
"v_mfma_f32_16x16x32_bf16 v[36:39], v[76:79], v[216:219], v[36:39]\n"
"ds_read_b64_tr_b16 v[210:211], v240 offset:4352\n"
"s_add_u32 m0, s48, 0x4400\n"
"s_add_u32 s43, s46, 0x1e0000\n"
"buffer_load_dwordx4 v246, s[8:11], s43 offen lds\n"
"v_mfma_f32_16x16x32_bf16 v[40:43], v[80:83], v[216:219], v[40:43]\n"
"ds_read_b128 v[108:111], v245 offset:0\n"
"v_mfma_f32_16x16x32_bf16 v[44:47], v[84:87], v[216:219], v[44:47]\n"
"v_mfma_f32_16x16x32_bf16 v[64:67], v[88:91], v[216:219], v[64:67]\n"
"s_waitcnt lgkmcnt(5)\n"
"v_mfma_f32_16x16x32_bf16 v[48:51], v[220:223], v[72:75], v[48:51]\n"
"ds_read_b64_tr_b16 v[212:213], v240 offset:320\n"
"v_mfma_f32_16x16x32_bf16 v[52:55], v[220:223], v[76:79], v[52:55]\n"
"ds_read_b64_tr_b16 v[214:215], v240 offset:4672\n"
"s_add_u32 s44, s46, 0x1e19c0\n"
"buffer_load_dwordx4 v246, s[8:11], s44 offen offset:1088 lds\n"
"v_mfma_f32_16x16x32_bf16 v[56:59], v[220:223], v[80:83], v[56:59]\n"
"v_mfma_f32_16x16x32_bf16 v[60:63], v[220:223], v[84:87], v[60:63]\n"
"s_waitcnt vmcnt(6)\n"
"s_waitcnt lgkmcnt(2)\n"
"s_barrier\n"
"v_mfma_f32_16x16x32_bf16 v[0:3], v[208:211], v[92:95], v[0:3]\n"
"ds_read_b64_tr_b16 v[216:217], v240 offset:640\n"
"v_mfma_f32_16x16x32_bf16 v[4:7], v[208:211], v[96:99], v[4:7]\n"
"ds_read_b64_tr_b16 v[218:219], v240 offset:4992\n"
"v_mfma_f32_16x16x32_bf16 v[8:11], v[208:211], v[100:103], v[8:11]\n"
"ds_read_b128 v[72:75], v242 offset:18432\n"
"s_add_u32 m0, s48, 0x8800\n"
"s_add_u32 s43, s46, 0x1fe000\n"
"buffer_load_dwordx4 v246, s[8:11], s43 offen lds\n"
"v_mfma_f32_16x16x32_bf16 v[12:15], v[208:211], v[104:107], v[12:15]\n"
"ds_read_b128 v[76:79], v242 offset:20480\n"
"s_waitcnt lgkmcnt(4)\n"
"v_mfma_f32_16x16x32_bf16 v[16:19], v[212:215], v[92:95], v[16:19]\n"
"v_add_u32 v241, s95, v241\n"
"ds_read_b64_tr_b16 v[220:221], v241 offset:0\n"
"v_mfma_f32_16x16x32_bf16 v[20:23], v[212:215], v[96:99], v[20:23]\n"
"ds_read_b64_tr_b16 v[222:223], v241 offset:4352\n"
"v_mfma_f32_16x16x32_bf16 v[24:27], v[212:215], v[100:103], v[24:27]\n"
"ds_read_b128 v[80:83], v242 offset:22528\n"
"v_mfma_f32_16x16x32_bf16 v[28:31], v[212:215], v[104:107], v[28:31]\n"
"ds_read_b128 v[84:87], v242 offset:24576\n"
"s_waitcnt lgkmcnt(6)\n"
"v_mfma_f32_16x16x32_bf16 v[32:35], v[92:95], v[216:219], v[32:35]\n"
"ds_read_b64_tr_b16 v[208:209], v240 offset:17408\n"
"v_mfma_f32_16x16x32_bf16 v[36:39], v[96:99], v[216:219], v[36:39]\n"
"ds_read_b64_tr_b16 v[210:211], v240 offset:21760\n"
"v_mfma_f32_16x16x32_bf16 v[40:43], v[100:103], v[216:219], v[40:43]\n"
"ds_read_b128 v[88:91], v244 offset:18432\n"
"v_mfma_f32_16x16x32_bf16 v[44:47], v[104:107], v[216:219], v[44:47]\n"
"s_add_u32 s44, s46, 0x1ff9c0\n"
"buffer_load_dwordx4 v246, s[8:11], s44 offen offset:1088 lds\n"
"v_mfma_f32_16x16x32_bf16 v[64:67], v[108:111], v[216:219], v[64:67]\n"
"s_waitcnt lgkmcnt(5)\n"
"v_mfma_f32_16x16x32_bf16 v[48:51], v[220:223], v[92:95], v[48:51]\n"
"ds_read_b64_tr_b16 v[212:213], v240 offset:17728\n"
"v_mfma_f32_16x16x32_bf16 v[52:55], v[220:223], v[96:99], v[52:55]\n"
"ds_read_b64_tr_b16 v[214:215], v240 offset:22080\n"
"v_mfma_f32_16x16x32_bf16 v[56:59], v[220:223], v[100:103], v[56:59]\n"
"v_mfma_f32_16x16x32_bf16 v[60:63], v[220:223], v[104:107], v[60:63]\n"
"s_waitcnt vmcnt(6)\n"
"s_barrier\n"
"s_waitcnt lgkmcnt(3)\n"
"v_mfma_f32_16x16x32_bf16 v[0:3], v[208:211], v[72:75], v[0:3]\n"
"ds_read_b64_tr_b16 v[216:217], v240 offset:18048\n"
"s_lshr_b32 s35, s34, 1\n"
"v_mfma_f32_16x16x32_bf16 v[4:7], v[208:211], v[76:79], v[4:7]\n"
"ds_read_b64_tr_b16 v[218:219], v240 offset:22400\n"
"s_add_u32 m0, s50, 0x19800\n"
"s_add_u32 s41, s45, 1152\n"
"buffer_load_dwordx4 v247, s[4:7], s41 offen lds\n"
"v_mfma_f32_16x16x32_bf16 v[8:11], v[208:211], v[80:83], v[8:11]\n"
"ds_read_b128 v[92:95], v243 offset:18432\n"
"s_and_b32 s36, s34, 1\n"
"v_mfma_f32_16x16x32_bf16 v[12:15], v[208:211], v[84:87], v[12:15]\n"
"ds_read_b128 v[96:99], v243 offset:20480\n"
"s_add_u32 s35, s35, s36\n"
"s_waitcnt lgkmcnt(4)\n"
"v_mfma_f32_16x16x32_bf16 v[16:19], v[212:215], v[72:75], v[16:19]\n"
"v_add_u32 v241, s47, v241\n"
"ds_read_b64_tr_b16 v[220:221], v241 offset:17408\n"
"s_cmp_lt_u32 s34, 16\n"
"s_cselect_b32 s35, s34, s35\n"
"v_mfma_f32_16x16x32_bf16 v[20:23], v[212:215], v[76:79], v[20:23]\n"
"ds_read_b64_tr_b16 v[222:223], v241 offset:21760\n"
"buffer_load_dwordx4 v248, s[4:7], s41 offen offset:1024 lds\n"
"s_cmp_lt_u32 s34, 8\n"
"s_cselect_b32 s36, 2, 0\n"
"v_mfma_f32_16x16x32_bf16 v[24:27], v[212:215], v[80:83], v[24:27]\n"
"ds_read_b128 v[100:103], v243 offset:22528\n"
"v_mfma_f32_16x16x32_bf16 v[28:31], v[212:215], v[84:87], v[28:31]\n"
"ds_read_b128 v[104:107], v243 offset:24576\n"
"s_add_u32 s35, s35, s36\n"
"s_waitcnt lgkmcnt(6)\n"
"v_mfma_f32_16x16x32_bf16 v[32:35], v[72:75], v[216:219], v[32:35]\n"
"v_add_u32 v240, 0xffff3400, v240\n"
"ds_read_b64_tr_b16 v[208:209], v240 offset:0\n"
"s_add_u32 s0, s35, 0\n"
"v_mfma_f32_16x16x32_bf16 v[36:39], v[76:79], v[216:219], v[36:39]\n"
"ds_read_b64_tr_b16 v[210:211], v240 offset:4352\n"
"s_add_u32 m0, s48, 0xcc00\n"
"s_add_u32 s43, s46, 0x21c000\n"
"buffer_load_dwordx4 v246, s[8:11], s43 offen lds\n"
"v_mfma_f32_16x16x32_bf16 v[40:43], v[80:83], v[216:219], v[40:43]\n"
"ds_read_b128 v[108:111], v245 offset:18432\n"
"s_and_b32 s0, s0, 3\n"
"s_add_u32 s1, s35, 1\n"
"v_mfma_f32_16x16x32_bf16 v[44:47], v[84:87], v[216:219], v[44:47]\n"
"s_and_b32 s1, s1, 3\n"
"v_mfma_f32_16x16x32_bf16 v[64:67], v[88:91], v[216:219], v[64:67]\n"
"s_add_u32 s58, s35, 2\n"
"s_waitcnt lgkmcnt(5)\n"
"v_mfma_f32_16x16x32_bf16 v[48:51], v[220:223], v[72:75], v[48:51]\n"
"ds_read_b64_tr_b16 v[212:213], v240 offset:320\n"
"s_and_b32 s58, s58, 3\n"
"v_mfma_f32_16x16x32_bf16 v[52:55], v[220:223], v[76:79], v[52:55]\n"
"ds_read_b64_tr_b16 v[214:215], v240 offset:4672\n"
"s_add_u32 s44, s46, 0x21d9c0\n"
"buffer_load_dwordx4 v246, s[8:11], s44 offen offset:1088 lds\n"
"v_mfma_f32_16x16x32_bf16 v[56:59], v[220:223], v[80:83], v[56:59]\n"
"s_add_u32 s59, s35, 3\n"
"s_and_b32 s59, s59, 3\n"
"v_mfma_f32_16x16x32_bf16 v[60:63], v[220:223], v[84:87], v[60:63]\n"
"s_waitcnt vmcnt(6)\n"
"s_waitcnt lgkmcnt(2)\n"
"s_barrier\n"
"v_mfma_f32_16x16x32_bf16 v[0:3], v[208:211], v[92:95], v[0:3]\n"
"ds_read_b64_tr_b16 v[216:217], v240 offset:640\n"
"v_mfma_f32_16x16x32_bf16 v[4:7], v[208:211], v[96:99], v[4:7]\n"
"ds_read_b64_tr_b16 v[218:219], v240 offset:4992\n"
"v_mfma_f32_16x16x32_bf16 v[8:11], v[208:211], v[100:103], v[8:11]\n"
"ds_read_b128 v[72:75], v242 offset:36864\n"
"s_add_u32 m0, s48, 0x11000\n"
"s_add_u32 s43, s46, 0x23a000\n"
"buffer_load_dwordx4 v246, s[8:11], s43 offen lds\n"
"v_mfma_f32_16x16x32_bf16 v[12:15], v[208:211], v[104:107], v[12:15]\n"
"ds_read_b128 v[76:79], v242 offset:38912\n"
"s_waitcnt lgkmcnt(4)\n"
"v_mfma_f32_16x16x32_bf16 v[16:19], v[212:215], v[92:95], v[16:19]\n"
"v_add_u32 v241, s99, v241\n"
"ds_read_b64_tr_b16 v[220:221], v241 offset:0\n"
"v_mfma_f32_16x16x32_bf16 v[20:23], v[212:215], v[96:99], v[20:23]\n"
"ds_read_b64_tr_b16 v[222:223], v241 offset:4352\n"
"v_mfma_f32_16x16x32_bf16 v[24:27], v[212:215], v[100:103], v[24:27]\n"
"ds_read_b128 v[80:83], v242 offset:40960\n"
"v_mfma_f32_16x16x32_bf16 v[28:31], v[212:215], v[104:107], v[28:31]\n"
"ds_read_b128 v[84:87], v242 offset:43008\n"
"s_waitcnt lgkmcnt(6)\n"
"v_mfma_f32_16x16x32_bf16 v[32:35], v[92:95], v[216:219], v[32:35]\n"
"ds_read_b64_tr_b16 v[208:209], v240 offset:17408\n"
"v_mfma_f32_16x16x32_bf16 v[36:39], v[96:99], v[216:219], v[36:39]\n"
"ds_read_b64_tr_b16 v[210:211], v240 offset:21760\n"
"v_mfma_f32_16x16x32_bf16 v[40:43], v[100:103], v[216:219], v[40:43]\n"
"ds_read_b128 v[88:91], v244 offset:36864\n"
"v_mfma_f32_16x16x32_bf16 v[44:47], v[104:107], v[216:219], v[44:47]\n"
"s_add_u32 s44, s46, 0x23b9c0\n"
"buffer_load_dwordx4 v246, s[8:11], s44 offen offset:1088 lds\n"
"v_mfma_f32_16x16x32_bf16 v[64:67], v[108:111], v[216:219], v[64:67]\n"
"s_waitcnt lgkmcnt(5)\n"
"v_mfma_f32_16x16x32_bf16 v[48:51], v[220:223], v[92:95], v[48:51]\n"
"ds_read_b64_tr_b16 v[212:213], v240 offset:17728\n"
"v_mfma_f32_16x16x32_bf16 v[52:55], v[220:223], v[96:99], v[52:55]\n"
"ds_read_b64_tr_b16 v[214:215], v240 offset:22080\n"
"v_mfma_f32_16x16x32_bf16 v[56:59], v[220:223], v[100:103], v[56:59]\n"
"v_mfma_f32_16x16x32_bf16 v[60:63], v[220:223], v[104:107], v[60:63]\n"
"s_waitcnt vmcnt(6)\n"
"s_barrier\n"
"s_waitcnt lgkmcnt(3)\n"
"v_mfma_f32_16x16x32_bf16 v[0:3], v[208:211], v[72:75], v[0:3]\n"
"ds_read_b64_tr_b16 v[216:217], v240 offset:18048\n"
"v_mfma_f32_16x16x32_bf16 v[4:7], v[208:211], v[76:79], v[4:7]\n"
"ds_read_b64_tr_b16 v[218:219], v240 offset:22400\n"
"v_mfma_f32_16x16x32_bf16 v[8:11], v[208:211], v[80:83], v[8:11]\n"
"ds_read_b128 v[92:95], v243 offset:36864\n"
"s_add_u32 m0, s88, 0x0\n"
"s_mul_i32 s96, s0, 491520\n"
"s_add_u32 s96, s96, s84\n"
"buffer_load_dwordx4 v253, s[20:23], s96 offen lds\n"
"v_mfma_f32_16x16x32_bf16 v[12:15], v[208:211], v[84:87], v[12:15]\n"
"ds_read_b128 v[96:99], v243 offset:38912\n"
"s_waitcnt lgkmcnt(4)\n"
"v_mfma_f32_16x16x32_bf16 v[16:19], v[212:215], v[72:75], v[16:19]\n"
"v_add_u32 v241, s47, v241\n"
"ds_read_b64_tr_b16 v[220:221], v241 offset:17408\n"
"v_mfma_f32_16x16x32_bf16 v[20:23], v[212:215], v[76:79], v[20:23]\n"
"ds_read_b64_tr_b16 v[222:223], v241 offset:21760\n"
"v_mfma_f32_16x16x32_bf16 v[24:27], v[212:215], v[80:83], v[24:27]\n"
"ds_read_b128 v[100:103], v243 offset:40960\n"
"v_mfma_f32_16x16x32_bf16 v[28:31], v[212:215], v[84:87], v[28:31]\n"
"ds_read_b128 v[104:107], v243 offset:43008\n"
"s_waitcnt lgkmcnt(6)\n"
"v_mfma_f32_16x16x32_bf16 v[32:35], v[72:75], v[216:219], v[32:35]\n"
"ds_read_b64_tr_b16 v[208:209], v240 offset:34816\n"
"v_mfma_f32_16x16x32_bf16 v[36:39], v[76:79], v[216:219], v[36:39]\n"
"ds_read_b64_tr_b16 v[210:211], v240 offset:39168\n"
"v_mfma_f32_16x16x32_bf16 v[40:43], v[80:83], v[216:219], v[40:43]\n"
"ds_read_b128 v[108:111], v245 offset:36864\n"
"v_mfma_f32_16x16x32_bf16 v[44:47], v[84:87], v[216:219], v[44:47]\n"
"s_add_u32 s97, s96, 0x4c00\n"
"buffer_load_dwordx4 v253, s[20:23], s97 offen offset:1024 lds\n"
"v_mfma_f32_16x16x32_bf16 v[64:67], v[88:91], v[216:219], v[64:67]\n"
"s_waitcnt lgkmcnt(5)\n"
"v_mfma_f32_16x16x32_bf16 v[48:51], v[220:223], v[72:75], v[48:51]\n"
"ds_read_b64_tr_b16 v[212:213], v240 offset:35136\n"
"v_mfma_f32_16x16x32_bf16 v[52:55], v[220:223], v[76:79], v[52:55]\n"
"ds_read_b64_tr_b16 v[214:215], v240 offset:39488\n"
"v_mfma_f32_16x16x32_bf16 v[56:59], v[220:223], v[80:83], v[56:59]\n"
"v_mfma_f32_16x16x32_bf16 v[60:63], v[220:223], v[84:87], v[60:63]\n"
"s_waitcnt vmcnt(4)\n"
"s_waitcnt lgkmcnt(2)\n"
"s_barrier\n"
"v_mfma_f32_16x16x32_bf16 v[0:3], v[208:211], v[92:95], v[0:3]\n"
"ds_read_b64_tr_b16 v[216:217], v240 offset:35456\n"
"v_mfma_f32_16x16x32_bf16 v[4:7], v[208:211], v[96:99], v[4:7]\n"
"ds_read_b64_tr_b16 v[218:219], v240 offset:39808\n"
"v_mfma_f32_16x16x32_bf16 v[8:11], v[208:211], v[100:103], v[8:11]\n"
"ds_read_b128 v[72:75], v242 offset:0\n"
"s_add_u32 m0, s88, 0x4400\n"
"s_mul_i32 s96, s1, 491520\n"
"s_add_u32 s96, s96, s84\n"
"buffer_load_dwordx4 v253, s[20:23], s96 offen lds\n"
"v_mfma_f32_16x16x32_bf16 v[12:15], v[208:211], v[104:107], v[12:15]\n"
"ds_read_b128 v[76:79], v242 offset:2048\n"
"s_waitcnt lgkmcnt(4)\n"
"v_mfma_f32_16x16x32_bf16 v[16:19], v[212:215], v[92:95], v[16:19]\n"
"v_add_u32 v241, s47, v241\n"
"ds_read_b64_tr_b16 v[220:221], v241 offset:34816\n"
"v_mfma_f32_16x16x32_bf16 v[20:23], v[212:215], v[96:99], v[20:23]\n"
"ds_read_b64_tr_b16 v[222:223], v241 offset:39168\n"
"v_mfma_f32_16x16x32_bf16 v[24:27], v[212:215], v[100:103], v[24:27]\n"
"ds_read_b128 v[80:83], v242 offset:4096\n"
"v_mfma_f32_16x16x32_bf16 v[28:31], v[212:215], v[104:107], v[28:31]\n"
"ds_read_b128 v[84:87], v242 offset:6144\n"
"s_waitcnt lgkmcnt(6)\n"
"v_mfma_f32_16x16x32_bf16 v[32:35], v[92:95], v[216:219], v[32:35]\n"
"v_add_u32 v240, 0xcc00, v240\n"
"ds_read_b64_tr_b16 v[208:209], v240 offset:0\n"
"v_mfma_f32_16x16x32_bf16 v[36:39], v[96:99], v[216:219], v[36:39]\n"
"ds_read_b64_tr_b16 v[210:211], v240 offset:4352\n"
"v_mfma_f32_16x16x32_bf16 v[40:43], v[100:103], v[216:219], v[40:43]\n"
"ds_read_b128 v[88:91], v244 offset:0\n"
"v_mfma_f32_16x16x32_bf16 v[44:47], v[104:107], v[216:219], v[44:47]\n"
"s_add_u32 s97, s96, 0x4c00\n"
"buffer_load_dwordx4 v253, s[20:23], s97 offen offset:1024 lds\n"
"v_mfma_f32_16x16x32_bf16 v[64:67], v[108:111], v[216:219], v[64:67]\n"
"s_waitcnt lgkmcnt(5)\n"
"v_mfma_f32_16x16x32_bf16 v[48:51], v[220:223], v[92:95], v[48:51]\n"
"ds_read_b64_tr_b16 v[212:213], v240 offset:320\n"
"v_mfma_f32_16x16x32_bf16 v[52:55], v[220:223], v[96:99], v[52:55]\n"
"ds_read_b64_tr_b16 v[214:215], v240 offset:4672\n"
"v_mfma_f32_16x16x32_bf16 v[56:59], v[220:223], v[100:103], v[56:59]\n"
"v_mfma_f32_16x16x32_bf16 v[60:63], v[220:223], v[104:107], v[60:63]\n"
"s_waitcnt vmcnt(4)\n"
"s_barrier\n"
"s_waitcnt lgkmcnt(3)\n"
"v_mfma_f32_16x16x32_bf16 v[0:3], v[208:211], v[72:75], v[0:3]\n"
"ds_read_b64_tr_b16 v[216:217], v240 offset:640\n"
"v_mfma_f32_16x16x32_bf16 v[4:7], v[208:211], v[76:79], v[4:7]\n"
"ds_read_b64_tr_b16 v[218:219], v240 offset:4992\n"
"v_mfma_f32_16x16x32_bf16 v[8:11], v[208:211], v[80:83], v[8:11]\n"
"ds_read_b128 v[92:95], v243 offset:0\n"
"s_add_u32 m0, s88, 0x8800\n"
"s_mul_i32 s96, s58, 491520\n"
"s_add_u32 s96, s96, s84\n"
"buffer_load_dwordx4 v253, s[20:23], s96 offen lds\n"
"v_mfma_f32_16x16x32_bf16 v[12:15], v[208:211], v[84:87], v[12:15]\n"
"ds_read_b128 v[96:99], v243 offset:2048\n"
"s_waitcnt lgkmcnt(4)\n"
"v_mfma_f32_16x16x32_bf16 v[16:19], v[212:215], v[72:75], v[16:19]\n"
"v_add_u32 v241, s95, v241\n"
"ds_read_b64_tr_b16 v[220:221], v241 offset:0\n"
"v_mfma_f32_16x16x32_bf16 v[20:23], v[212:215], v[76:79], v[20:23]\n"
"ds_read_b64_tr_b16 v[222:223], v241 offset:4352\n"
"v_mfma_f32_16x16x32_bf16 v[24:27], v[212:215], v[80:83], v[24:27]\n"
"ds_read_b128 v[100:103], v243 offset:4096\n"
"v_mfma_f32_16x16x32_bf16 v[28:31], v[212:215], v[84:87], v[28:31]\n"
"ds_read_b128 v[104:107], v243 offset:6144\n"
"s_waitcnt lgkmcnt(6)\n"
"v_mfma_f32_16x16x32_bf16 v[32:35], v[72:75], v[216:219], v[32:35]\n"
"v_add_u32 v241, s47, v241\n"
"ds_read_b64_tr_b16 v[208:209], v241 offset:17408\n"
"v_mfma_f32_16x16x32_bf16 v[36:39], v[76:79], v[216:219], v[36:39]\n"
"ds_read_b64_tr_b16 v[210:211], v241 offset:21760\n"
"v_mfma_f32_16x16x32_bf16 v[40:43], v[80:83], v[216:219], v[40:43]\n"
"ds_read_b128 v[108:111], v245 offset:0\n"
"v_mfma_f32_16x16x32_bf16 v[44:47], v[84:87], v[216:219], v[44:47]\n"
"s_add_u32 s97, s96, 0x4c00\n"
"buffer_load_dwordx4 v253, s[20:23], s97 offen offset:1024 lds\n"
"v_mfma_f32_16x16x32_bf16 v[64:67], v[88:91], v[216:219], v[64:67]\n"
"s_waitcnt lgkmcnt(5)\n"
"v_mfma_f32_16x16x32_bf16 v[48:51], v[220:223], v[72:75], v[48:51]\n"
"ds_read_b64_tr_b16 v[212:213], v240 offset:17728\n"
"v_mfma_f32_16x16x32_bf16 v[52:55], v[220:223], v[76:79], v[52:55]\n"
"ds_read_b64_tr_b16 v[214:215], v240 offset:22080\n"
"v_mfma_f32_16x16x32_bf16 v[56:59], v[220:223], v[80:83], v[56:59]\n"
"v_mfma_f32_16x16x32_bf16 v[60:63], v[220:223], v[84:87], v[60:63]\n"
"s_waitcnt lgkmcnt(2)\n"
"s_barrier\n"
"v_mfma_f32_16x16x32_bf16 v[48:51], v[208:211], v[92:95], v[48:51]\n"
"ds_read_b64_tr_b16 v[216:217], v240 offset:18048\n"
"v_mfma_f32_16x16x32_bf16 v[52:55], v[208:211], v[96:99], v[52:55]\n"
"ds_read_b64_tr_b16 v[218:219], v240 offset:22400\n"
"s_add_u32 m0, s88, 0xcc00\n"
"s_mul_i32 s96, s59, 491520\n"
"s_add_u32 s96, s96, s84\n"
"buffer_load_dwordx4 v253, s[20:23], s96 offen lds\n"
"v_mfma_f32_16x16x32_bf16 v[56:59], v[208:211], v[100:103], v[56:59]\n"
"v_mfma_f32_16x16x32_bf16 v[60:63], v[208:211], v[104:107], v[60:63]\n"
"s_waitcnt lgkmcnt(2)\n"
"v_mfma_f32_16x16x32_bf16 v[16:19], v[212:215], v[92:95], v[16:19]\n"
"ds_read_b64_tr_b16 v[220:221], v240 offset:17408\n"
"s_add_u32 s97, s96, 0x4c00\n"
"buffer_load_dwordx4 v253, s[20:23], s97 offen offset:1024 lds\n"
"v_mfma_f32_16x16x32_bf16 v[20:23], v[212:215], v[96:99], v[20:23]\n"
"ds_read_b64_tr_b16 v[222:223], v240 offset:21760\n"
"v_mfma_f32_16x16x32_bf16 v[24:27], v[212:215], v[100:103], v[24:27]\n"
"v_mfma_f32_16x16x32_bf16 v[28:31], v[212:215], v[104:107], v[28:31]\n"
"v_cvt_pk_bf16_f32 v196, v48, v49\n"
"v_cvt_pk_bf16_f32 v197, v50, v51\n"
"s_waitcnt lgkmcnt(2)\n"
"v_mfma_f32_16x16x32_bf16 v[32:35], v[92:95], v[216:219], v[32:35]\n"
"ds_write_b64 v156, v[196:197] offset:0\n"
"v_cvt_pk_bf16_f32 v198, v52, v53\n"
"v_mfma_f32_16x16x32_bf16 v[36:39], v[96:99], v[216:219], v[36:39]\n"
"v_cvt_pk_bf16_f32 v199, v54, v55\n"
"ds_write_b64 v156, v[198:199] offset:512\n"
"v_mfma_f32_16x16x32_bf16 v[40:43], v[100:103], v[216:219], v[40:43]\n"
"v_cvt_pk_bf16_f32 v200, v56, v57\n"
"v_cvt_pk_bf16_f32 v201, v58, v59\n"
"v_mfma_f32_16x16x32_bf16 v[44:47], v[104:107], v[216:219], v[44:47]\n"
"ds_write_b64 v156, v[200:201] offset:1024\n"
"v_cvt_pk_bf16_f32 v202, v60, v61\n"
"v_mfma_f32_16x16x32_bf16 v[64:67], v[108:111], v[216:219], v[64:67]\n"
"v_cvt_pk_bf16_f32 v203, v62, v63\n"
"ds_write_b64 v156, v[202:203] offset:1536\n"
"s_waitcnt lgkmcnt(4)\n"
"v_mfma_f32_16x16x32_bf16 v[0:3], v[220:223], v[92:95], v[0:3]\n"
"v_mfma_f32_16x16x32_bf16 v[4:7], v[220:223], v[96:99], v[4:7]\n"
"v_mfma_f32_16x16x32_bf16 v[8:11], v[220:223], v[100:103], v[8:11]\n"
"v_mfma_f32_16x16x32_bf16 v[12:15], v[220:223], v[104:107], v[12:15]\n"
"v_cvt_pk_bf16_f32 v180, v20, v21\n"
"v_cvt_pk_bf16_f32 v181, v22, v23\n"
"ds_write_b64 v159, v[180:181] offset:0\n"
"v_cvt_pk_bf16_f32 v192, v28, v29\n"
"v_cvt_pk_bf16_f32 v193, v30, v31\n"
"ds_write_b64 v159, v[192:193] offset:1024\n"
"v_cvt_pk_bf16_f32 v176, v16, v17\n"
"v_cvt_pk_bf16_f32 v177, v18, v19\n"
"v_cvt_pk_bf16_f32 v188, v24, v25\n"
"v_cvt_pk_bf16_f32 v189, v26, v27\n"
"v_cvt_pk_bf16_f32 v206, v64, v65\n"
"v_cvt_pk_bf16_f32 v207, v66, v67\n"
"ds_write_b64 v158, v[206:207] offset:8\n"
"v_cvt_pk_bf16_f32 v132, v36, v37\n"
"v_cvt_pk_bf16_f32 v133, v38, v39\n"
"ds_write_b64 v159, v[132:133] offset:8\n"
"v_cvt_pk_bf16_f32 v138, v44, v45\n"
"v_cvt_pk_bf16_f32 v139, v46, v47\n"
"ds_write_b64 v159, v[138:139] offset:1032\n"
"s_branch .Lqtail\n"
".Lkend:\n"
    : "+{v0}"(tid)
    : "{s[0:1]}"(__builtin_amdgcn_kernarg_segment_ptr()), "{s2}"(blockIdx.x), "{s3}"((unsigned)(size_t)smem)
    : "v1", "v2", "v3", "v4", "v5", "v6", "v7", "v8", "v9", "v10", "v11", "v12", "v13", "v14", "v15", "v16", "v17", "v18", "v19", "v20", "v21", "v22", "v23", "v24", "v25", "v26", "v27", "v28", "v29", "v30", "v31", "v32", "v33", "v34", "v35", "v36", "v37", "v38", "v39", "v40", "v41", "v42", "v43", "v44", "v45", "v46", "v47", "v48", "v49", "v50", "v51", "v52", "v53", "v54", "v55", "v56", "v57", "v58", "v59", "v60", "v61", "v62", "v63", "v64", "v65", "v66", "v67", "v68", "v69", "v70", "v71", "v72", "v73", "v74", "v75", "v76", "v77", "v78", "v79", "v80", "v81", "v82", "v83", "v84", "v85", "v86", "v87", "v88", "v89", "v90", "v91", "v92", "v93", "v94", "v95", "v96", "v97", "v98", "v99", "v100", "v101", "v102", "v103", "v104", "v105", "v106", "v107", "v108", "v109", "v110", "v111", "v112", "v113", "v114", "v115", "v116", "v117", "v118", "v119", "v120", "v121", "v122", "v123", "v124", "v125", "v126", "v127", "v128", "v129", "v130", "v131", "v132", "v133", "v134", "v135", "v136", "v137", "v138", "v139", "v140", "v141", "v142", "v143", "v144", "v145", "v146", "v147", "v148", "v149", "v150", "v151", "v152", "v153", "v154", "v155", "v156", "v157", "v158", "v159", "v160", "v161", "v162", "v163", "v164", "v165", "v166", "v167", "v168", "v169", "v170", "v171", "v172", "v173", "v174", "v175", "v176", "v177", "v178", "v179", "v180", "v181", "v182", "v183", "v184", "v185", "v186", "v187", "v188", "v189", "v190", "v191", "v192", "v193", "v194", "v195", "v196", "v197", "v198", "v199", "v200", "v201", "v202", "v203", "v204", "v205", "v206", "v207", "v208", "v209", "v210", "v211", "v212", "v213", "v214", "v215", "v216", "v217", "v218", "v219", "v220", "v221", "v222", "v223", "v224", "v225", "v226", "v227", "v228", "v229", "v230", "v231", "v232", "v233", "v234", "v235", "v236", "v237", "v238", "v239", "v240", "v241", "v242", "v243", "v244", "v245", "v246", "v247", "v248", "v249", "v250", "v251", "v252", "v253", "v254", "v255", "s4", "s5", "s6", "s7", "s8", "s9", "s10", "s11", "s12", "s13", "s14", "s15", "s16", "s17", "s18", "s19", "s20", "s21", "s22", "s23", "s24", "s25", "s26", "s27", "s28", "s29", "s30", "s31", "s32", "s33", "s34", "s35", "s36", "s37", "s38", "s39", "s40", "s41", "s42", "s43", "s44", "s45", "s46", "s47", "s48", "s49", "s50", "s51", "s52", "s53", "s54", "s55", "s56", "s57", "s58", "s59", "s60", "s61", "s62", "s63", "s64", "s65", "s66", "s67", "s68", "s69", "s70", "s71", "s72", "s73", "s74", "s75", "s76", "s77", "s78", "s79", "s80", "s81", "s82", "s83", "s84", "s85", "s86", "s87", "s88", "s89", "s90", "s91", "s92", "s93", "s94", "s95", "s96", "s97", "s98", "s99", "vcc", "scc", "memory");
}
extern "C" void kernel_launch(void* const* d_in, const int* in_sizes, int n_in, void* d_out, int out_size, void* d_ws, size_t ws_size, hipStream_t stream) {
  KArgs a;
  a.x = d_in[0]; a.wqkv = d_in[1]; a.wo = d_in[2]; a.wg = d_in[4]; a.wu = d_in[5]; a.wd = d_in[6]; a.gw = d_in[7];
  a.out = d_out; a.ws = d_ws;
  fused_kernel<<<256, 512, 0, stream>>>(a);
}
```
